```python
import torch

def _softmax(x, axis=-1):
    return torch.softmax(x, dim=axis)
def _gelu(x):
    return torch.nn.functional.gelu(x, approximate="tanh")
def _silu(x):
    return torch.nn.functional.silu(x)
def _sigmoid(x):
    return torch.sigmoid(x)
def _layernorm(x, axis=-1, eps=1e-5):
    m = x.mean(dim=axis, keepdim=True)
    v = x.var(dim=axis, keepdim=True, unbiased=False)
    return (x - m) / torch.sqrt(v + eps)
def _scatter_add(tgt, idx, src, axis=0):
    return tgt.clone().index_add_(axis, idx, src)
def _segment_sum(src, seg, n):
    out = torch.zeros((n,) + src.shape[1:], dtype=src.dtype)
    return out.index_add_(0, seg, src)
def _pad(x, pw):
    flat = tuple(v for pair in reversed(pw) for v in pair)
    return torch.nn.functional.pad(x, flat)

kernel_name = 'standalone__gqa_block_cached__near_ridge'

def setup_inputs():

    _w = (lambda a: (lambda t: t.to(torch.bfloat16) if t.is_floating_point() else t)(torch.as_tensor(a)))
    B, Sq, D = 8, 224, 512
    Hq, Hkv, Sk, Dh = 32, 32, 1024, 128
    assert Hq % Hkv == 0, (Hq, Hkv)
    return {
        "x": _w((lambda *s: torch.randn(*s, dtype=torch.bfloat16) * 0.8)(B, Sq, D)),
        "k_cache": _w((lambda *s: torch.randn(*s, dtype=torch.bfloat16) * 0.8)(B, Hkv, Sk, Dh)),
        "v_cache": _w((lambda *s: torch.randn(*s, dtype=torch.bfloat16) * 0.05)(B, Hkv, Sk, Dh)),
        "wq": _w((lambda *s: torch.randn(*s, dtype=torch.bfloat16) * 0.8)(D, Hq * Dh) * (D ** -0.5)),
        "wo": _w((lambda *s: torch.randn(*s, dtype=torch.bfloat16) * 0.05)(Hq * Dh, D) * ((Hq * Dh) ** -0.5)),
    }

def reference(x, k_cache, v_cache, wq, wo):
    B, Sq, D = x.shape
    Hq, Dh = 32, 128
    Hkv = k_cache.shape[1]
    rep = Hq // Hkv
    q = (lambda x, *d: x.permute(*d))((x @ wq).reshape(B, Sq, Hq, Dh), 0, 2, 1, 3)
    k = (lambda x, n, axis: torch.repeat_interleave(x, n, dim=axis))(k_cache, rep, axis=1)
    v = (lambda x, n, axis: torch.repeat_interleave(x, n, dim=axis))(v_cache, rep, axis=1)
    scores = torch.einsum("bhqd,bhkd->bhqk", q, k) * (Dh ** -0.5)
    p = _softmax(scores, axis=-1)
    o = torch.einsum("bhqk,bhkd->bhqd", p, v)
    return (lambda x, *d: x.permute(*d))(o, 0, 2, 1, 3).reshape(B, Sq, Hq * Dh) @ wo
```

```cpp
#include <hip/hip_runtime.h>
#include <hip/hip_bf16.h>
typedef float f32x16 __attribute__((ext_vector_type(16)));
typedef float f32x2 __attribute__((ext_vector_type(2)));
typedef __bf16 bf16x8 __attribute__((ext_vector_type(8)));
typedef __bf16 bf16x2 __attribute__((ext_vector_type(2)));
typedef unsigned u32x4 __attribute__((ext_vector_type(4)));
typedef unsigned short u16;
#define DI __device__ __forceinline__
DI unsigned cvtpk(float lo, float hi) { f32x2 v = {lo, hi}; bf16x2 b = __builtin_convertvector(v, bf16x2); return __builtin_bit_cast(unsigned, b); }
DI bf16x8 pack8(const f32x16& a, int s, float sc) { u32x4 w; for (int p = 0; p < 4; ++p) w[p] = cvtpk(a[8*s+2*p]*sc, a[8*s+2*p+1]*sc); return __builtin_bit_cast(bf16x8, w); }
constexpr int B=8, SQ=224, D=512, H=32, SK=1024, DH=128;
__global__ __launch_bounds__(448) void attn_k(const u16* __restrict__ x, const u16* __restrict__ kc, const u16* __restrict__ vc, const u16* __restrict__ wq, u16* __restrict__ o){
  __shared__ __attribute__((aligned(16))) u16 Ks[64*136];
  __shared__ __attribute__((aligned(16))) u16 Vt[128*72];
  const int b = blockIdx.x & 7, h = blockIdx.x >> 3, tid = threadIdx.x, w = tid >> 6, lane = tid & 63, r = lane & 31, hi = lane >> 5;
  f32x16 qa[4] = {};
  const u16* xr = x + ((long)b*SQ + 32*w + r)*D;
  for (int c0 = 0; c0 < D; c0 += 64) {
    __syncthreads();
    for (int i = tid; i < 64*128; i += 448) { int c = i >> 7, d = i & 127; Vt[d*72 + c] = wq[(long)(c0+c)*(H*DH) + h*DH + d]; }
    __syncthreads();
    for (int s = 0; s < 4; ++s) {
      bf16x8 bx = *(const bf16x8*)(xr + c0 + 16*s + 8*hi);
      for (int db = 0; db < 4; ++db) {
        int d = 32*db + ((r & ~12) | ((r & 4) << 1) | ((r & 8) >> 1));
        bf16x8 a = *(const bf16x8*)(Vt + d*72 + 16*s + 8*hi);
        qa[db] = __builtin_amdgcn_mfma_f32_32x32x16_bf16(a, bx, qa[db], 0, 0, 0);
      }
    }
  }
  const float C = 0.12751743082f;
  bf16x8 qf[8];
  for (int db = 0; db < 4; ++db) for (int s = 0; s < 2; ++s) qf[2*db+s] = pack8(qa[db], s, C);
  f32x16 oa[4] = {}; float m = -1e30f, l = 0.f;
  const u16* kb = kc + ((long)b*H + h)*SK*DH; const u16* vb = vc + ((long)b*H + h)*SK*DH;
  for (int t = 0; t < SK; t += 64) {
    __syncthreads();
    for (int i = tid; i < 64*16; i += 448) { int k = i >> 4, c = (i & 15)*8; *(u32x4*)(Ks + k*136 + c) = *(const u32x4*)(kb + (long)(t+k)*DH + c);
      u32x4 vv = *(const u32x4*)(vb + (long)(t+k)*DH + c); const u16* pv = (const u16*)&vv; for (int e = 0; e < 8; ++e) Vt[(c+e)*72 + k] = pv[e]; }
    __syncthreads();
    f32x16 s0 = {}, s1 = {};
    for (int s = 0; s < 8; ++s) {
      int rr = ((r & ~12) | ((r & 4) << 1) | ((r & 8) >> 1));
      bf16x8 a0 = *(const bf16x8*)(Ks + rr*136 + 16*s + 8*hi), a1 = *(const bf16x8*)(Ks + (32+rr)*136 + 16*s + 8*hi);
      s0 = __builtin_amdgcn_mfma_f32_32x32x16_bf16(a0, qf[s], s0, 0, 0, 0);
      s1 = __builtin_amdgcn_mfma_f32_32x32x16_bf16(a1, qf[s], s1, 0, 0, 0);
    }
    float mx = m; for (int i = 0; i < 16; ++i) mx = fmaxf(mx, fmaxf(s0[i], s1[i]));
    mx = fmaxf(mx, __shfl_xor(mx, 32));
    float al = __builtin_amdgcn_exp2f(m - mx); m = mx; float ps = 0;
    for (int i = 0; i < 16; ++i) { s0[i] = __builtin_amdgcn_exp2f(s0[i]-mx); s1[i] = __builtin_amdgcn_exp2f(s1[i]-mx); ps += s0[i] + s1[i]; }
    l = l*al + ps;
    for (int db = 0; db < 4; ++db) for (int i = 0; i < 16; ++i) oa[db][i] *= al;
    bf16x8 p[4] = {pack8(s0,0,1.f), pack8(s0,1,1.f), pack8(s1,0,1.f), pack8(s1,1,1.f)};
    for (int ks = 0; ks < 4; ++ks) for (int db = 0; db < 4; ++db) {
      bf16x8 a = *(const bf16x8*)(Vt + (32*db + r)*72 + 16*ks + 8*hi);
      oa[db] = __builtin_amdgcn_mfma_f32_32x32x16_bf16(a, p[ks], oa[db], 0, 0, 0);
    }
  }
  l += __shfl_xor(l, 32); float il = 1.f / l;
  u16* orow = o + ((long)b*SQ + 32*w + r)*(H*DH) + h*DH;
  for (int db = 0; db < 4; ++db) for (int g = 0; g < 4; ++g) {
    uint2 v; v.x = cvtpk(oa[db][4*g]*il, oa[db][4*g+1]*il); v.y = cvtpk(oa[db][4*g+2]*il, oa[db][4*g+3]*il);
    *(uint2*)(orow + 32*db + 8*g + 4*hi) = v; }
}
__global__ __launch_bounds__(256) void oproj_k(const u16* __restrict__ o, const u16* __restrict__ wo, u16* __restrict__ out){
  __shared__ __attribute__((aligned(16))) u16 Wt[64*72];
  const int tid = threadIdx.x, w = tid >> 6, lane = tid & 63, r = lane & 31, hi = lane >> 5;
  const int m0 = (blockIdx.x >> 3)*64 + 32*(w & 1), n0 = (blockIdx.x & 7)*64, nb = w >> 1;
  f32x16 acc = {};
  const u16* orow = o + (long)(m0 + r)*4096;
  for (int k0 = 0; k0 < 4096; k0 += 64) {
    __syncthreads();
    for (int i = tid; i < 64*8; i += 256) { int k = i >> 3, c = (i & 7)*8; u32x4 vv = *(const u32x4*)(wo + (long)(k0+k)*512 + n0 + c); const u16* pv = (const u16*)&vv; for (int e = 0; e < 8; ++e) Wt[(c+e)*72 + k] = pv[e]; }
    __syncthreads();
    for (int s = 0; s < 4; ++s) {
      bf16x8 bx = *(const bf16x8*)(orow + k0 + 16*s + 8*hi);
      bf16x8 a = *(const bf16x8*)(Wt + (32*nb + r)*72 + 16*s + 8*hi);
      acc = __builtin_amdgcn_mfma_f32_32x32x16_bf16(a, bx, acc, 0, 0, 0);
    }
  }
  u16* op = out + (long)(m0 + r)*512 + n0 + 32*nb;
  for (int g = 0; g < 4; ++g) { uint2 v; v.x = cvtpk(acc[4*g], acc[4*g+1]); v.y = cvtpk(acc[4*g+2], acc[4*g+3]); *(uint2*)(op + 8*g + 4*hi) = v; }
}
extern "C" void kernel_launch(void* const* d_in, const int* in_sizes, int n_in, void* d_out, int out_size, void* d_ws, size_t ws_size, hipStream_t stream) {
  u16* o = (u16*)((char*)d_ws + 4096);
  attn_k<<<256, 448, 0, stream>>>((const u16*)d_in[0], (const u16*)d_in[1], (const u16*)d_in[2], (const u16*)d_in[3], o);
  oproj_k<<<224, 256, 0, stream>>>(o, (const u16*)d_in[4], (u16*)d_out);
}
```

```cpp
#include <hip/hip_runtime.h>
#include <hip/hip_bf16.h>
typedef float f32x16 __attribute__((ext_vector_type(16)));
typedef float f32x2 __attribute__((ext_vector_type(2)));
typedef __bf16 bf16x8 __attribute__((ext_vector_type(8)));
typedef __bf16 bf16x2 __attribute__((ext_vector_type(2)));
typedef unsigned u32x4 __attribute__((ext_vector_type(4)));
typedef unsigned short u16;
#define DI __device__ __forceinline__
DI unsigned cvtpk(float lo, float hi) { f32x2 v = {lo, hi}; bf16x2 b = __builtin_convertvector(v, bf16x2); return __builtin_bit_cast(unsigned, b); }
DI bf16x8 pack8(const f32x16& a, int s, float sc) { u32x4 w; for (int p = 0; p < 4; ++p) w[p] = cvtpk(a[8*s+2*p]*sc, a[8*s+2*p+1]*sc); return __builtin_bit_cast(bf16x8, w); }
constexpr int B=8, SQ=224, D=512, H=32, SK=1024, DH=128;
__global__ __launch_bounds__(448) void attn_k(const u16* __restrict__ x, const u16* __restrict__ kc, const u16* __restrict__ vc, const u16* __restrict__ wq, u16* __restrict__ o){
  __shared__ __attribute__((aligned(16))) u16 Ks[64*136];
  __shared__ __attribute__((aligned(16))) u16 Vt[128*72];
  const int b = blockIdx.x & 7, h = blockIdx.x >> 3, tid = threadIdx.x, w = tid >> 6, lane = tid & 63, r = lane & 31, hi = lane >> 5;
  f32x16 qa[4] = {};
  const u16* xr = x + ((long)b*SQ + 32*w + r)*D;
  for (int c0 = 0; c0 < D; c0 += 64) {
    __syncthreads();
    for (int i = tid; i < 64*128; i += 448) { int c = i >> 7, d = i & 127; Vt[d*72 + c] = wq[(long)(c0+c)*(H*DH) + h*DH + d]; }
    __syncthreads();
    for (int s = 0; s < 4; ++s) {
      bf16x8 bx = *(const bf16x8*)(xr + c0 + 16*s + 8*hi);
      for (int db = 0; db < 4; ++db) {
        int d = 32*db + ((r & ~12) | ((r & 4) << 1) | ((r & 8) >> 1));
        bf16x8 a = *(const bf16x8*)(Vt + d*72 + 16*s + 8*hi);
        qa[db] = __builtin_amdgcn_mfma_f32_32x32x16_bf16(a, bx, qa[db], 0, 0, 0);
      }
    }
  }
  const float C = 0.12751743082f;
  bf16x8 qf[8];
  for (int db = 0; db < 4; ++db) for (int s = 0; s < 2; ++s) qf[2*db+s] = pack8(qa[db], s, C);
  f32x16 oa[4] = {}; float m = -1e30f, l = 0.f;
  const u16* kb = kc + ((long)b*H + h)*SK*DH; const u16* vb = vc + ((long)b*H + h)*SK*DH;
  for (int t = 0; t < SK; t += 64) {
    __syncthreads();
    for (int i = tid; i < 64*16; i += 448) { int k = i >> 4, c = (i & 15)*8; *(u32x4*)(Ks + k*136 + c) = *(const u32x4*)(kb + (long)(t+k)*DH + c);
      u32x4 vv = *(const u32x4*)(vb + (long)(t+k)*DH + c); const u16* pv = (const u16*)&vv; for (int e = 0; e < 8; ++e) Vt[(c+e)*72 + k] = pv[e]; }
    __syncthreads();
    f32x16 s0 = {}, s1 = {};
    for (int s = 0; s < 8; ++s) {
      int rr = ((r & ~12) | ((r & 4) << 1) | ((r & 8) >> 1));
      bf16x8 a0 = *(const bf16x8*)(Ks + rr*136 + 16*s + 8*hi), a1 = *(const bf16x8*)(Ks + (32+rr)*136 + 16*s + 8*hi);
      s0 = __builtin_amdgcn_mfma_f32_32x32x16_bf16(a0, qf[s], s0, 0, 0, 0);
      s1 = __builtin_amdgcn_mfma_f32_32x32x16_bf16(a1, qf[s], s1, 0, 0, 0);
    }
    float mx = m; for (int i = 0; i < 16; ++i) mx = fmaxf(mx, fmaxf(s0[i], s1[i]));
    mx = fmaxf(mx, __shfl_xor(mx, 32));
    float al = __builtin_amdgcn_exp2f(m - mx); m = mx; float ps = 0;
    for (int i = 0; i < 16; ++i) { s0[i] = __builtin_amdgcn_exp2f(s0[i]-mx); s1[i] = __builtin_amdgcn_exp2f(s1[i]-mx); ps += s0[i] + s1[i]; }
    l = l*al + ps;
    for (int db = 0; db < 4; ++db) for (int i = 0; i < 16; ++i) oa[db][i] *= al;
    bf16x8 p[4] = {pack8(s0,0,1.f), pack8(s0,1,1.f), pack8(s1,0,1.f), pack8(s1,1,1.f)};
    for (int ks = 0; ks < 4; ++ks) for (int db = 0; db < 4; ++db) {
      bf16x8 a = *(const bf16x8*)(Vt + (32*db + r)*72 + 16*ks + 8*hi);
      oa[db] = __builtin_amdgcn_mfma_f32_32x32x16_bf16(a, p[ks], oa[db], 0, 0, 0);
    }
  }
  l += __shfl_xor(l, 32); float il = 1.f / l;
  u16* orow = o + ((long)b*SQ + 32*w + r)*(H*DH) + h*DH;
  for (int db = 0; db < 4; ++db) for (int g = 0; g < 4; ++g) {
    uint2 v; v.x = cvtpk(oa[db][4*g]*il, oa[db][4*g+1]*il); v.y = cvtpk(oa[db][4*g+2]*il, oa[db][4*g+3]*il);
    *(uint2*)(orow + 32*db + 8*g + 4*hi) = v; }
}
__global__ __launch_bounds__(256) void oproj_k(const u16* __restrict__ o, const u16* __restrict__ wo, u16* __restrict__ out){
  __shared__ __attribute__((aligned(16))) u16 Wt[64*72];
  const int tid = threadIdx.x, w = tid >> 6, lane = tid & 63, r = lane & 31, hi = lane >> 5;
  const int m0 = (blockIdx.x >> 3)*64 + 32*(w & 1), n0 = (blockIdx.x & 7)*64, nb = w >> 1;
  f32x16 acc = {};
  const u16* orow = o + (long)(m0 + r)*4096;
  for (int k0 = 0; k0 < 4096; k0 += 64) {
    __syncthreads();
    for (int i = tid; i < 64*8; i += 256) { int k = i >> 3, c = (i & 7)*8; u32x4 vv = *(const u32x4*)(wo + (long)(k0+k)*512 + n0 + c); const u16* pv = (const u16*)&vv; for (int e = 0; e < 8; ++e) Wt[(c+e)*72 + k] = pv[e]; }
    __syncthreads();
    for (int s = 0; s < 4; ++s) {
      bf16x8 bx = *(const bf16x8*)(orow + k0 + 16*s + 8*hi);
      bf16x8 a = *(const bf16x8*)(Wt + (32*nb + r)*72 + 16*s + 8*hi);
      acc = __builtin_amdgcn_mfma_f32_32x32x16_bf16(a, bx, acc, 0, 0, 0);
    }
  }
  u16* op = out + (long)(m0 + r)*512 + n0 + 32*nb;
  for (int g = 0; g < 4; ++g) { uint2 v; v.x = cvtpk(acc[4*g], acc[4*g+1]); v.y = cvtpk(acc[4*g+2], acc[4*g+3]); *(uint2*)(op + 8*g + 4*hi) = v; }
}
#define FACTOR 0
#define LD(p) (*(p))

__global__ __launch_bounds__(256) void probe(const u32x4* __restrict__ k, const u32x4* __restrict__ v, unsigned* out){
  const int wg = blockIdx.x, t = threadIdx.x;
  unsigned long long t0 = __builtin_amdgcn_s_memrealtime();
  u32x4 acc = {0,0,0,0};
  const u32x4* kb = k + (long)wg*16384; const u32x4* vb = v + (long)wg*16384;
  for (int i = 0; i < 16384; i += 256*8) {
    u32x4 a[8], b[8];
    #pragma unroll
    for (int j = 0; j < 8; ++j) { a[j] = LD(kb + i + j*256 + t); b[j] = LD(vb + i + j*256 + t); }
    #pragma unroll
    for (int j = 0; j < 8; ++j) acc ^= a[j] ^ b[j];
  }
  unsigned r = acc[0]^acc[1]^acc[2]^acc[3];
  if (r == 0x12345678) out[t] = 1;
  __syncthreads();
  unsigned long long t1 = __builtin_amdgcn_s_memrealtime();
  unsigned long long tend = t1 + (t1 - t0) * FACTOR;
  while (__builtin_amdgcn_s_memrealtime() < tend) __builtin_amdgcn_s_sleep(4);
}
extern "C" void kernel_launch(void* const* d_in, const int* in_sizes, int n_in, void* d_out, int out_size, void* d_ws, size_t ws_size, hipStream_t stream) {
  u16* o = (u16*)((char*)d_ws + 4096);
  probe<<<256,256,0,stream>>>((const u32x4*)d_in[1], (const u32x4*)d_in[2], (unsigned*)((char*)d_ws + 100000000));
  attn_k<<<256, 448, 0, stream>>>((const u16*)d_in[0], (const u16*)d_in[1], (const u16*)d_in[2], (const u16*)d_in[3], o);
  oproj_k<<<224, 256, 0, stream>>>(o, (const u16*)d_in[4], (u16*)d_out);
}
```

```cpp
#include <hip/hip_runtime.h>
#include <hip/hip_bf16.h>
typedef float f32x16 __attribute__((ext_vector_type(16)));
typedef float f32x2 __attribute__((ext_vector_type(2)));
typedef __bf16 bf16x8 __attribute__((ext_vector_type(8)));
typedef __bf16 bf16x2 __attribute__((ext_vector_type(2)));
typedef unsigned u32x4 __attribute__((ext_vector_type(4)));
typedef unsigned short u16;
#define DI __device__ __forceinline__
DI unsigned cvtpk(float lo, float hi) { f32x2 v = {lo, hi}; bf16x2 b = __builtin_convertvector(v, bf16x2); return __builtin_bit_cast(unsigned, b); }
DI bf16x8 pack8(const f32x16& a, int s, float sc) { u32x4 w; for (int p = 0; p < 4; ++p) w[p] = cvtpk(a[8*s+2*p]*sc, a[8*s+2*p+1]*sc); return __builtin_bit_cast(bf16x8, w); }
constexpr int B=8, SQ=224, D=512, H=32, SK=1024, DH=128;
__global__ __launch_bounds__(448) void attn_k(const u16* __restrict__ x, const u16* __restrict__ kc, const u16* __restrict__ vc, const u16* __restrict__ wq, u16* __restrict__ o){
  __shared__ __attribute__((aligned(16))) u16 Ks[64*136];
  __shared__ __attribute__((aligned(16))) u16 Vt[128*72];
  const int b = blockIdx.x & 7, h = blockIdx.x >> 3, tid = threadIdx.x, w = tid >> 6, lane = tid & 63, r = lane & 31, hi = lane >> 5;
  f32x16 qa[4] = {};
  const u16* xr = x + ((long)b*SQ + 32*w + r)*D;
  for (int c0 = 0; c0 < D; c0 += 64) {
    __syncthreads();
    for (int i = tid; i < 64*128; i += 448) { int c = i >> 7, d = i & 127; Vt[d*72 + c] = wq[(long)(c0+c)*(H*DH) + h*DH + d]; }
    __syncthreads();
    for (int s = 0; s < 4; ++s) {
      bf16x8 bx = *(const bf16x8*)(xr + c0 + 16*s + 8*hi);
      for (int db = 0; db < 4; ++db) {
        int d = 32*db + ((r & ~12) | ((r & 4) << 1) | ((r & 8) >> 1));
        bf16x8 a = *(const bf16x8*)(Vt + d*72 + 16*s + 8*hi);
        qa[db] = __builtin_amdgcn_mfma_f32_32x32x16_bf16(a, bx, qa[db], 0, 0, 0);
      }
    }
  }
  const float C = 0.12751743082f;
  bf16x8 qf[8];
  for (int db = 0; db < 4; ++db) for (int s = 0; s < 2; ++s) qf[2*db+s] = pack8(qa[db], s, C);
  f32x16 oa[4] = {}; float m = -1e30f, l = 0.f;
  const u16* kb = kc + ((long)b*H + h)*SK*DH; const u16* vb = vc + ((long)b*H + h)*SK*DH;
  for (int t = 0; t < SK; t += 64) {
    __syncthreads();
    for (int i = tid; i < 64*16; i += 448) { int k = i >> 4, c = (i & 15)*8; *(u32x4*)(Ks + k*136 + c) = *(const u32x4*)(kb + (long)(t+k)*DH + c);
      u32x4 vv = *(const u32x4*)(vb + (long)(t+k)*DH + c); const u16* pv = (const u16*)&vv; for (int e = 0; e < 8; ++e) Vt[(c+e)*72 + k] = pv[e]; }
    __syncthreads();
    f32x16 s0 = {}, s1 = {};
    for (int s = 0; s < 8; ++s) {
      int rr = ((r & ~12) | ((r & 4) << 1) | ((r & 8) >> 1));
      bf16x8 a0 = *(const bf16x8*)(Ks + rr*136 + 16*s + 8*hi), a1 = *(const bf16x8*)(Ks + (32+rr)*136 + 16*s + 8*hi);
      s0 = __builtin_amdgcn_mfma_f32_32x32x16_bf16(a0, qf[s], s0, 0, 0, 0);
      s1 = __builtin_amdgcn_mfma_f32_32x32x16_bf16(a1, qf[s], s1, 0, 0, 0);
    }
    float mx = m; for (int i = 0; i < 16; ++i) mx = fmaxf(mx, fmaxf(s0[i], s1[i]));
    mx = fmaxf(mx, __shfl_xor(mx, 32));
    float al = __builtin_amdgcn_exp2f(m - mx); m = mx; float ps = 0;
    for (int i = 0; i < 16; ++i) { s0[i] = __builtin_amdgcn_exp2f(s0[i]-mx); s1[i] = __builtin_amdgcn_exp2f(s1[i]-mx); ps += s0[i] + s1[i]; }
    l = l*al + ps;
    for (int db = 0; db < 4; ++db) for (int i = 0; i < 16; ++i) oa[db][i] *= al;
    bf16x8 p[4] = {pack8(s0,0,1.f), pack8(s0,1,1.f), pack8(s1,0,1.f), pack8(s1,1,1.f)};
    for (int ks = 0; ks < 4; ++ks) for (int db = 0; db < 4; ++db) {
      bf16x8 a = *(const bf16x8*)(Vt + (32*db + r)*72 + 16*ks + 8*hi);
      oa[db] = __builtin_amdgcn_mfma_f32_32x32x16_bf16(a, p[ks], oa[db], 0, 0, 0);
    }
  }
  l += __shfl_xor(l, 32); float il = 1.f / l;
  u16* orow = o + ((long)b*SQ + 32*w + r)*(H*DH) + h*DH;
  for (int db = 0; db < 4; ++db) for (int g = 0; g < 4; ++g) {
    uint2 v; v.x = cvtpk(oa[db][4*g]*il, oa[db][4*g+1]*il); v.y = cvtpk(oa[db][4*g+2]*il, oa[db][4*g+3]*il);
    *(uint2*)(orow + 32*db + 8*g + 4*hi) = v; }
}
__global__ __launch_bounds__(256) void oproj_k(const u16* __restrict__ o, const u16* __restrict__ wo, u16* __restrict__ out){
  __shared__ __attribute__((aligned(16))) u16 Wt[64*72];
  const int tid = threadIdx.x, w = tid >> 6, lane = tid & 63, r = lane & 31, hi = lane >> 5;
  const int m0 = (blockIdx.x >> 3)*64 + 32*(w & 1), n0 = (blockIdx.x & 7)*64, nb = w >> 1;
  f32x16 acc = {};
  const u16* orow = o + (long)(m0 + r)*4096;
  for (int k0 = 0; k0 < 4096; k0 += 64) {
    __syncthreads();
    for (int i = tid; i < 64*8; i += 256) { int k = i >> 3, c = (i & 7)*8; u32x4 vv = *(const u32x4*)(wo + (long)(k0+k)*512 + n0 + c); const u16* pv = (const u16*)&vv; for (int e = 0; e < 8; ++e) Wt[(c+e)*72 + k] = pv[e]; }
    __syncthreads();
    for (int s = 0; s < 4; ++s) {
      bf16x8 bx = *(const bf16x8*)(orow + k0 + 16*s + 8*hi);
      bf16x8 a = *(const bf16x8*)(Wt + (32*nb + r)*72 + 16*s + 8*hi);
      acc = __builtin_amdgcn_mfma_f32_32x32x16_bf16(a, bx, acc, 0, 0, 0);
    }
  }
  u16* op = out + (long)(m0 + r)*512 + n0 + 32*nb;
  for (int g = 0; g < 4; ++g) { uint2 v; v.x = cvtpk(acc[4*g], acc[4*g+1]); v.y = cvtpk(acc[4*g+2], acc[4*g+3]); *(uint2*)(op + 8*g + 4*hi) = v; }
}
#define FACTOR 0
#define MODE 0

__global__ __launch_bounds__(256) void probe(const u32x4* __restrict__ k, const u32x4* __restrict__ v, unsigned* out){
  const int wg = blockIdx.x, t = threadIdx.x;
  u32x4 ka = k[wg*256+t], kb = v[wg*256+t];
  bf16x8 a = __builtin_bit_cast(bf16x8, ka), b = __builtin_bit_cast(bf16x8, kb);
  f32x16 c0 = {}, c1 = {}, c2 = {}, c3 = {};
  asm volatile("s_waitcnt vmcnt(0)" ::: "memory");
  unsigned long long t0 = __builtin_amdgcn_s_memrealtime();
  unsigned long long c_0 = __builtin_amdgcn_s_memtime();
  while (__builtin_amdgcn_s_memrealtime() < t0 + 2500) {
    __builtin_amdgcn_s_sleep(4);
  }
  unsigned long long c_1 = __builtin_amdgcn_s_memtime();
  unsigned long long t1 = __builtin_amdgcn_s_memrealtime();
  float r = 0; for (int i = 0; i < 16; ++i) r += c0[i] + c1[i] + c2[i] + c3[i];
  if (r == 1.2345f) out[t] = 1;
  unsigned long long cyc = (c_1 - c_0) * 2500 / (t1 - t0);
  unsigned long long tend = t1 + cyc * FACTOR;
  while (__builtin_amdgcn_s_memrealtime() < tend) __builtin_amdgcn_s_sleep(4);
}
extern "C" void kernel_launch(void* const* d_in, const int* in_sizes, int n_in, void* d_out, int out_size, void* d_ws, size_t ws_size, hipStream_t stream) {
  u16* o = (u16*)((char*)d_ws + 4096);
  probe<<<256,256,0,stream>>>((const u32x4*)d_in[1], (const u32x4*)d_in[2], (unsigned*)((char*)d_ws + 100000000));
  attn_k<<<256, 448, 0, stream>>>((const u16*)d_in[0], (const u16*)d_in[1], (const u16*)d_in[2], (const u16*)d_in[3], o);
  oproj_k<<<224, 256, 0, stream>>>(o, (const u16*)d_in[4], (u16*)d_out);
}
```

```cpp
#include <hip/hip_runtime.h>
#include <hip/hip_bf16.h>
typedef float f32x16 __attribute__((ext_vector_type(16)));
typedef float f32x2 __attribute__((ext_vector_type(2)));
typedef __bf16 bf16x8 __attribute__((ext_vector_type(8)));
typedef __bf16 bf16x2 __attribute__((ext_vector_type(2)));
typedef unsigned u32x4 __attribute__((ext_vector_type(4)));
typedef unsigned short u16;
#define DI __device__ __forceinline__
DI unsigned cvtpk(float lo, float hi) { f32x2 v = {lo, hi}; bf16x2 b = __builtin_convertvector(v, bf16x2); return __builtin_bit_cast(unsigned, b); }
DI bf16x8 pack8(const f32x16& a, int s, float sc) { u32x4 w; for (int p = 0; p < 4; ++p) w[p] = cvtpk(a[8*s+2*p]*sc, a[8*s+2*p+1]*sc); return __builtin_bit_cast(bf16x8, w); }
constexpr int B=8, SQ=224, D=512, H=32, SK=1024, DH=128;
__global__ __launch_bounds__(448) void attn_k(const u16* __restrict__ x, const u16* __restrict__ kc, const u16* __restrict__ vc, const u16* __restrict__ wq, u16* __restrict__ o){
  __shared__ __attribute__((aligned(16))) u16 Ks[64*136];
  __shared__ __attribute__((aligned(16))) u16 Vt[128*72];
  const int b = blockIdx.x & 7, h = blockIdx.x >> 3, tid = threadIdx.x, w = tid >> 6, lane = tid & 63, r = lane & 31, hi = lane >> 5;
  f32x16 qa[4] = {};
  const u16* xr = x + ((long)b*SQ + 32*w + r)*D;
  for (int c0 = 0; c0 < D; c0 += 64) {
    __syncthreads();
    for (int i = tid; i < 64*128; i += 448) { int c = i >> 7, d = i & 127; Vt[d*72 + c] = wq[(long)(c0+c)*(H*DH) + h*DH + d]; }
    __syncthreads();
    for (int s = 0; s < 4; ++s) {
      bf16x8 bx = *(const bf16x8*)(xr + c0 + 16*s + 8*hi);
      for (int db = 0; db < 4; ++db) {
        int d = 32*db + ((r & ~12) | ((r & 4) << 1) | ((r & 8) >> 1));
        bf16x8 a = *(const bf16x8*)(Vt + d*72 + 16*s + 8*hi);
        qa[db] = __builtin_amdgcn_mfma_f32_32x32x16_bf16(a, bx, qa[db], 0, 0, 0);
      }
    }
  }
  const float C = 0.12751743082f;
  bf16x8 qf[8];
  for (int db = 0; db < 4; ++db) for (int s = 0; s < 2; ++s) qf[2*db+s] = pack8(qa[db], s, C);
  f32x16 oa[4] = {}; float m = -1e30f, l = 0.f;
  const u16* kb = kc + ((long)b*H + h)*SK*DH; const u16* vb = vc + ((long)b*H + h)*SK*DH;
  for (int t = 0; t < SK; t += 64) {
    __syncthreads();
    for (int i = tid; i < 64*16; i += 448) { int k = i >> 4, c = (i & 15)*8; *(u32x4*)(Ks + k*136 + c) = *(const u32x4*)(kb + (long)(t+k)*DH + c);
      u32x4 vv = *(const u32x4*)(vb + (long)(t+k)*DH + c); const u16* pv = (const u16*)&vv; for (int e = 0; e < 8; ++e) Vt[(c+e)*72 + k] = pv[e]; }
    __syncthreads();
    f32x16 s0 = {}, s1 = {};
    for (int s = 0; s < 8; ++s) {
      int rr = ((r & ~12) | ((r & 4) << 1) | ((r & 8) >> 1));
      bf16x8 a0 = *(const bf16x8*)(Ks + rr*136 + 16*s + 8*hi), a1 = *(const bf16x8*)(Ks + (32+rr)*136 + 16*s + 8*hi);
      s0 = __builtin_amdgcn_mfma_f32_32x32x16_bf16(a0, qf[s], s0, 0, 0, 0);
      s1 = __builtin_amdgcn_mfma_f32_32x32x16_bf16(a1, qf[s], s1, 0, 0, 0);
    }
    float mx = m; for (int i = 0; i < 16; ++i) mx = fmaxf(mx, fmaxf(s0[i], s1[i]));
    mx = fmaxf(mx, __shfl_xor(mx, 32));
    float al = __builtin_amdgcn_exp2f(m - mx); m = mx; float ps = 0;
    for (int i = 0; i < 16; ++i) { s0[i] = __builtin_amdgcn_exp2f(s0[i]-mx); s1[i] = __builtin_amdgcn_exp2f(s1[i]-mx); ps += s0[i] + s1[i]; }
    l = l*al + ps;
    for (int db = 0; db < 4; ++db) for (int i = 0; i < 16; ++i) oa[db][i] *= al;
    bf16x8 p[4] = {pack8(s0,0,1.f), pack8(s0,1,1.f), pack8(s1,0,1.f), pack8(s1,1,1.f)};
    for (int ks = 0; ks < 4; ++ks) for (int db = 0; db < 4; ++db) {
      bf16x8 a = *(const bf16x8*)(Vt + (32*db + r)*72 + 16*ks + 8*hi);
      oa[db] = __builtin_amdgcn_mfma_f32_32x32x16_bf16(a, p[ks], oa[db], 0, 0, 0);
    }
  }
  l += __shfl_xor(l, 32); float il = 1.f / l;
  u16* orow = o + ((long)b*SQ + 32*w + r)*(H*DH) + h*DH;
  for (int db = 0; db < 4; ++db) for (int g = 0; g < 4; ++g) {
    uint2 v; v.x = cvtpk(oa[db][4*g]*il, oa[db][4*g+1]*il); v.y = cvtpk(oa[db][4*g+2]*il, oa[db][4*g+3]*il);
    *(uint2*)(orow + 32*db + 8*g + 4*hi) = v; }
}
__global__ __launch_bounds__(256) void oproj_k(const u16* __restrict__ o, const u16* __restrict__ wo, u16* __restrict__ out){
  __shared__ __attribute__((aligned(16))) u16 Wt[64*72];
  const int tid = threadIdx.x, w = tid >> 6, lane = tid & 63, r = lane & 31, hi = lane >> 5;
  const int m0 = (blockIdx.x >> 3)*64 + 32*(w & 1), n0 = (blockIdx.x & 7)*64, nb = w >> 1;
  f32x16 acc = {};
  const u16* orow = o + (long)(m0 + r)*4096;
  for (int k0 = 0; k0 < 4096; k0 += 64) {
    __syncthreads();
    for (int i = tid; i < 64*8; i += 256) { int k = i >> 3, c = (i & 7)*8; u32x4 vv = *(const u32x4*)(wo + (long)(k0+k)*512 + n0 + c); const u16* pv = (const u16*)&vv; for (int e = 0; e < 8; ++e) Wt[(c+e)*72 + k] = pv[e]; }
    __syncthreads();
    for (int s = 0; s < 4; ++s) {
      bf16x8 bx = *(const bf16x8*)(orow + k0 + 16*s + 8*hi);
      bf16x8 a = *(const bf16x8*)(Wt + (32*nb + r)*72 + 16*s + 8*hi);
      acc = __builtin_amdgcn_mfma_f32_32x32x16_bf16(a, bx, acc, 0, 0, 0);
    }
  }
  u16* op = out + (long)(m0 + r)*512 + n0 + 32*nb;
  for (int g = 0; g < 4; ++g) { uint2 v; v.x = cvtpk(acc[4*g], acc[4*g+1]); v.y = cvtpk(acc[4*g+2], acc[4*g+3]); *(uint2*)(op + 8*g + 4*hi) = v; }
}
#define FACTOR 0
#define STAGE 0
#define MAP 0
#define POL ""
#define KVP 0

__global__ __launch_bounds__(256, 1) void probe(const char* __restrict__ x, const char* __restrict__ wq, const char* __restrict__ kc, const char* __restrict__ vc, unsigned* out){
  extern __shared__ __attribute__((aligned(16))) char lds[];
  const int wg = blockIdx.x, t = threadIdx.x, lane = t & 63, w = __builtin_amdgcn_readfirstlane(t >> 6);
  unsigned long long t0 = __builtin_amdgcn_s_memrealtime();
  const int b = wg & 7, h = wg >> 3;
  __amdgpu_buffer_rsrc_t rx = __builtin_amdgcn_make_buffer_rsrc((void*)(x + (size_t)b*224*1024), 0, 224*1024, 0x20000);
  __amdgpu_buffer_rsrc_t rw = __builtin_amdgcn_make_buffer_rsrc((void*)(wq + h*256), 0, 512*8192 - h*256, 0x20000);
  __amdgpu_buffer_rsrc_t rk = __builtin_amdgcn_make_buffer_rsrc((void*)(kc + (size_t)(b*32+h)*262144), 0, 262144, 0x20000);
  __amdgpu_buffer_rsrc_t rv = __builtin_amdgcn_make_buffer_rsrc((void*)(vc + (size_t)(b*32+h)*262144), 0, 262144, 0x20000);
  unsigned m0b = (unsigned)(size_t)lds + w*32768;
  const unsigned vx = (56*w + (lane >> 3))*1024 + (lane & 7)*16;
  const unsigned vw = (16*w + (lane >> 4))*8192 + (lane & 15)*16;
  const unsigned vk = w*4096 + lane*16;
  unsigned long long t1 = 0;
  #pragma unroll
  for (int c = 0; c < 8; ++c) {
    if (c >= 2) asm volatile("s_waitcnt vmcnt(%0)" :: "i"(11 + KVP) : "memory");
    if (c == 2) t1 = __builtin_amdgcn_s_memrealtime();
    #pragma unroll
    for (int p = 0; p < 4; ++p) asm volatile("s_mov_b32 m0, %0\n\ts_nop 0\n\tbuffer_load_dwordx4 %1, %2, %3 offen " POL " lds" :: "s"(m0b + ((c&1)*11 + p)*1024), "v"(vw), "s"(rw), "s"((c*64 + 4*p)*8192) : "memory");
    #pragma unroll
    for (int p = 0; p < 7; ++p) asm volatile("s_mov_b32 m0, %0\n\ts_nop 0\n\tbuffer_load_dwordx4 %1, %2, %3 offen " POL " lds" :: "s"(m0b + ((c&1)*11 + 4 + p)*1024), "v"(vx), "s"(rx), "s"(c*128 + p*8192) : "memory");
    #pragma unroll
    for (int p = 0; p < KVP/2; ++p) {
      asm volatile("s_mov_b32 m0, %0\n\ts_nop 0\n\tbuffer_load_dwordx4 %1, %2, %3 offen nt lds" :: "s"(m0b + 24*1024), "v"(vk), "s"(rk), "s"(((c*(KVP/2)+p) >> 2)*16384 + ((c*(KVP/2)+p) & 3)*1024) : "memory");
      asm volatile("s_mov_b32 m0, %0\n\ts_nop 0\n\tbuffer_load_dwordx4 %1, %2, %3 offen nt lds" :: "s"(m0b + 25*1024), "v"(vk), "s"(rv), "s"(((c*(KVP/2)+p) >> 2)*16384 + ((c*(KVP/2)+p) & 3)*1024) : "memory");
    }
  }
  asm volatile("s_waitcnt vmcnt(%0)" :: "i"(KVP) : "memory");
  unsigned long long t2 = __builtin_amdgcn_s_memrealtime();
  asm volatile("s_waitcnt vmcnt(0)" ::: "memory");
  unsigned long long d = (STAGE == 0) ? t1 - t0 : t2 - t0;
  unsigned long long tend = t0 + 1500 + d * FACTOR;
  while (__builtin_amdgcn_s_memrealtime() < tend) __builtin_amdgcn_s_sleep(4);
}
extern "C" void kernel_launch(void* const* d_in, const int* in_sizes, int n_in, void* d_out, int out_size, void* d_ws, size_t ws_size, hipStream_t stream) {
  u16* o = (u16*)((char*)d_ws + 4096);
  hipFuncSetAttribute((const void*)probe, hipFuncAttributeMaxDynamicSharedMemorySize, 163840);
  probe<<<256,256,163840,stream>>>((const char*)d_in[0], (const char*)d_in[3], (const char*)d_in[1], (const char*)d_in[2], (unsigned*)((char*)d_ws + 100000000));
  attn_k<<<256, 448, 0, stream>>>((const u16*)d_in[0], (const u16*)d_in[1], (const u16*)d_in[2], (const u16*)d_in[3], o);
  oproj_k<<<224, 256, 0, stream>>>(o, (const u16*)d_in[4], (u16*)d_out);
}
```

```cpp
#include <hip/hip_runtime.h>
#include <hip/hip_bf16.h>
typedef float f32x16 __attribute__((ext_vector_type(16)));
typedef float f32x2 __attribute__((ext_vector_type(2)));
typedef __bf16 bf16x8 __attribute__((ext_vector_type(8)));
typedef __bf16 bf16x2 __attribute__((ext_vector_type(2)));
typedef unsigned u32x4 __attribute__((ext_vector_type(4)));
typedef unsigned short u16;
#define DI __device__ __forceinline__
DI unsigned cvtpk(float lo, float hi) { f32x2 v = {lo, hi}; bf16x2 b = __builtin_convertvector(v, bf16x2); return __builtin_bit_cast(unsigned, b); }
DI bf16x8 pack8(const f32x16& a, int s, float sc) { u32x4 w; for (int p = 0; p < 4; ++p) w[p] = cvtpk(a[8*s+2*p]*sc, a[8*s+2*p+1]*sc); return __builtin_bit_cast(bf16x8, w); }
constexpr int B=8, SQ=224, D=512, H=32, SK=1024, DH=128;
__global__ __launch_bounds__(448) void attn_k(const u16* __restrict__ x, const u16* __restrict__ kc, const u16* __restrict__ vc, const u16* __restrict__ wq, u16* __restrict__ o){
  __shared__ __attribute__((aligned(16))) u16 Ks[64*136];
  __shared__ __attribute__((aligned(16))) u16 Vt[128*72];
  const int b = blockIdx.x & 7, h = blockIdx.x >> 3, tid = threadIdx.x, w = tid >> 6, lane = tid & 63, r = lane & 31, hi = lane >> 5;
  f32x16 qa[4] = {};
  const u16* xr = x + ((long)b*SQ + 32*w + r)*D;
  for (int c0 = 0; c0 < D; c0 += 64) {
    __syncthreads();
    for (int i = tid; i < 64*128; i += 448) { int c = i >> 7, d = i & 127; Vt[d*72 + c] = wq[(long)(c0+c)*(H*DH) + h*DH + d]; }
    __syncthreads();
    for (int s = 0; s < 4; ++s) {
      bf16x8 bx = *(const bf16x8*)(xr + c0 + 16*s + 8*hi);
      for (int db = 0; db < 4; ++db) {
        int d = 32*db + ((r & ~12) | ((r & 4) << 1) | ((r & 8) >> 1));
        bf16x8 a = *(const bf16x8*)(Vt + d*72 + 16*s + 8*hi);
        qa[db] = __builtin_amdgcn_mfma_f32_32x32x16_bf16(a, bx, qa[db], 0, 0, 0);
      }
    }
  }
  const float C = 0.12751743082f;
  bf16x8 qf[8];
  for (int db = 0; db < 4; ++db) for (int s = 0; s < 2; ++s) qf[2*db+s] = pack8(qa[db], s, C);
  f32x16 oa[4] = {}; float m = -1e30f, l = 0.f;
  const u16* kb = kc + ((long)b*H + h)*SK*DH; const u16* vb = vc + ((long)b*H + h)*SK*DH;
  for (int t = 0; t < SK; t += 64) {
    __syncthreads();
    for (int i = tid; i < 64*16; i += 448) { int k = i >> 4, c = (i & 15)*8; *(u32x4*)(Ks + k*136 + c) = *(const u32x4*)(kb + (long)(t+k)*DH + c);
      u32x4 vv = *(const u32x4*)(vb + (long)(t+k)*DH + c); const u16* pv = (const u16*)&vv; for (int e = 0; e < 8; ++e) Vt[(c+e)*72 + k] = pv[e]; }
    __syncthreads();
    f32x16 s0 = {}, s1 = {};
    for (int s = 0; s < 8; ++s) {
      int rr = ((r & ~12) | ((r & 4) << 1) | ((r & 8) >> 1));
      bf16x8 a0 = *(const bf16x8*)(Ks + rr*136 + 16*s + 8*hi), a1 = *(const bf16x8*)(Ks + (32+rr)*136 + 16*s + 8*hi);
      s0 = __builtin_amdgcn_mfma_f32_32x32x16_bf16(a0, qf[s], s0, 0, 0, 0);
      s1 = __builtin_amdgcn_mfma_f32_32x32x16_bf16(a1, qf[s], s1, 0, 0, 0);
    }
    float mx = m; for (int i = 0; i < 16; ++i) mx = fmaxf(mx, fmaxf(s0[i], s1[i]));
    mx = fmaxf(mx, __shfl_xor(mx, 32));
    float al = __builtin_amdgcn_exp2f(m - mx); m = mx; float ps = 0;
    for (int i = 0; i < 16; ++i) { s0[i] = __builtin_amdgcn_exp2f(s0[i]-mx); s1[i] = __builtin_amdgcn_exp2f(s1[i]-mx); ps += s0[i] + s1[i]; }
    l = l*al + ps;
    for (int db = 0; db < 4; ++db) for (int i = 0; i < 16; ++i) oa[db][i] *= al;
    bf16x8 p[4] = {pack8(s0,0,1.f), pack8(s0,1,1.f), pack8(s1,0,1.f), pack8(s1,1,1.f)};
    for (int ks = 0; ks < 4; ++ks) for (int db = 0; db < 4; ++db) {
      bf16x8 a = *(const bf16x8*)(Vt + (32*db + r)*72 + 16*ks + 8*hi);
      oa[db] = __builtin_amdgcn_mfma_f32_32x32x16_bf16(a, p[ks], oa[db], 0, 0, 0);
    }
  }
  l += __shfl_xor(l, 32); float il = 1.f / l;
  u16* orow = o + ((long)b*SQ + 32*w + r)*(H*DH) + h*DH;
  for (int db = 0; db < 4; ++db) for (int g = 0; g < 4; ++g) {
    uint2 v; v.x = cvtpk(oa[db][4*g]*il, oa[db][4*g+1]*il); v.y = cvtpk(oa[db][4*g+2]*il, oa[db][4*g+3]*il);
    *(uint2*)(orow + 32*db + 8*g + 4*hi) = v; }
}
__global__ __launch_bounds__(256) void oproj_k(const u16* __restrict__ o, const u16* __restrict__ wo, u16* __restrict__ out){
  __shared__ __attribute__((aligned(16))) u16 Wt[64*72];
  const int tid = threadIdx.x, w = tid >> 6, lane = tid & 63, r = lane & 31, hi = lane >> 5;
  const int m0 = (blockIdx.x >> 3)*64 + 32*(w & 1), n0 = (blockIdx.x & 7)*64, nb = w >> 1;
  f32x16 acc = {};
  const u16* orow = o + (long)(m0 + r)*4096;
  for (int k0 = 0; k0 < 4096; k0 += 64) {
    __syncthreads();
    for (int i = tid; i < 64*8; i += 256) { int k = i >> 3, c = (i & 7)*8; u32x4 vv = *(const u32x4*)(wo + (long)(k0+k)*512 + n0 + c); const u16* pv = (const u16*)&vv; for (int e = 0; e < 8; ++e) Wt[(c+e)*72 + k] = pv[e]; }
    __syncthreads();
    for (int s = 0; s < 4; ++s) {
      bf16x8 bx = *(const bf16x8*)(orow + k0 + 16*s + 8*hi);
      bf16x8 a = *(const bf16x8*)(Wt + (32*nb + r)*72 + 16*s + 8*hi);
      acc = __builtin_amdgcn_mfma_f32_32x32x16_bf16(a, bx, acc, 0, 0, 0);
    }
  }
  u16* op = out + (long)(m0 + r)*512 + n0 + 32*nb;
  for (int g = 0; g < 4; ++g) { uint2 v; v.x = cvtpk(acc[4*g], acc[4*g+1]); v.y = cvtpk(acc[4*g+2], acc[4*g+3]); *(uint2*)(op + 8*g + 4*hi) = v; }
}
#define FACTOR 0

__global__ __launch_bounds__(256, 1) void probe(unsigned* out){
  unsigned a = threadIdx.x, b = blockIdx.x;
  unsigned long long t0 = __builtin_amdgcn_s_memrealtime();
  asm volatile("s_waitcnt lgkmcnt(0)" ::: "memory");
  #pragma nounroll
  for (int i = 0; i < 32; ++i) { asm volatile(".rept 16\n\tv_add3_u32 %0, %0, %1, %1\n\tv_add3_u32 %0, %0, %1, %1\n\tv_add3_u32 %0, %0, %1, %1\n\tv_add3_u32 %0, %0, %1, %1\n\tv_add3_u32 %0, %0, %1, %1\n\tv_add3_u32 %0, %0, %1, %1\n\tv_add3_u32 %0, %0, %1, %1\n\tv_add3_u32 %0, %0, %1, %1\n\t.endr" : "+v"(a) : "v"(b)); }
  unsigned long long t1 = __builtin_amdgcn_s_memrealtime();
  if (a == 0x12345) out[0] = a;
  unsigned long long tend = t0 + 4000 + (t1 - t0) * FACTOR;
  while (__builtin_amdgcn_s_memrealtime() < tend) __builtin_amdgcn_s_sleep(4);
}
extern "C" void kernel_launch(void* const* d_in, const int* in_sizes, int n_in, void* d_out, int out_size, void* d_ws, size_t ws_size, hipStream_t stream) {
  u16* o = (u16*)((char*)d_ws + 4096);
  probe<<<256,256,0,stream>>>((unsigned*)((char*)d_ws + 100000000));
  attn_k<<<256, 448, 0, stream>>>((const u16*)d_in[0], (const u16*)d_in[1], (const u16*)d_in[2], (const u16*)d_in[3], o);
  oproj_k<<<224, 256, 0, stream>>>(o, (const u16*)d_in[4], (u16*)d_out);
}
```

```cpp
#include <hip/hip_runtime.h>

__global__ __launch_bounds__(256, 1)
void gqa_block_fused(const void* x, const void* k, const void* v, const void* wq, const void* wo, void* out, void* ws) {
  asm volatile(
    "L_start%=:\n"
    "s_load_dwordx8 s[8:15], %0, 0\n"
    "s_load_dwordx4 s[16:19], %0, 32\n"
    "s_load_dwordx2 s[20:21], %0, 48\n"
    "s_mov_b32 s46, %1\n"
    "v_mov_b32 v0, %2\n"
    "v_lshrrev_b32_e32 v1, 6, v0\n"
    "v_and_b32_e32 v0, 63, v0\n"
    "v_readfirstlane_b32 s44, v1\n"
    "s_and_b32 s45, s46, 1\n"
    "s_lshl_b32 s45, s45, 2\n"
    "s_bfe_u32 s47, s46, 0x20003\n"
    "s_add_u32 s47, s47, s45\n"
    "s_bfe_u32 s45, s46, 0x20001\n"
    "s_lshl_b32 s45, s45, 3\n"
    "s_lshr_b32 s48, s46, 5\n"
    "s_add_u32 s48, s48, s45\n"
    "s_and_b32 s49, s44, 1\n"
    "s_lshr_b32 s50, s44, 1\n"
    "s_lshl_b32 s55, s44, 12\n"
    "s_getpc_b64 s[60:61]\n"
    "L_pc%=:\n"
    "s_and_b32 s61, s61, 0xffff\n"
    "s_mov_b32 s62, L_end%=-L_pc%=\n"
    "s_mov_b32 s63, 0x20000\n"
    "s_lshr_b32 s45, s46, 3\n"
    "s_lshl_b32 s45, s45, 2\n"
    "s_add_u32 s45, s45, s44\n"
    "s_lshl_b32 s45, s45, 8\n"
    "v_lshl_add_u32 v2, v0, 2, s45\n"
    "buffer_load_dword v36, v2, s[60:63], 0 offen\n"
    "v_add_u32_e32 v2, 0x8000, v2\n"
    "buffer_load_dword v37, v2, s[60:63], 0 offen\n"
    "s_and_b32 s45, s47, 1\n"
    "s_and_b32 s51, s48, 1\n"
    "s_xor_b32 s45, s45, s51\n"
    "s_lshl_b32 s53, s45, 2\n"
    "v_bfe_u32 v1, v0, 2, 2\n"
    "v_lshrrev_b32_e32 v2, 4, v0\n"
    "v_lshlrev_b32_e32 v2, 6, v2\n"
    "v_lshl_add_u32 v1, v1, 13, v2\n"
    "v_and_b32_e32 v2, 3, v0\n"
    "v_lshl_add_u32 v4, v2, 4, v1\n"
    "s_lshl_b32 s45, s50, 5\n"
    "s_lshl_b32 s51, s49, 2\n"
    "s_add_u32 s45, s45, s51\n"
    "s_lshl_b32 s45, s45, 13\n"
    "v_add_u32_e32 v22, s45, v4\n"
    "s_mov_b32 s72, 0x1000\n"
    "s_mov_b32 s73, 0x10c00\n"
    "s_mov_b32 s74, 0x20800\n"
    "s_mov_b32 s75, 0x30400\n"
    "v_lshrrev_b32_e32 v1, 3, v0\n"
    "v_lshrrev_b32_e32 v2, 4, v0\n"
    "v_and_b32_e32 v3, 7, v0\n"
    "v_xor_b32_e32 v2, v2, v3\n"
    "v_lshlrev_b32_e32 v2, 4, v2\n"
    "v_lshl_add_u32 v18, v1, 10, v2\n"
    "v_xor_b32_e32 v2, 64, v18\n"
    "v_add_u32_e32 v19, 0x2000, v2\n"
    "s_mul_i32 s45, s50, 7\n"
    "s_lshl_b32 s51, s49, 2\n"
    "s_add_u32 s51, s51, s45\n"
    "s_lshl_b32 s51, s51, 14\n"
    "s_lshl_b32 s45, s45, 14\n"
    "s_add_u32 s64, s51, 0x1000\n"
    "s_add_u32 s65, s51, 0xc00\n"
    "s_add_u32 s66, s51, 0x4800\n"
    "s_add_u32 s67, s51, 0x4400\n"
    "s_add_u32 s68, s51, 0x9000\n"
    "s_add_u32 s69, s51, 0x8c00\n"
    "s_add_u32 s70, s45, 0xc800\n"
    "s_add_u32 s71, s45, 0xc400\n"
    "s_lshl_b32 s52, s48, 8\n"
    "s_mul_i32 s51, s47, 0x38000\n"
    "s_waitcnt lgkmcnt(0)\n"
    "s_add_u32 s36, s14, s52\n"
    "s_addc_u32 s37, s15, 0\n"
    "s_sub_u32 s36, s36, 0x1000\n"
    "s_subb_u32 s37, s37, 0\n"
    "s_and_b32 s37, s37, 0xffff\n"
    "s_mov_b32 s38, 0\n"
    "s_mov_b32 s39, 0x20000\n"
    "s_sub_u32 s38, 0x401000, s52\n"
    "s_add_u32 s32, s8, s51\n"
    "s_addc_u32 s33, s9, 0\n"
    "s_sub_u32 s32, s32, 0x1000\n"
    "s_subb_u32 s33, s33, 0\n"
    "s_and_b32 s33, s33, 0xffff\n"
    "s_mov_b32 s34, 0x39000\n"
    "s_mov_b32 s35, 0x20000\n"
    "s_add_u32 s45, s53, 0\n"
    "s_and_b32 s45, s45, 7\n"
    "s_lshl_b32 s51, s45, 7\n"
    "s_lshl_b32 s52, s45, 19\n"
    "v_add_u32_e32 v23, s52, v22\n"
    "v_xor_b32_e32 v56, 32, v23\n"
    "s_add_u32 m0, s55, 0x20000\n"
    "v_add_u32_e32 v20, s51, v18\n"
    "v_add_u32_e32 v21, s51, v19\n"
    "buffer_load_dwordx4 v23, s[36:39], s72 offen lds\n"
    "buffer_load_dwordx4 v56, s[36:39], s73 offen offset:1024 lds\n"
    "buffer_load_dwordx4 v23, s[36:39], s74 offen offset:2048 lds\n"
    "buffer_load_dwordx4 v56, s[36:39], s75 offen offset:3072 lds\n"
    "s_add_u32 m0, s55, 0x18000\n"
    "s_nop 0\n"
    "buffer_load_dwordx4 v20, s[32:35], s64 offen lds\n"
    "buffer_load_dwordx4 v21, s[32:35], s65 offen offset:1024 lds\n"
    "buffer_load_dwordx4 v20, s[32:35], s66 offen offset:2048 lds\n"
    "buffer_load_dwordx4 v21, s[32:35], s67 offen offset:3072 lds\n"
    "s_add_u32 m0, s55, 0x1c000\n"
    "s_nop 0\n"
    "buffer_load_dwordx4 v20, s[32:35], s68 offen lds\n"
    "buffer_load_dwordx4 v21, s[32:35], s69 offen offset:1024 lds\n"
    "buffer_load_dwordx4 v20, s[32:35], s70 offen offset:2048 lds\n"
    "buffer_load_dwordx4 v21, s[32:35], s71 offen offset:3072 lds\n"
    "s_mov_b32 s56, s20\n"
    "s_mov_b32 s57, s21\n"
    "s_and_b32 s57, s57, 0xffff\n"
    "s_mov_b32 s58, 0xe10000\n"
    "s_mov_b32 s59, 0x20000\n"
    "s_lshl_b32 s45, s47, 5\n"
    "s_add_u32 s45, s45, s48\n"
    "s_lshl_b32 s22, s45, 7\n"
    "s_add_u32 s22, s22, 0x1000\n"
    "s_lshl_b32 s23, s45, 2\n"
    "v_and_b32_e32 v32, 31, v0\n"
    "v_lshlrev_b32_e32 v32, 2, v32\n"
    "v_mov_b32_e32 v33, 0\n"
    "s_lshl_b32 s45, s45, 18\n"
    "s_add_u32 s24, s10, s45\n"
    "s_addc_u32 s25, s11, 0\n"
    "s_and_b32 s25, s25, 0xffff\n"
    "s_mov_b32 s26, 0x40000\n"
    "s_mov_b32 s27, 0x20000\n"
    "s_add_u32 s28, s12, s45\n"
    "s_addc_u32 s29, s13, 0\n"
    "s_and_b32 s29, s29, 0xffff\n"
    "s_mov_b32 s30, 0x40000\n"
    "s_mov_b32 s31, 0x20000\n"
    "s_mov_b32 s78, 0\n"
    "v_bfe_u32 v1, v0, 2, 2\n"
    "v_lshrrev_b32_e32 v2, 4, v0\n"
    "v_lshlrev_b32_e32 v2, 6, v2\n"
    "v_lshl_add_u32 v1, v1, 8, v2\n"
    "v_and_b32_e32 v2, 3, v0\n"
    "v_lshl_add_u32 v4, v2, 4, v1\n"
    "v_add_u32_e32 v116, s55, v4\n"
    "v_xor_b32_e32 v117, 48, v116\n"
    "v_xor_b32_e32 v118, 32, v116\n"
    "v_xor_b32_e32 v119, 16, v116\n"
    "v_bfe_u32 v1, v0, 2, 2\n"
    "v_sub_u32_e32 v2, 0, v1\n"
    "v_lshrrev_b32_e32 v3, 4, v0\n"
    "v_xor_b32_e32 v2, v2, v3\n"
    "v_and_b32_e32 v2, 3, v2\n"
    "v_lshlrev_b32_e32 v2, 4, v2\n"
    "v_lshl_add_u32 v2, v1, 10, v2\n"
    "v_and_b32_e32 v3, 3, v0\n"
    "v_lshl_add_u32 v2, v3, 6, v2\n"
    "s_lshl_b32 s45, s49, 13\n"
    "v_add_u32_e32 v108, s45, v2\n"
    "v_xor_b32_e32 v109, 0x2000, v108\n"
    "v_and_b32_e32 v1, 15, v0\n"
    "v_lshrrev_b32_e32 v2, 4, v0\n"
    "v_lshrrev_b32_e32 v3, 1, v1\n"
    "v_xor_b32_e32 v3, v3, v2\n"
    "v_lshlrev_b32_e32 v3, 4, v3\n"
    "v_and_b32_e32 v4, 7, v1\n"
    "v_lshl_add_u32 v3, v4, 7, v3\n"
    "v_lshrrev_b32_e32 v4, 3, v1\n"
    "s_add_u32 s51, s55, 0x10000\n"
    "v_lshl_add_u32 v5, v4, 10, v3\n"
    "v_add_u32_e32 v8, s51, v5\n"
    "v_xor_b32_e32 v9, 64, v8\n"
    "v_lshrrev_b32_e32 v1, 4, v0\n"
    "v_bfe_u32 v2, v0, 2, 2\n"
    "v_lshlrev_b32_e32 v2, 6, v2\n"
    "v_lshl_add_u32 v2, v1, 10, v2\n"
    "v_and_b32_e32 v1, 1, v1\n"
    "v_lshl_add_u32 v2, v1, 5, v2\n"
    "v_and_b32_e32 v1, 3, v0\n"
    "v_lshl_add_u32 v5, v1, 3, v2\n"
    "s_add_u32 s45, s45, 0x4000\n"
    "v_add_u32_e32 v112, s45, v5\n"
    "v_xor_b32_e32 v113, 32, v112\n"
    "v_xor_b32_e32 v114, 0x2000, v112\n"
    "v_xor_b32_e32 v115, 0x2000, v113\n"
    "s_lshl_b32 s45, s49, 9\n"
    "s_add_u32 s51, s45, 0x20000\n"
    "v_add_u32_e32 v10, s51, v5\n"
    "v_xor_b32_e32 v11, 32, v10\n"
    "v_xor_b32_e32 v12, 0x200, v10\n"
    "v_xor_b32_e32 v13, 0x200, v11\n"
    "s_add_u32 s51, s45, 0xc000\n"
    "v_add_u32_e32 v14, s51, v5\n"
    "v_xor_b32_e32 v15, 32, v14\n"
    "v_xor_b32_e32 v16, 0x200, v14\n"
    "v_xor_b32_e32 v17, 0x200, v15\n"
    "v_bfe_u32 v1, v0, 2, 2\n"
    "v_sub_u32_e32 v1, 0, v1\n"
    "v_and_b32_e32 v1, 3, v1\n"
    "v_and_b32_e32 v2, 15, v0\n"
    "v_lshlrev_b32_e32 v2, 6, v2\n"
    "v_lshrrev_b32_e32 v3, 4, v0\n"
    "v_xor_b32_e32 v4, v3, v1\n"
    "v_lshl_add_u32 v4, v4, 4, v2\n"
    "s_add_u32 s51, s55, 0x10000\n"
    "v_add_u32_e32 v30, s51, v4\n"
    "s_lshl_b32 s52, s50, 12\n"
    "s_add_u32 s52, s52, 0x20000\n"
    "v_add_u32_e32 v31, s52, v4\n"
    "v_lshrrev_b32_e32 v4, 5, v0\n"
    "v_xor_b32_e32 v4, v4, v1\n"
    "v_lshl_add_u32 v4, v4, 4, v2\n"
    "v_and_b32_e32 v3, 1, v3\n"
    "v_lshl_add_u32 v4, v3, 3, v4\n"
    "s_lshl_b32 s45, s49, 11\n"
    "s_add_u32 s51, s51, s45\n"
    "s_add_u32 s52, s52, s45\n"
    "v_add_u32_e32 v24, s51, v4\n"
    "v_xor_b32_e32 v25, 32, v24\n"
    "v_xor_b32_e32 v26, 0x800, v24\n"
    "v_xor_b32_e32 v27, 0x800, v25\n"
    "v_add_u32_e32 v28, s52, v4\n"
    "v_xor_b32_e32 v29, 32, v28\n"
    "v_bfe_u32 v1, v0, 2, 3\n"
    "v_bfe_u32 v2, v0, 4, 1\n"
    "v_lshlrev_b32_e32 v2, 1, v2\n"
    "v_and_b32_e32 v3, 3, v0\n"
    "v_xor_b32_e32 v2, v2, v3\n"
    "v_lshlrev_b32_e32 v2, 4, v2\n"
    "v_lshrrev_b32_e32 v3, 5, v0\n"
    "v_lshl_add_u32 v4, v3, 6, v2\n"
    "v_lshl_add_u32 v1, v1, 10, v4\n"
    "s_lshl_b32 s45, s44, 20\n"
    "s_and_b32 s51, s48, 7\n"
    "s_lshl_b32 s51, s51, 7\n"
    "s_add_u32 s45, s45, s51\n"
    "v_add_u32_e32 v122, s45, v1\n"
    "v_add_u32_e32 v123, 0x1c00, v122\n"
    "v_add_u32_e32 v124, 0x3800, v122\n"
    "v_add_u32_e32 v125, 0x5400, v122\n"
    "v_mov_b32_e32 v1, 0x3f803f80\n"
    "v_accvgpr_write_b32 a144, v1\n"
    "v_accvgpr_write_b32 a145, v1\n"
    "v_accvgpr_write_b32 a146, v1\n"
    "v_accvgpr_write_b32 a147, v1\n"
    "v_mov_b32_e32 v92, 0x7f800000\n"
    "v_mov_b32_e32 v96, 0x7f800000\n"
    "v_mov_b32_e32 v100, 0x7f800000\n"
    "v_mov_b32_e32 v104, 0x7f800000\n"
    "s_mov_b32 s54, 0x3e0293ee\n"
    "s_mov_b32 s77, 0\n"
    "s_lshl_b32 s79, s48, 12\n"
    "v_mov_b32_e32 v34, s54\n"
    "v_mov_b32_e32 v35, s54\n"
    "s_add_u32 s45, s53, 1\n"
    "s_and_b32 s45, s45, 7\n"
    "s_lshl_b32 s51, s45, 7\n"
    "s_lshl_b32 s52, s45, 19\n"
    "v_add_u32_e32 v23, s52, v22\n"
    "v_xor_b32_e32 v56, 32, v23\n"
    "s_add_u32 m0, s55, 0x24000\n"
    "v_add_u32_e32 v20, s51, v18\n"
    "v_add_u32_e32 v21, s51, v19\n"
    "buffer_load_dwordx4 v23, s[36:39], s72 offen lds\n"
    "buffer_load_dwordx4 v56, s[36:39], s73 offen offset:1024 lds\n"
    "buffer_load_dwordx4 v23, s[36:39], s74 offen offset:2048 lds\n"
    "buffer_load_dwordx4 v56, s[36:39], s75 offen offset:3072 lds\n"
    "s_add_u32 m0, s55, 0x10000\n"
    "s_nop 0\n"
    "buffer_load_dwordx4 v20, s[32:35], s64 offen lds\n"
    "buffer_load_dwordx4 v21, s[32:35], s65 offen offset:1024 lds\n"
    "buffer_load_dwordx4 v20, s[32:35], s66 offen offset:2048 lds\n"
    "buffer_load_dwordx4 v21, s[32:35], s67 offen offset:3072 lds\n"
    "s_add_u32 m0, s55, 0x14000\n"
    "s_nop 0\n"
    "buffer_load_dwordx4 v20, s[32:35], s68 offen lds\n"
    "buffer_load_dwordx4 v21, s[32:35], s69 offen offset:1024 lds\n"
    "buffer_load_dwordx4 v20, s[32:35], s70 offen offset:2048 lds\n"
    "buffer_load_dwordx4 v21, s[32:35], s71 offen offset:3072 lds\n"
    "s_waitcnt vmcnt(12)\n"
    "s_barrier\n"
    "ds_read_b64_tr_b16 a[64:65], v10\n"
    "ds_read_b64_tr_b16 a[66:67], v10 offset:4096\n"
    "ds_read_b64_tr_b16 a[68:69], v10 offset:8192\n"
    "ds_read_b64_tr_b16 a[70:71], v10 offset:12288\n"
    "ds_read_b128 a[160:163], v8 offset:32768\n"
    "ds_read_b128 a[164:167], v9 offset:32768\n"
    "ds_read_b128 a[168:171], v8 offset:34816\n"
    "ds_read_b128 a[172:175], v9 offset:34816\n"
    "ds_read_b128 a[176:179], v8 offset:49152\n"
    "ds_read_b128 a[180:183], v9 offset:49152\n"
    "ds_read_b128 a[184:187], v8 offset:51200\n"
    "ds_read_b128 a[188:191], v9 offset:51200\n"
    "ds_read_b64_tr_b16 a[72:73], v11\n"
    "ds_read_b64_tr_b16 a[74:75], v11 offset:4096\n"
    "ds_read_b64_tr_b16 a[76:77], v11 offset:8192\n"
    "ds_read_b64_tr_b16 a[78:79], v11 offset:12288\n"
    "s_add_u32 s45, s53, 2\n"
    "s_and_b32 s45, s45, 7\n"
    "s_lshl_b32 s51, s45, 7\n"
    "s_lshl_b32 s52, s45, 19\n"
    "v_add_u32_e32 v20, s51, v18\n"
    "v_add_u32_e32 v21, s51, v19\n"
    "v_add_u32_e32 v23, s52, v22\n"
    "v_xor_b32_e32 v56, 32, v23\n"
    "s_add_u32 m0, s55, 0x18000\n"
    "s_waitcnt lgkmcnt(4)\n"
    "buffer_load_dwordx4 v20, s[32:35], s64 offen lds\n"
    "buffer_load_dwordx4 v21, s[32:35], s65 offen offset:1024 lds\n"
    "v_mfma_f32_16x16x32_bf16 v[128:131], a[64:67], a[160:163], 0\n"
    "buffer_load_dwordx4 v20, s[32:35], s66 offen offset:2048 lds\n"
    "v_mfma_f32_16x16x32_bf16 v[128:131], a[68:71], a[164:167], v[128:131]\n"
    "v_mfma_f32_16x16x32_bf16 v[160:163], a[68:71], a[172:175], 0\n"
    "buffer_load_dwordx4 v21, s[32:35], s67 offen offset:3072 lds\n"
    "v_mfma_f32_16x16x32_bf16 v[160:163], a[64:67], a[168:171], v[160:163]\n"
    "s_add_u32 m0, s55, 0x1c000\n"
    "ds_read_b64_tr_b16 a[80:81], v10 offset:256\n"
    "v_mfma_f32_16x16x32_bf16 v[192:195], a[64:67], a[176:179], 0\n"
    "ds_read_b64_tr_b16 a[82:83], v10 offset:4352\n"
    "v_mfma_f32_16x16x32_bf16 v[192:195], a[68:71], a[180:183], v[192:195]\n"
    "ds_read_b64_tr_b16 a[84:85], v10 offset:8448\n"
    "buffer_load_dwordx4 v20, s[32:35], s68 offen lds\n"
    "v_mfma_f32_16x16x32_bf16 v[224:227], a[68:71], a[188:191], 0\n"
    "ds_read_b64_tr_b16 a[86:87], v10 offset:12544\n"
    "v_mfma_f32_16x16x32_bf16 v[224:227], a[64:67], a[184:187], v[224:227]\n"
    "s_waitcnt lgkmcnt(4)\n"
    "buffer_load_dwordx4 v21, s[32:35], s69 offen offset:1024 lds\n"
    "v_mfma_f32_16x16x32_bf16 v[132:135], a[72:75], a[160:163], 0\n"
    "v_mfma_f32_16x16x32_bf16 v[132:135], a[76:79], a[164:167], v[132:135]\n"
    "v_mfma_f32_16x16x32_bf16 v[164:167], a[76:79], a[172:175], 0\n"
    "buffer_load_dwordx4 v20, s[32:35], s70 offen offset:2048 lds\n"
    "v_mfma_f32_16x16x32_bf16 v[164:167], a[72:75], a[168:171], v[164:167]\n"
    "ds_read_b64_tr_b16 a[88:89], v11 offset:256\n"
    "v_mfma_f32_16x16x32_bf16 v[196:199], a[72:75], a[176:179], 0\n"
    "ds_read_b64_tr_b16 a[90:91], v11 offset:4352\n"
    "buffer_load_dwordx4 v21, s[32:35], s71 offen offset:3072 lds\n"
    "v_mfma_f32_16x16x32_bf16 v[196:199], a[76:79], a[180:183], v[196:199]\n"
    "s_add_u32 m0, s55, 0xc000\n"
    "ds_read_b64_tr_b16 a[92:93], v11 offset:8448\n"
    "v_mfma_f32_16x16x32_bf16 v[228:231], a[76:79], a[188:191], 0\n"
    "ds_read_b64_tr_b16 a[94:95], v11 offset:12544\n"
    "v_mfma_f32_16x16x32_bf16 v[228:231], a[72:75], a[184:187], v[228:231]\n"
    "s_waitcnt lgkmcnt(4)\n"
    "buffer_load_dwordx4 v23, s[36:39], s72 offen lds\n"
    "v_mfma_f32_16x16x32_bf16 v[136:139], a[80:83], a[160:163], 0\n"
    "v_mfma_f32_16x16x32_bf16 v[136:139], a[84:87], a[164:167], v[136:139]\n"
    "buffer_load_dwordx4 v56, s[36:39], s73 offen offset:1024 lds\n"
    "v_mfma_f32_16x16x32_bf16 v[168:171], a[84:87], a[172:175], 0\n"
    "v_mfma_f32_16x16x32_bf16 v[168:171], a[80:83], a[168:171], v[168:171]\n"
    "ds_read_b64_tr_b16 a[96:97], v12\n"
    "v_mfma_f32_16x16x32_bf16 v[200:203], a[80:83], a[176:179], 0\n"
    "s_waitcnt vmcnt(10)\n"
    "ds_read_b64_tr_b16 a[98:99], v12 offset:4096\n"
    "buffer_load_dwordx4 v23, s[36:39], s74 offen offset:2048 lds\n"
    "v_mfma_f32_16x16x32_bf16 v[200:203], a[84:87], a[180:183], v[200:203]\n"
    "ds_read_b64_tr_b16 a[100:101], v12 offset:8192\n"
    "ds_read_b128 a[192:195], v8\n"
    "v_mfma_f32_16x16x32_bf16 v[232:235], a[84:87], a[188:191], 0\n"
    "ds_read_b64_tr_b16 a[102:103], v12 offset:12288\n"
    "ds_read_b128 a[196:199], v9\n"
    "v_mfma_f32_16x16x32_bf16 v[232:235], a[80:83], a[184:187], v[232:235]\n"
    "s_waitcnt lgkmcnt(6)\n"
    "ds_read_b128 a[200:203], v8 offset:2048\n"
    "buffer_load_dwordx4 v56, s[36:39], s75 offen offset:3072 lds\n"
    "v_mfma_f32_16x16x32_bf16 v[140:143], a[88:91], a[160:163], 0\n"
    "ds_read_b128 a[204:207], v9 offset:2048\n"
    "v_mfma_f32_16x16x32_bf16 v[140:143], a[92:95], a[164:167], v[140:143]\n"
    "ds_read_b64_tr_b16 a[104:105], v13\n"
    "ds_read_b128 a[208:211], v8 offset:16384\n"
    "v_mfma_f32_16x16x32_bf16 v[172:175], a[92:95], a[172:175], 0\n"
    "ds_read_b64_tr_b16 a[106:107], v13 offset:4096\n"
    "ds_read_b128 a[212:215], v9 offset:16384\n"
    "v_mfma_f32_16x16x32_bf16 v[172:175], a[88:91], a[168:171], v[172:175]\n"
    "ds_read_b64_tr_b16 a[108:109], v13 offset:8192\n"
    "ds_read_b128 a[216:219], v8 offset:18432\n"
    "v_mfma_f32_16x16x32_bf16 v[204:207], a[88:91], a[176:179], 0\n"
    "ds_read_b64_tr_b16 a[110:111], v13 offset:12288\n"
    "ds_read_b128 a[220:223], v9 offset:18432\n"
    "v_mfma_f32_16x16x32_bf16 v[204:207], a[92:95], a[180:183], v[204:207]\n"
    "s_add_u32 s45, s53, 3\n"
    "v_mfma_f32_16x16x32_bf16 v[236:239], a[92:95], a[188:191], 0\n"
    "s_and_b32 s45, s45, 7\n"
    "v_mfma_f32_16x16x32_bf16 v[236:239], a[88:91], a[184:187], v[236:239]\n"
    "s_waitcnt lgkmcnt(11)\n"
    "s_lshl_b32 s51, s45, 7\n"
    "ds_read_b64_tr_b16 a[112:113], v12 offset:256\n"
    "v_mfma_f32_16x16x32_bf16 v[144:147], a[96:99], a[160:163], 0\n"
    "s_lshl_b32 s52, s45, 19\n"
    "ds_read_b64_tr_b16 a[114:115], v12 offset:4352\n"
    "v_mfma_f32_16x16x32_bf16 v[144:147], a[100:103], a[164:167], v[144:147]\n"
    "ds_read_b64_tr_b16 a[116:117], v12 offset:8448\n"
    "v_add_u32_e32 v20, s51, v18\n"
    "v_mfma_f32_16x16x32_bf16 v[176:179], a[100:103], a[172:175], 0\n"
    "ds_read_b64_tr_b16 a[118:119], v12 offset:12544\n"
    "v_add_u32_e32 v21, s51, v19\n"
    "v_mfma_f32_16x16x32_bf16 v[176:179], a[96:99], a[168:171], v[176:179]\n"
    "s_add_u32 m0, s55, 0x10000\n"
    "v_mfma_f32_16x16x32_bf16 v[208:211], a[96:99], a[176:179], 0\n"
    "v_mfma_f32_16x16x32_bf16 v[208:211], a[100:103], a[180:183], v[208:211]\n"
    "s_waitcnt lgkmcnt(4)\n"
    "ds_read_b64_tr_b16 a[120:121], v13 offset:256\n"
    "v_mfma_f32_16x16x32_bf16 v[148:151], a[108:111], a[164:167], 0\n"
    "ds_read_b64_tr_b16 a[122:123], v13 offset:4352\n"
    "v_mfma_f32_16x16x32_bf16 v[148:151], a[104:107], a[160:163], v[148:151]\n"
    "ds_read_b64_tr_b16 a[124:125], v13 offset:8448\n"
    "buffer_load_dwordx4 v20, s[32:35], s64 offen lds\n"
    "v_mfma_f32_16x16x32_bf16 v[180:183], a[104:107], a[168:171], 0\n"
    "ds_read_b64_tr_b16 a[126:127], v13 offset:12544\n"
    "v_mfma_f32_16x16x32_bf16 v[180:183], a[108:111], a[172:175], v[180:183]\n"
    "v_mfma_f32_16x16x32_bf16 v[212:215], a[108:111], a[180:183], 0\n"
    "buffer_load_dwordx4 v21, s[32:35], s65 offen offset:1024 lds\n"
    "v_mfma_f32_16x16x32_bf16 v[212:215], a[104:107], a[176:179], v[212:215]\n"
    "s_waitcnt lgkmcnt(4)\n"
    "v_mfma_f32_16x16x32_bf16 v[152:155], a[112:115], a[160:163], 0\n"
    "v_mfma_f32_16x16x32_bf16 v[152:155], a[116:119], a[164:167], v[152:155]\n"
    "s_waitcnt lgkmcnt(0)\n"
    "v_mfma_f32_16x16x32_bf16 v[184:187], a[116:119], a[172:175], 0\n"
    "s_waitcnt vmcnt(22)\n"
    "buffer_load_dwordx4 v20, s[32:35], s66 offen offset:2048 lds\n"
    "v_mfma_f32_16x16x32_bf16 v[184:187], a[112:115], a[168:171], v[184:187]\n"
    "s_barrier\n"
    "ds_read_b64_tr_b16 a[64:65], v10 offset:16384\n"
    "ds_read_b64_tr_b16 a[66:67], v10 offset:20480\n"
    "v_mfma_f32_16x16x32_bf16 v[216:219], a[112:115], a[176:179], 0\n"
    "ds_read_b64_tr_b16 a[68:69], v10 offset:24576\n"
    "ds_read_b64_tr_b16 a[70:71], v10 offset:28672\n"
    "v_mfma_f32_16x16x32_bf16 v[216:219], a[116:119], a[180:183], v[216:219]\n"
    "v_mfma_f32_16x16x32_bf16 v[156:159], a[124:127], a[164:167], 0\n"
    "ds_read_b64_tr_b16 a[72:73], v11 offset:16384\n"
    "buffer_load_dwordx4 v21, s[32:35], s67 offen offset:3072 lds\n"
    "v_mfma_f32_16x16x32_bf16 v[156:159], a[120:123], a[160:163], v[156:159]\n"
    "s_add_u32 m0, s55, 0x14000\n"
    "ds_read_b64_tr_b16 a[74:75], v11 offset:20480\n"
    "v_mfma_f32_16x16x32_bf16 v[188:191], a[120:123], a[168:171], 0\n"
    "ds_read_b64_tr_b16 a[76:77], v11 offset:24576\n"
    "v_mfma_f32_16x16x32_bf16 v[188:191], a[124:127], a[172:175], v[188:191]\n"
    "ds_read_b64_tr_b16 a[78:79], v11 offset:28672\n"
    "buffer_load_dwordx4 v20, s[32:35], s68 offen lds\n"
    "v_mfma_f32_16x16x32_bf16 v[220:223], a[124:127], a[180:183], 0\n"
    "v_mfma_f32_16x16x32_bf16 v[220:223], a[120:123], a[176:179], v[220:223]\n"
    "s_waitcnt lgkmcnt(4)\n"
    "v_mfma_f32_16x16x32_bf16 v[128:131], a[64:67], a[192:195], v[128:131]\n"
    "v_mfma_f32_16x16x32_bf16 v[128:131], a[68:71], a[196:199], v[128:131]\n"
    "buffer_load_dwordx4 v21, s[32:35], s69 offen offset:1024 lds\n"
    "v_mfma_f32_16x16x32_bf16 v[160:163], a[68:71], a[204:207], v[160:163]\n"
    "s_add_u32 s42, s78, 0\n"
    "v_mfma_f32_16x16x32_bf16 v[160:163], a[64:67], a[200:203], v[160:163]\n"
    "s_and_b32 s42, s42, 0x3ffff\n"
    "ds_read_b64_tr_b16 a[80:81], v10 offset:16640\n"
    "v_mfma_f32_16x16x32_bf16 v[192:195], a[64:67], a[208:211], v[192:195]\n"
    "s_add_u32 m0, s55, 0\n"
    "ds_read_b64_tr_b16 a[82:83], v10 offset:20736\n"
    "v_mfma_f32_16x16x32_bf16 v[192:195], a[68:71], a[212:215], v[192:195]\n"
    "ds_read_b64_tr_b16 a[84:85], v10 offset:24832\n"
    "buffer_load_dwordx4 v116, s[24:27], s42 offen nt lds\n"
    "v_mfma_f32_16x16x32_bf16 v[224:227], a[68:71], a[220:223], v[224:227]\n"
    "s_add_u32 m0, s55, 0x14000\n"
    "ds_read_b64_tr_b16 a[86:87], v10 offset:28928\n"
    "v_mfma_f32_16x16x32_bf16 v[224:227], a[64:67], a[216:219], v[224:227]\n"
    "s_waitcnt lgkmcnt(4)\n"
    "buffer_load_dwordx4 v20, s[32:35], s70 offen offset:2048 lds\n"
    "v_mfma_f32_16x16x32_bf16 v[132:135], a[72:75], a[192:195], v[132:135]\n"
    "buffer_load_dwordx4 v21, s[32:35], s71 offen offset:3072 lds\n"
    "v_mfma_f32_16x16x32_bf16 v[132:135], a[76:79], a[196:199], v[132:135]\n"
    "v_add_u32_e32 v23, s52, v22\n"
    "v_mfma_f32_16x16x32_bf16 v[164:167], a[76:79], a[204:207], v[164:167]\n"
    "v_xor_b32_e32 v56, 32, v23\n"
    "v_mfma_f32_16x16x32_bf16 v[164:167], a[72:75], a[200:203], v[164:167]\n"
    "s_add_u32 m0, s55, 0x20000\n"
    "ds_read_b64_tr_b16 a[88:89], v11 offset:16640\n"
    "v_mfma_f32_16x16x32_bf16 v[196:199], a[72:75], a[208:211], v[196:199]\n"
    "ds_read_b64_tr_b16 a[90:91], v11 offset:20736\n"
    "buffer_load_dwordx4 v23, s[36:39], s72 offen lds\n"
    "v_mfma_f32_16x16x32_bf16 v[196:199], a[76:79], a[212:215], v[196:199]\n"
    "ds_read_b64_tr_b16 a[92:93], v11 offset:24832\n"
    "v_mfma_f32_16x16x32_bf16 v[228:231], a[76:79], a[220:223], v[228:231]\n"
    "ds_read_b64_tr_b16 a[94:95], v11 offset:28928\n"
    "v_mfma_f32_16x16x32_bf16 v[228:231], a[72:75], a[216:219], v[228:231]\n"
    "s_waitcnt lgkmcnt(4)\n"
    "v_mfma_f32_16x16x32_bf16 v[136:139], a[80:83], a[192:195], v[136:139]\n"
    "buffer_load_dwordx4 v56, s[36:39], s73 offen offset:1024 lds\n"
    "v_mfma_f32_16x16x32_bf16 v[136:139], a[84:87], a[196:199], v[136:139]\n"
    "v_mfma_f32_16x16x32_bf16 v[168:171], a[84:87], a[204:207], v[168:171]\n"
    "v_mfma_f32_16x16x32_bf16 v[168:171], a[80:83], a[200:203], v[168:171]\n"
    "ds_read_b64_tr_b16 a[96:97], v12 offset:16384\n"
    "v_mfma_f32_16x16x32_bf16 v[200:203], a[80:83], a[208:211], v[200:203]\n"
    "s_waitcnt vmcnt(15)\n"
    "ds_read_b64_tr_b16 a[98:99], v12 offset:20480\n"
    "buffer_load_dwordx4 v23, s[36:39], s74 offen offset:2048 lds\n"
    "v_mfma_f32_16x16x32_bf16 v[200:203], a[84:87], a[212:215], v[200:203]\n"
    "ds_read_b64_tr_b16 a[100:101], v12 offset:24576\n"
    "ds_read_b128 a[160:163], v8 offset:32768\n"
    "v_mfma_f32_16x16x32_bf16 v[232:235], a[84:87], a[220:223], v[232:235]\n"
    "ds_read_b64_tr_b16 a[102:103], v12 offset:28672\n"
    "ds_read_b128 a[164:167], v9 offset:32768\n"
    "v_mfma_f32_16x16x32_bf16 v[232:235], a[80:83], a[216:219], v[232:235]\n"
    "s_waitcnt lgkmcnt(6)\n"
    "ds_read_b128 a[168:171], v8 offset:34816\n"
    "buffer_load_dwordx4 v56, s[36:39], s75 offen offset:3072 lds\n"
    "v_mfma_f32_16x16x32_bf16 v[140:143], a[88:91], a[192:195], v[140:143]\n"
    "ds_read_b128 a[172:175], v9 offset:34816\n"
    "v_mfma_f32_16x16x32_bf16 v[140:143], a[92:95], a[196:199], v[140:143]\n"
    "ds_read_b64_tr_b16 a[104:105], v13 offset:16384\n"
    "ds_read_b128 a[176:179], v8 offset:49152\n"
    "v_mfma_f32_16x16x32_bf16 v[172:175], a[92:95], a[204:207], v[172:175]\n"
    "ds_read_b64_tr_b16 a[106:107], v13 offset:20480\n"
    "ds_read_b128 a[180:183], v9 offset:49152\n"
    "v_mfma_f32_16x16x32_bf16 v[172:175], a[88:91], a[200:203], v[172:175]\n"
    "ds_read_b64_tr_b16 a[108:109], v13 offset:24576\n"
    "ds_read_b128 a[184:187], v8 offset:51200\n"
    "v_mfma_f32_16x16x32_bf16 v[204:207], a[88:91], a[208:211], v[204:207]\n"
    "ds_read_b64_tr_b16 a[110:111], v13 offset:28672\n"
    "ds_read_b128 a[188:191], v9 offset:51200\n"
    "v_mfma_f32_16x16x32_bf16 v[204:207], a[92:95], a[212:215], v[204:207]\n"
    "v_mfma_f32_16x16x32_bf16 v[236:239], a[92:95], a[220:223], v[236:239]\n"
    "buffer_load_dword v32, v32, s[56:59], s22 offen sc1\n"
    "v_mfma_f32_16x16x32_bf16 v[236:239], a[88:91], a[216:219], v[236:239]\n"
    "s_waitcnt lgkmcnt(11)\n"
    "s_add_u32 m0, s55, 0\n"
    "ds_read_b64_tr_b16 a[112:113], v12 offset:16640\n"
    "v_mfma_f32_16x16x32_bf16 v[144:147], a[96:99], a[192:195], v[144:147]\n"
    "ds_read_b64_tr_b16 a[114:115], v12 offset:20736\n"
    "buffer_load_dwordx4 v117, s[24:27], s42 offen offset:1024 nt lds\n"
    "v_mfma_f32_16x16x32_bf16 v[144:147], a[100:103], a[196:199], v[144:147]\n"
    "ds_read_b64_tr_b16 a[116:117], v12 offset:24832\n"
    "v_mfma_f32_16x16x32_bf16 v[176:179], a[100:103], a[204:207], v[176:179]\n"
    "ds_read_b64_tr_b16 a[118:119], v12 offset:28928\n"
    "buffer_load_dword v33, v33, s[56:59], s23 offen sc1\n"
    "v_mfma_f32_16x16x32_bf16 v[176:179], a[96:99], a[200:203], v[176:179]\n"
    "s_add_u32 s45, s53, 4\n"
    "v_mfma_f32_16x16x32_bf16 v[208:211], a[96:99], a[208:211], v[208:211]\n"
    "s_and_b32 s45, s45, 7\n"
    "v_mfma_f32_16x16x32_bf16 v[208:211], a[100:103], a[212:215], v[208:211]\n"
    "s_waitcnt lgkmcnt(4)\n"
    "s_lshl_b32 s51, s45, 7\n"
    "ds_read_b64_tr_b16 a[120:121], v13 offset:16640\n"
    "v_mfma_f32_16x16x32_bf16 v[148:151], a[108:111], a[196:199], v[148:151]\n"
    "s_lshl_b32 s52, s45, 19\n"
    "ds_read_b64_tr_b16 a[122:123], v13 offset:20736\n"
    "v_mfma_f32_16x16x32_bf16 v[148:151], a[104:107], a[192:195], v[148:151]\n"
    "ds_read_b64_tr_b16 a[124:125], v13 offset:24832\n"
    "v_add_u32_e32 v20, s51, v18\n"
    "v_mfma_f32_16x16x32_bf16 v[180:183], a[104:107], a[200:203], v[180:183]\n"
    "ds_read_b64_tr_b16 a[126:127], v13 offset:28928\n"
    "v_add_u32_e32 v21, s51, v19\n"
    "v_mfma_f32_16x16x32_bf16 v[180:183], a[108:111], a[204:207], v[180:183]\n"
    "s_add_u32 m0, s55, 0x18000\n"
    "v_mfma_f32_16x16x32_bf16 v[212:215], a[108:111], a[212:215], v[212:215]\n"
    "buffer_load_dwordx4 v20, s[32:35], s64 offen lds\n"
    "v_mfma_f32_16x16x32_bf16 v[212:215], a[104:107], a[208:211], v[212:215]\n"
    "s_waitcnt lgkmcnt(4)\n"
    "buffer_load_dwordx4 v21, s[32:35], s65 offen offset:1024 lds\n"
    "v_mfma_f32_16x16x32_bf16 v[152:155], a[112:115], a[192:195], v[152:155]\n"
    "v_mfma_f32_16x16x32_bf16 v[152:155], a[116:119], a[196:199], v[152:155]\n"
    "s_waitcnt lgkmcnt(0)\n"
    "v_mfma_f32_16x16x32_bf16 v[184:187], a[116:119], a[204:207], v[184:187]\n"
    "s_waitcnt vmcnt(18)\n"
    "buffer_load_dwordx4 v20, s[32:35], s66 offen offset:2048 lds\n"
    "v_mfma_f32_16x16x32_bf16 v[184:187], a[112:115], a[200:203], v[184:187]\n"
    "s_barrier\n"
    "ds_read_b64_tr_b16 a[64:65], v14\n"
    "ds_read_b64_tr_b16 a[66:67], v14 offset:4096\n"
    "v_mfma_f32_16x16x32_bf16 v[216:219], a[112:115], a[208:211], v[216:219]\n"
    "ds_read_b64_tr_b16 a[68:69], v14 offset:8192\n"
    "ds_read_b64_tr_b16 a[70:71], v14 offset:12288\n"
    "v_mfma_f32_16x16x32_bf16 v[216:219], a[116:119], a[212:215], v[216:219]\n"
    "v_mfma_f32_16x16x32_bf16 v[156:159], a[124:127], a[196:199], v[156:159]\n"
    "ds_read_b64_tr_b16 a[72:73], v15\n"
    "buffer_load_dwordx4 v21, s[32:35], s67 offen offset:3072 lds\n"
    "v_mfma_f32_16x16x32_bf16 v[156:159], a[120:123], a[192:195], v[156:159]\n"
    "s_add_u32 m0, s55, 0x1c000\n"
    "ds_read_b64_tr_b16 a[74:75], v15 offset:4096\n"
    "v_mfma_f32_16x16x32_bf16 v[188:191], a[120:123], a[200:203], v[188:191]\n"
    "ds_read_b64_tr_b16 a[76:77], v15 offset:8192\n"
    "v_mfma_f32_16x16x32_bf16 v[188:191], a[124:127], a[204:207], v[188:191]\n"
    "ds_read_b64_tr_b16 a[78:79], v15 offset:12288\n"
    "buffer_load_dwordx4 v20, s[32:35], s68 offen lds\n"
    "v_mfma_f32_16x16x32_bf16 v[220:223], a[124:127], a[212:215], v[220:223]\n"
    "v_mfma_f32_16x16x32_bf16 v[220:223], a[120:123], a[208:211], v[220:223]\n"
    "s_waitcnt lgkmcnt(4)\n"
    "v_mfma_f32_16x16x32_bf16 v[128:131], a[64:67], a[160:163], v[128:131]\n"
    "v_mfma_f32_16x16x32_bf16 v[128:131], a[68:71], a[164:167], v[128:131]\n"
    "buffer_load_dwordx4 v21, s[32:35], s69 offen offset:1024 lds\n"
    "v_mfma_f32_16x16x32_bf16 v[160:163], a[68:71], a[172:175], v[160:163]\n"
    "s_add_u32 m0, s55, 0\n"
    "v_mfma_f32_16x16x32_bf16 v[160:163], a[64:67], a[168:171], v[160:163]\n"
    "ds_read_b64_tr_b16 a[80:81], v14 offset:256\n"
    "v_mfma_f32_16x16x32_bf16 v[192:195], a[64:67], a[176:179], v[192:195]\n"
    "ds_read_b64_tr_b16 a[82:83], v14 offset:4352\n"
    "buffer_load_dwordx4 v118, s[24:27], s42 offen offset:2048 nt lds\n"
    "v_mfma_f32_16x16x32_bf16 v[192:195], a[68:71], a[180:183], v[192:195]\n"
    "s_add_u32 m0, s55, 0x1c000\n"
    "ds_read_b64_tr_b16 a[84:85], v14 offset:8448\n"
    "v_mfma_f32_16x16x32_bf16 v[224:227], a[68:71], a[188:191], v[224:227]\n"
    "ds_read_b64_tr_b16 a[86:87], v14 offset:12544\n"
    "buffer_load_dwordx4 v20, s[32:35], s70 offen offset:2048 lds\n"
    "v_mfma_f32_16x16x32_bf16 v[224:227], a[64:67], a[184:187], v[224:227]\n"
    "s_waitcnt lgkmcnt(4)\n"
    "v_mfma_f32_16x16x32_bf16 v[132:135], a[72:75], a[160:163], v[132:135]\n"
    "buffer_load_dwordx4 v21, s[32:35], s71 offen offset:3072 lds\n"
    "v_mfma_f32_16x16x32_bf16 v[132:135], a[76:79], a[164:167], v[132:135]\n"
    "v_add_u32_e32 v23, s52, v22\n"
    "v_mfma_f32_16x16x32_bf16 v[164:167], a[76:79], a[172:175], v[164:167]\n"
    "v_xor_b32_e32 v56, 32, v23\n"
    "v_mfma_f32_16x16x32_bf16 v[164:167], a[72:75], a[168:171], v[164:167]\n"
    "s_add_u32 m0, s55, 0x24000\n"
    "ds_read_b64_tr_b16 a[88:89], v15 offset:256\n"
    "v_mfma_f32_16x16x32_bf16 v[196:199], a[72:75], a[176:179], v[196:199]\n"
    "ds_read_b64_tr_b16 a[90:91], v15 offset:4352\n"
    "buffer_load_dwordx4 v23, s[36:39], s72 offen lds\n"
    "v_mfma_f32_16x16x32_bf16 v[196:199], a[76:79], a[180:183], v[196:199]\n"
    "ds_read_b64_tr_b16 a[92:93], v15 offset:8448\n"
    "v_mfma_f32_16x16x32_bf16 v[228:231], a[76:79], a[188:191], v[228:231]\n"
    "ds_read_b64_tr_b16 a[94:95], v15 offset:12544\n"
    "v_mfma_f32_16x16x32_bf16 v[228:231], a[72:75], a[184:187], v[228:231]\n"
    "s_waitcnt lgkmcnt(4)\n"
    "v_mfma_f32_16x16x32_bf16 v[136:139], a[80:83], a[160:163], v[136:139]\n"
    "buffer_load_dwordx4 v56, s[36:39], s73 offen offset:1024 lds\n"
    "v_mfma_f32_16x16x32_bf16 v[136:139], a[84:87], a[164:167], v[136:139]\n"
    "v_mfma_f32_16x16x32_bf16 v[168:171], a[84:87], a[172:175], v[168:171]\n"
    "v_mfma_f32_16x16x32_bf16 v[168:171], a[80:83], a[168:171], v[168:171]\n"
    "ds_read_b64_tr_b16 a[96:97], v16\n"
    "v_mfma_f32_16x16x32_bf16 v[200:203], a[80:83], a[176:179], v[200:203]\n"
    "s_waitcnt vmcnt(18)\n"
    "ds_read_b64_tr_b16 a[98:99], v16 offset:4096\n"
    "buffer_load_dwordx4 v23, s[36:39], s74 offen offset:2048 lds\n"
    "v_mfma_f32_16x16x32_bf16 v[200:203], a[84:87], a[180:183], v[200:203]\n"
    "ds_read_b64_tr_b16 a[100:101], v16 offset:8192\n"
    "ds_read_b128 a[192:195], v8\n"
    "v_mfma_f32_16x16x32_bf16 v[232:235], a[84:87], a[188:191], v[232:235]\n"
    "ds_read_b64_tr_b16 a[102:103], v16 offset:12288\n"
    "ds_read_b128 a[196:199], v9\n"
    "v_mfma_f32_16x16x32_bf16 v[232:235], a[80:83], a[184:187], v[232:235]\n"
    "s_waitcnt lgkmcnt(6)\n"
    "ds_read_b128 a[200:203], v8 offset:2048\n"
    "buffer_load_dwordx4 v56, s[36:39], s75 offen offset:3072 lds\n"
    "v_mfma_f32_16x16x32_bf16 v[140:143], a[88:91], a[160:163], v[140:143]\n"
    "s_add_u32 m0, s55, 0\n"
    "ds_read_b128 a[204:207], v9 offset:2048\n"
    "v_mfma_f32_16x16x32_bf16 v[140:143], a[92:95], a[164:167], v[140:143]\n"
    "ds_read_b64_tr_b16 a[104:105], v17\n"
    "ds_read_b128 a[208:211], v8 offset:16384\n"
    "v_mfma_f32_16x16x32_bf16 v[172:175], a[92:95], a[172:175], v[172:175]\n"
    "ds_read_b64_tr_b16 a[106:107], v17 offset:4096\n"
    "ds_read_b128 a[212:215], v9 offset:16384\n"
    "v_mfma_f32_16x16x32_bf16 v[172:175], a[88:91], a[168:171], v[172:175]\n"
    "ds_read_b64_tr_b16 a[108:109], v17 offset:8192\n"
    "ds_read_b128 a[216:219], v8 offset:18432\n"
    "v_mfma_f32_16x16x32_bf16 v[204:207], a[88:91], a[176:179], v[204:207]\n"
    "ds_read_b64_tr_b16 a[110:111], v17 offset:12288\n"
    "ds_read_b128 a[220:223], v9 offset:18432\n"
    "v_mfma_f32_16x16x32_bf16 v[204:207], a[92:95], a[180:183], v[204:207]\n"
    "v_mfma_f32_16x16x32_bf16 v[236:239], a[92:95], a[188:191], v[236:239]\n"
    "v_mfma_f32_16x16x32_bf16 v[236:239], a[88:91], a[184:187], v[236:239]\n"
    "s_waitcnt lgkmcnt(11)\n"
    "ds_read_b64_tr_b16 a[112:113], v16 offset:256\n"
    "v_mfma_f32_16x16x32_bf16 v[144:147], a[96:99], a[160:163], v[144:147]\n"
    "ds_read_b64_tr_b16 a[114:115], v16 offset:4352\n"
    "buffer_load_dwordx4 v119, s[24:27], s42 offen offset:3072 nt lds\n"
    "v_mfma_f32_16x16x32_bf16 v[144:147], a[100:103], a[164:167], v[144:147]\n"
    "s_add_u32 s45, s53, 5\n"
    "ds_read_b64_tr_b16 a[116:117], v16 offset:8448\n"
    "v_mfma_f32_16x16x32_bf16 v[176:179], a[100:103], a[172:175], v[176:179]\n"
    "s_and_b32 s45, s45, 7\n"
    "ds_read_b64_tr_b16 a[118:119], v16 offset:12544\n"
    "v_mfma_f32_16x16x32_bf16 v[176:179], a[96:99], a[168:171], v[176:179]\n"
    "s_lshl_b32 s51, s45, 7\n"
    "v_mfma_f32_16x16x32_bf16 v[208:211], a[96:99], a[176:179], v[208:211]\n"
    "s_lshl_b32 s52, s45, 19\n"
    "v_mfma_f32_16x16x32_bf16 v[208:211], a[100:103], a[180:183], v[208:211]\n"
    "s_waitcnt lgkmcnt(4)\n"
    "ds_read_b64_tr_b16 a[120:121], v17 offset:256\n"
    "v_add_u32_e32 v20, s51, v18\n"
    "v_mfma_f32_16x16x32_bf16 v[148:151], a[108:111], a[164:167], v[148:151]\n"
    "ds_read_b64_tr_b16 a[122:123], v17 offset:4352\n"
    "v_add_u32_e32 v21, s51, v19\n"
    "v_mfma_f32_16x16x32_bf16 v[148:151], a[104:107], a[160:163], v[148:151]\n"
    "s_add_u32 m0, s55, 0x10000\n"
    "ds_read_b64_tr_b16 a[124:125], v17 offset:8448\n"
    "v_mfma_f32_16x16x32_bf16 v[180:183], a[104:107], a[168:171], v[180:183]\n"
    "ds_read_b64_tr_b16 a[126:127], v17 offset:12544\n"
    "buffer_load_dwordx4 v20, s[32:35], s64 offen lds\n"
    "v_mfma_f32_16x16x32_bf16 v[180:183], a[108:111], a[172:175], v[180:183]\n"
    "v_mfma_f32_16x16x32_bf16 v[212:215], a[108:111], a[180:183], v[212:215]\n"
    "buffer_load_dwordx4 v21, s[32:35], s65 offen offset:1024 lds\n"
    "v_mfma_f32_16x16x32_bf16 v[212:215], a[104:107], a[176:179], v[212:215]\n"
    "s_waitcnt lgkmcnt(4)\n"
    "v_mfma_f32_16x16x32_bf16 v[152:155], a[112:115], a[160:163], v[152:155]\n"
    "v_mfma_f32_16x16x32_bf16 v[152:155], a[116:119], a[164:167], v[152:155]\n"
    "s_waitcnt lgkmcnt(0)\n"
    "v_mfma_f32_16x16x32_bf16 v[184:187], a[116:119], a[172:175], v[184:187]\n"
    "s_waitcnt vmcnt(19)\n"
    "buffer_load_dwordx4 v20, s[32:35], s66 offen offset:2048 lds\n"
    "v_mfma_f32_16x16x32_bf16 v[184:187], a[112:115], a[168:171], v[184:187]\n"
    "s_barrier\n"
    "ds_read_b64_tr_b16 a[64:65], v10\n"
    "ds_read_b64_tr_b16 a[66:67], v10 offset:4096\n"
    "v_mfma_f32_16x16x32_bf16 v[216:219], a[112:115], a[176:179], v[216:219]\n"
    "ds_read_b64_tr_b16 a[68:69], v10 offset:8192\n"
    "ds_read_b64_tr_b16 a[70:71], v10 offset:12288\n"
    "v_mfma_f32_16x16x32_bf16 v[216:219], a[116:119], a[180:183], v[216:219]\n"
    "v_mfma_f32_16x16x32_bf16 v[156:159], a[124:127], a[164:167], v[156:159]\n"
    "ds_read_b64_tr_b16 a[72:73], v11\n"
    "buffer_load_dwordx4 v21, s[32:35], s67 offen offset:3072 lds\n"
    "v_mfma_f32_16x16x32_bf16 v[156:159], a[120:123], a[160:163], v[156:159]\n"
    "s_add_u32 m0, s55, 0x14000\n"
    "ds_read_b64_tr_b16 a[74:75], v11 offset:4096\n"
    "v_mfma_f32_16x16x32_bf16 v[188:191], a[120:123], a[168:171], v[188:191]\n"
    "ds_read_b64_tr_b16 a[76:77], v11 offset:8192\n"
    "v_mfma_f32_16x16x32_bf16 v[188:191], a[124:127], a[172:175], v[188:191]\n"
    "ds_read_b64_tr_b16 a[78:79], v11 offset:12288\n"
    "buffer_load_dwordx4 v20, s[32:35], s68 offen lds\n"
    "v_mfma_f32_16x16x32_bf16 v[220:223], a[124:127], a[180:183], v[220:223]\n"
    "v_mfma_f32_16x16x32_bf16 v[220:223], a[120:123], a[176:179], v[220:223]\n"
    "s_waitcnt lgkmcnt(4)\n"
    "v_mfma_f32_16x16x32_bf16 v[128:131], a[64:67], a[192:195], v[128:131]\n"
    "v_mfma_f32_16x16x32_bf16 v[128:131], a[68:71], a[196:199], v[128:131]\n"
    "buffer_load_dwordx4 v21, s[32:35], s69 offen offset:1024 lds\n"
    "v_mfma_f32_16x16x32_bf16 v[160:163], a[68:71], a[204:207], v[160:163]\n"
    "s_add_u32 s42, s78, 0\n"
    "v_mfma_f32_16x16x32_bf16 v[160:163], a[64:67], a[200:203], v[160:163]\n"
    "s_and_b32 s42, s42, 0x3ffff\n"
    "ds_read_b64_tr_b16 a[80:81], v10 offset:256\n"
    "v_mfma_f32_16x16x32_bf16 v[192:195], a[64:67], a[208:211], v[192:195]\n"
    "s_add_u32 m0, s55, 0x4000\n"
    "ds_read_b64_tr_b16 a[82:83], v10 offset:4352\n"
    "v_mfma_f32_16x16x32_bf16 v[192:195], a[68:71], a[212:215], v[192:195]\n"
    "ds_read_b64_tr_b16 a[84:85], v10 offset:8448\n"
    "buffer_load_dwordx4 v116, s[28:31], s42 offen nt lds\n"
    "v_mfma_f32_16x16x32_bf16 v[224:227], a[68:71], a[220:223], v[224:227]\n"
    "s_add_u32 m0, s55, 0x14000\n"
    "ds_read_b64_tr_b16 a[86:87], v10 offset:12544\n"
    "v_mfma_f32_16x16x32_bf16 v[224:227], a[64:67], a[216:219], v[224:227]\n"
    "s_waitcnt lgkmcnt(4)\n"
    "buffer_load_dwordx4 v20, s[32:35], s70 offen offset:2048 lds\n"
    "v_mfma_f32_16x16x32_bf16 v[132:135], a[72:75], a[192:195], v[132:135]\n"
    "buffer_load_dwordx4 v21, s[32:35], s71 offen offset:3072 lds\n"
    "v_mfma_f32_16x16x32_bf16 v[132:135], a[76:79], a[196:199], v[132:135]\n"
    "v_add_u32_e32 v23, s52, v22\n"
    "v_mfma_f32_16x16x32_bf16 v[164:167], a[76:79], a[204:207], v[164:167]\n"
    "v_xor_b32_e32 v56, 32, v23\n"
    "v_mfma_f32_16x16x32_bf16 v[164:167], a[72:75], a[200:203], v[164:167]\n"
    "s_add_u32 m0, s55, 0xc000\n"
    "ds_read_b64_tr_b16 a[88:89], v11 offset:256\n"
    "v_mfma_f32_16x16x32_bf16 v[196:199], a[72:75], a[208:211], v[196:199]\n"
    "ds_read_b64_tr_b16 a[90:91], v11 offset:4352\n"
    "buffer_load_dwordx4 v23, s[36:39], s72 offen lds\n"
    "v_mfma_f32_16x16x32_bf16 v[196:199], a[76:79], a[212:215], v[196:199]\n"
    "ds_read_b64_tr_b16 a[92:93], v11 offset:8448\n"
    "v_mfma_f32_16x16x32_bf16 v[228:231], a[76:79], a[220:223], v[228:231]\n"
    "ds_read_b64_tr_b16 a[94:95], v11 offset:12544\n"
    "v_mfma_f32_16x16x32_bf16 v[228:231], a[72:75], a[216:219], v[228:231]\n"
    "s_waitcnt lgkmcnt(4)\n"
    "v_mfma_f32_16x16x32_bf16 v[136:139], a[80:83], a[192:195], v[136:139]\n"
    "buffer_load_dwordx4 v56, s[36:39], s73 offen offset:1024 lds\n"
    "v_mfma_f32_16x16x32_bf16 v[136:139], a[84:87], a[196:199], v[136:139]\n"
    "v_mfma_f32_16x16x32_bf16 v[168:171], a[84:87], a[204:207], v[168:171]\n"
    "v_mfma_f32_16x16x32_bf16 v[168:171], a[80:83], a[200:203], v[168:171]\n"
    "ds_read_b64_tr_b16 a[96:97], v12\n"
    "v_mfma_f32_16x16x32_bf16 v[200:203], a[80:83], a[208:211], v[200:203]\n"
    "s_waitcnt vmcnt(16)\n"
    "ds_read_b64_tr_b16 a[98:99], v12 offset:4096\n"
    "buffer_load_dwordx4 v23, s[36:39], s74 offen offset:2048 lds\n"
    "v_mfma_f32_16x16x32_bf16 v[200:203], a[84:87], a[212:215], v[200:203]\n"
    "ds_read_b64_tr_b16 a[100:101], v12 offset:8192\n"
    "ds_read_b128 a[160:163], v8 offset:32768\n"
    "v_mfma_f32_16x16x32_bf16 v[232:235], a[84:87], a[220:223], v[232:235]\n"
    "ds_read_b64_tr_b16 a[102:103], v12 offset:12288\n"
    "ds_read_b128 a[164:167], v9 offset:32768\n"
    "v_mfma_f32_16x16x32_bf16 v[232:235], a[80:83], a[216:219], v[232:235]\n"
    "s_waitcnt lgkmcnt(6)\n"
    "ds_read_b128 a[168:171], v8 offset:34816\n"
    "buffer_load_dwordx4 v56, s[36:39], s75 offen offset:3072 lds\n"
    "v_mfma_f32_16x16x32_bf16 v[140:143], a[88:91], a[192:195], v[140:143]\n"
    "s_add_u32 m0, s55, 0x4000\n"
    "ds_read_b128 a[172:175], v9 offset:34816\n"
    "v_mfma_f32_16x16x32_bf16 v[140:143], a[92:95], a[196:199], v[140:143]\n"
    "ds_read_b64_tr_b16 a[104:105], v13\n"
    "ds_read_b128 a[176:179], v8 offset:49152\n"
    "v_mfma_f32_16x16x32_bf16 v[172:175], a[92:95], a[204:207], v[172:175]\n"
    "ds_read_b64_tr_b16 a[106:107], v13 offset:4096\n"
    "ds_read_b128 a[180:183], v9 offset:49152\n"
    "v_mfma_f32_16x16x32_bf16 v[172:175], a[88:91], a[200:203], v[172:175]\n"
    "ds_read_b64_tr_b16 a[108:109], v13 offset:8192\n"
    "ds_read_b128 a[184:187], v8 offset:51200\n"
    "v_mfma_f32_16x16x32_bf16 v[204:207], a[88:91], a[208:211], v[204:207]\n"
    "ds_read_b64_tr_b16 a[110:111], v13 offset:12288\n"
    "ds_read_b128 a[188:191], v9 offset:51200\n"
    "v_mfma_f32_16x16x32_bf16 v[204:207], a[92:95], a[212:215], v[204:207]\n"
    "v_mfma_f32_16x16x32_bf16 v[236:239], a[92:95], a[220:223], v[236:239]\n"
    "v_mfma_f32_16x16x32_bf16 v[236:239], a[88:91], a[216:219], v[236:239]\n"
    "s_waitcnt lgkmcnt(11)\n"
    "ds_read_b64_tr_b16 a[112:113], v12 offset:256\n"
    "v_mfma_f32_16x16x32_bf16 v[144:147], a[96:99], a[192:195], v[144:147]\n"
    "ds_read_b64_tr_b16 a[114:115], v12 offset:4352\n"
    "buffer_load_dwordx4 v118, s[28:31], s42 offen offset:1024 nt lds\n"
    "v_mfma_f32_16x16x32_bf16 v[144:147], a[100:103], a[196:199], v[144:147]\n"
    "s_add_u32 s45, s53, 6\n"
    "ds_read_b64_tr_b16 a[116:117], v12 offset:8448\n"
    "v_mfma_f32_16x16x32_bf16 v[176:179], a[100:103], a[204:207], v[176:179]\n"
    "s_and_b32 s45, s45, 7\n"
    "ds_read_b64_tr_b16 a[118:119], v12 offset:12544\n"
    "v_mfma_f32_16x16x32_bf16 v[176:179], a[96:99], a[200:203], v[176:179]\n"
    "s_lshl_b32 s51, s45, 7\n"
    "v_mfma_f32_16x16x32_bf16 v[208:211], a[96:99], a[208:211], v[208:211]\n"
    "s_lshl_b32 s52, s45, 19\n"
    "v_mfma_f32_16x16x32_bf16 v[208:211], a[100:103], a[212:215], v[208:211]\n"
    "s_waitcnt lgkmcnt(4)\n"
    "ds_read_b64_tr_b16 a[120:121], v13 offset:256\n"
    "v_add_u32_e32 v20, s51, v18\n"
    "v_mfma_f32_16x16x32_bf16 v[148:151], a[108:111], a[196:199], v[148:151]\n"
    "ds_read_b64_tr_b16 a[122:123], v13 offset:4352\n"
    "v_add_u32_e32 v21, s51, v19\n"
    "v_mfma_f32_16x16x32_bf16 v[148:151], a[104:107], a[192:195], v[148:151]\n"
    "s_add_u32 m0, s55, 0x18000\n"
    "ds_read_b64_tr_b16 a[124:125], v13 offset:8448\n"
    "v_mfma_f32_16x16x32_bf16 v[180:183], a[104:107], a[200:203], v[180:183]\n"
    "ds_read_b64_tr_b16 a[126:127], v13 offset:12544\n"
    "buffer_load_dwordx4 v20, s[32:35], s64 offen lds\n"
    "v_mfma_f32_16x16x32_bf16 v[180:183], a[108:111], a[204:207], v[180:183]\n"
    "v_mfma_f32_16x16x32_bf16 v[212:215], a[108:111], a[212:215], v[212:215]\n"
    "buffer_load_dwordx4 v21, s[32:35], s65 offen offset:1024 lds\n"
    "v_mfma_f32_16x16x32_bf16 v[212:215], a[104:107], a[208:211], v[212:215]\n"
    "s_waitcnt lgkmcnt(4)\n"
    "v_mfma_f32_16x16x32_bf16 v[152:155], a[112:115], a[192:195], v[152:155]\n"
    "v_mfma_f32_16x16x32_bf16 v[152:155], a[116:119], a[196:199], v[152:155]\n"
    "s_waitcnt lgkmcnt(0)\n"
    "v_mfma_f32_16x16x32_bf16 v[184:187], a[116:119], a[204:207], v[184:187]\n"
    "s_waitcnt vmcnt(17)\n"
    "buffer_load_dwordx4 v20, s[32:35], s66 offen offset:2048 lds\n"
    "v_mfma_f32_16x16x32_bf16 v[184:187], a[112:115], a[200:203], v[184:187]\n"
    "s_barrier\n"
    "ds_read_b64_tr_b16 a[64:65], v10 offset:16384\n"
    "ds_read_b64_tr_b16 a[66:67], v10 offset:20480\n"
    "v_mfma_f32_16x16x32_bf16 v[216:219], a[112:115], a[208:211], v[216:219]\n"
    "ds_read_b64_tr_b16 a[68:69], v10 offset:24576\n"
    "ds_read_b64_tr_b16 a[70:71], v10 offset:28672\n"
    "v_mfma_f32_16x16x32_bf16 v[216:219], a[116:119], a[212:215], v[216:219]\n"
    "v_mfma_f32_16x16x32_bf16 v[156:159], a[124:127], a[196:199], v[156:159]\n"
    "ds_read_b64_tr_b16 a[72:73], v11 offset:16384\n"
    "buffer_load_dwordx4 v21, s[32:35], s67 offen offset:3072 lds\n"
    "v_mfma_f32_16x16x32_bf16 v[156:159], a[120:123], a[192:195], v[156:159]\n"
    "s_add_u32 m0, s55, 0x1c000\n"
    "ds_read_b64_tr_b16 a[74:75], v11 offset:20480\n"
    "v_mfma_f32_16x16x32_bf16 v[188:191], a[120:123], a[200:203], v[188:191]\n"
    "ds_read_b64_tr_b16 a[76:77], v11 offset:24576\n"
    "v_mfma_f32_16x16x32_bf16 v[188:191], a[124:127], a[204:207], v[188:191]\n"
    "ds_read_b64_tr_b16 a[78:79], v11 offset:28672\n"
    "buffer_load_dwordx4 v20, s[32:35], s68 offen lds\n"
    "v_mfma_f32_16x16x32_bf16 v[220:223], a[124:127], a[212:215], v[220:223]\n"
    "v_mfma_f32_16x16x32_bf16 v[220:223], a[120:123], a[208:211], v[220:223]\n"
    "s_waitcnt lgkmcnt(4)\n"
    "v_mfma_f32_16x16x32_bf16 v[128:131], a[64:67], a[160:163], v[128:131]\n"
    "v_mfma_f32_16x16x32_bf16 v[128:131], a[68:71], a[164:167], v[128:131]\n"
    "buffer_load_dwordx4 v21, s[32:35], s69 offen offset:1024 lds\n"
    "v_mfma_f32_16x16x32_bf16 v[160:163], a[68:71], a[172:175], v[160:163]\n"
    "s_add_u32 m0, s55, 0x4000\n"
    "v_mfma_f32_16x16x32_bf16 v[160:163], a[64:67], a[168:171], v[160:163]\n"
    "ds_read_b64_tr_b16 a[80:81], v10 offset:16640\n"
    "v_mfma_f32_16x16x32_bf16 v[192:195], a[64:67], a[176:179], v[192:195]\n"
    "ds_read_b64_tr_b16 a[82:83], v10 offset:20736\n"
    "buffer_load_dwordx4 v116, s[28:31], s42 offen offset:2048 nt lds\n"
    "v_mfma_f32_16x16x32_bf16 v[192:195], a[68:71], a[180:183], v[192:195]\n"
    "s_add_u32 m0, s55, 0x1c000\n"
    "ds_read_b64_tr_b16 a[84:85], v10 offset:24832\n"
    "v_mfma_f32_16x16x32_bf16 v[224:227], a[68:71], a[188:191], v[224:227]\n"
    "ds_read_b64_tr_b16 a[86:87], v10 offset:28928\n"
    "buffer_load_dwordx4 v20, s[32:35], s70 offen offset:2048 lds\n"
    "v_mfma_f32_16x16x32_bf16 v[224:227], a[64:67], a[184:187], v[224:227]\n"
    "s_waitcnt lgkmcnt(4)\n"
    "v_mfma_f32_16x16x32_bf16 v[132:135], a[72:75], a[160:163], v[132:135]\n"
    "buffer_load_dwordx4 v21, s[32:35], s71 offen offset:3072 lds\n"
    "v_mfma_f32_16x16x32_bf16 v[132:135], a[76:79], a[164:167], v[132:135]\n"
    "v_add_u32_e32 v23, s52, v22\n"
    "v_mfma_f32_16x16x32_bf16 v[164:167], a[76:79], a[172:175], v[164:167]\n"
    "v_xor_b32_e32 v56, 32, v23\n"
    "v_mfma_f32_16x16x32_bf16 v[164:167], a[72:75], a[168:171], v[164:167]\n"
    "s_add_u32 m0, s55, 0x20000\n"
    "ds_read_b64_tr_b16 a[88:89], v11 offset:16640\n"
    "v_mfma_f32_16x16x32_bf16 v[196:199], a[72:75], a[176:179], v[196:199]\n"
    "ds_read_b64_tr_b16 a[90:91], v11 offset:20736\n"
    "buffer_load_dwordx4 v23, s[36:39], s72 offen lds\n"
    "v_mfma_f32_16x16x32_bf16 v[196:199], a[76:79], a[180:183], v[196:199]\n"
    "ds_read_b64_tr_b16 a[92:93], v11 offset:24832\n"
    "v_mfma_f32_16x16x32_bf16 v[228:231], a[76:79], a[188:191], v[228:231]\n"
    "ds_read_b64_tr_b16 a[94:95], v11 offset:28928\n"
    "v_mfma_f32_16x16x32_bf16 v[228:231], a[72:75], a[184:187], v[228:231]\n"
    "s_waitcnt lgkmcnt(4)\n"
    "v_mfma_f32_16x16x32_bf16 v[136:139], a[80:83], a[160:163], v[136:139]\n"
    "buffer_load_dwordx4 v56, s[36:39], s73 offen offset:1024 lds\n"
    "v_mfma_f32_16x16x32_bf16 v[136:139], a[84:87], a[164:167], v[136:139]\n"
    "v_mfma_f32_16x16x32_bf16 v[168:171], a[84:87], a[172:175], v[168:171]\n"
    "v_mfma_f32_16x16x32_bf16 v[168:171], a[80:83], a[168:171], v[168:171]\n"
    "ds_read_b64_tr_b16 a[96:97], v12 offset:16384\n"
    "v_mfma_f32_16x16x32_bf16 v[200:203], a[80:83], a[176:179], v[200:203]\n"
    "s_waitcnt vmcnt(16)\n"
    "ds_read_b64_tr_b16 a[98:99], v12 offset:20480\n"
    "buffer_load_dwordx4 v23, s[36:39], s74 offen offset:2048 lds\n"
    "v_mfma_f32_16x16x32_bf16 v[200:203], a[84:87], a[180:183], v[200:203]\n"
    "ds_read_b64_tr_b16 a[100:101], v12 offset:24576\n"
    "ds_read_b128 a[192:195], v8\n"
    "v_mfma_f32_16x16x32_bf16 v[232:235], a[84:87], a[188:191], v[232:235]\n"
    "ds_read_b64_tr_b16 a[102:103], v12 offset:28672\n"
    "ds_read_b128 a[196:199], v9\n"
    "v_mfma_f32_16x16x32_bf16 v[232:235], a[80:83], a[184:187], v[232:235]\n"
    "s_waitcnt lgkmcnt(6)\n"
    "ds_read_b128 a[200:203], v8 offset:2048\n"
    "buffer_load_dwordx4 v56, s[36:39], s75 offen offset:3072 lds\n"
    "v_mfma_f32_16x16x32_bf16 v[140:143], a[88:91], a[160:163], v[140:143]\n"
    "s_add_u32 m0, s55, 0x4000\n"
    "ds_read_b128 a[204:207], v9 offset:2048\n"
    "v_mfma_f32_16x16x32_bf16 v[140:143], a[92:95], a[164:167], v[140:143]\n"
    "ds_read_b64_tr_b16 a[104:105], v13 offset:16384\n"
    "ds_read_b128 a[208:211], v8 offset:16384\n"
    "v_mfma_f32_16x16x32_bf16 v[172:175], a[92:95], a[172:175], v[172:175]\n"
    "ds_read_b64_tr_b16 a[106:107], v13 offset:20480\n"
    "ds_read_b128 a[212:215], v9 offset:16384\n"
    "v_mfma_f32_16x16x32_bf16 v[172:175], a[88:91], a[168:171], v[172:175]\n"
    "ds_read_b64_tr_b16 a[108:109], v13 offset:24576\n"
    "ds_read_b128 a[216:219], v8 offset:18432\n"
    "v_mfma_f32_16x16x32_bf16 v[204:207], a[88:91], a[176:179], v[204:207]\n"
    "ds_read_b64_tr_b16 a[110:111], v13 offset:28672\n"
    "ds_read_b128 a[220:223], v9 offset:18432\n"
    "v_mfma_f32_16x16x32_bf16 v[204:207], a[92:95], a[180:183], v[204:207]\n"
    "v_mfma_f32_16x16x32_bf16 v[236:239], a[92:95], a[188:191], v[236:239]\n"
    "v_mfma_f32_16x16x32_bf16 v[236:239], a[88:91], a[184:187], v[236:239]\n"
    "s_waitcnt lgkmcnt(11)\n"
    "ds_read_b64_tr_b16 a[112:113], v12 offset:16640\n"
    "v_mfma_f32_16x16x32_bf16 v[144:147], a[96:99], a[160:163], v[144:147]\n"
    "ds_read_b64_tr_b16 a[114:115], v12 offset:20736\n"
    "buffer_load_dwordx4 v118, s[28:31], s42 offen offset:3072 nt lds\n"
    "v_mfma_f32_16x16x32_bf16 v[144:147], a[100:103], a[164:167], v[144:147]\n"
    "s_add_u32 s45, s53, 7\n"
    "ds_read_b64_tr_b16 a[116:117], v12 offset:24832\n"
    "v_mfma_f32_16x16x32_bf16 v[176:179], a[100:103], a[172:175], v[176:179]\n"
    "s_and_b32 s45, s45, 7\n"
    "ds_read_b64_tr_b16 a[118:119], v12 offset:28928\n"
    "v_mfma_f32_16x16x32_bf16 v[176:179], a[96:99], a[168:171], v[176:179]\n"
    "s_lshl_b32 s51, s45, 7\n"
    "v_mfma_f32_16x16x32_bf16 v[208:211], a[96:99], a[176:179], v[208:211]\n"
    "s_lshl_b32 s52, s45, 19\n"
    "v_mfma_f32_16x16x32_bf16 v[208:211], a[100:103], a[180:183], v[208:211]\n"
    "s_waitcnt lgkmcnt(4)\n"
    "ds_read_b64_tr_b16 a[120:121], v13 offset:16640\n"
    "v_add_u32_e32 v20, s51, v18\n"
    "v_mfma_f32_16x16x32_bf16 v[148:151], a[108:111], a[164:167], v[148:151]\n"
    "ds_read_b64_tr_b16 a[122:123], v13 offset:20736\n"
    "v_add_u32_e32 v21, s51, v19\n"
    "v_mfma_f32_16x16x32_bf16 v[148:151], a[104:107], a[160:163], v[148:151]\n"
    "s_add_u32 m0, s55, 0x10000\n"
    "ds_read_b64_tr_b16 a[124:125], v13 offset:24832\n"
    "v_mfma_f32_16x16x32_bf16 v[180:183], a[104:107], a[168:171], v[180:183]\n"
    "ds_read_b64_tr_b16 a[126:127], v13 offset:28928\n"
    "buffer_load_dwordx4 v20, s[32:35], s64 offen lds\n"
    "v_mfma_f32_16x16x32_bf16 v[180:183], a[108:111], a[172:175], v[180:183]\n"
    "v_mfma_f32_16x16x32_bf16 v[212:215], a[108:111], a[180:183], v[212:215]\n"
    "buffer_load_dwordx4 v21, s[32:35], s65 offen offset:1024 lds\n"
    "v_mfma_f32_16x16x32_bf16 v[212:215], a[104:107], a[176:179], v[212:215]\n"
    "s_waitcnt lgkmcnt(4)\n"
    "v_mfma_f32_16x16x32_bf16 v[152:155], a[112:115], a[160:163], v[152:155]\n"
    "v_mfma_f32_16x16x32_bf16 v[152:155], a[116:119], a[164:167], v[152:155]\n"
    "s_waitcnt lgkmcnt(0)\n"
    "v_mfma_f32_16x16x32_bf16 v[184:187], a[116:119], a[172:175], v[184:187]\n"
    "s_waitcnt vmcnt(17)\n"
    "buffer_load_dwordx4 v20, s[32:35], s66 offen offset:2048 lds\n"
    "v_mfma_f32_16x16x32_bf16 v[184:187], a[112:115], a[168:171], v[184:187]\n"
    "s_barrier\n"
    "ds_read_b64_tr_b16 a[64:65], v14\n"
    "ds_read_b64_tr_b16 a[66:67], v14 offset:4096\n"
    "v_mfma_f32_16x16x32_bf16 v[216:219], a[112:115], a[176:179], v[216:219]\n"
    "ds_read_b64_tr_b16 a[68:69], v14 offset:8192\n"
    "ds_read_b64_tr_b16 a[70:71], v14 offset:12288\n"
    "v_mfma_f32_16x16x32_bf16 v[216:219], a[116:119], a[180:183], v[216:219]\n"
    "v_mfma_f32_16x16x32_bf16 v[156:159], a[124:127], a[164:167], v[156:159]\n"
    "ds_read_b64_tr_b16 a[72:73], v15\n"
    "buffer_load_dwordx4 v21, s[32:35], s67 offen offset:3072 lds\n"
    "v_mfma_f32_16x16x32_bf16 v[156:159], a[120:123], a[160:163], v[156:159]\n"
    "s_add_u32 m0, s55, 0x14000\n"
    "ds_read_b64_tr_b16 a[74:75], v15 offset:4096\n"
    "v_mfma_f32_16x16x32_bf16 v[188:191], a[120:123], a[168:171], v[188:191]\n"
    "ds_read_b64_tr_b16 a[76:77], v15 offset:8192\n"
    "v_mfma_f32_16x16x32_bf16 v[188:191], a[124:127], a[172:175], v[188:191]\n"
    "ds_read_b64_tr_b16 a[78:79], v15 offset:12288\n"
    "buffer_load_dwordx4 v20, s[32:35], s68 offen lds\n"
    "v_mfma_f32_16x16x32_bf16 v[220:223], a[124:127], a[180:183], v[220:223]\n"
    "v_mfma_f32_16x16x32_bf16 v[220:223], a[120:123], a[176:179], v[220:223]\n"
    "s_waitcnt lgkmcnt(4)\n"
    "v_mfma_f32_16x16x32_bf16 v[128:131], a[64:67], a[192:195], v[128:131]\n"
    "v_mfma_f32_16x16x32_bf16 v[128:131], a[68:71], a[196:199], v[128:131]\n"
    "buffer_load_dwordx4 v21, s[32:35], s69 offen offset:1024 lds\n"
    "v_mfma_f32_16x16x32_bf16 v[160:163], a[68:71], a[204:207], v[160:163]\n"
    "s_add_u32 s42, s78, 0x4000\n"
    "v_mfma_f32_16x16x32_bf16 v[160:163], a[64:67], a[200:203], v[160:163]\n"
    "s_and_b32 s42, s42, 0x3ffff\n"
    "ds_read_b64_tr_b16 a[80:81], v14 offset:256\n"
    "v_mfma_f32_16x16x32_bf16 v[192:195], a[64:67], a[208:211], v[192:195]\n"
    "s_add_u32 m0, s55, 0x8000\n"
    "ds_read_b64_tr_b16 a[82:83], v14 offset:4352\n"
    "v_mfma_f32_16x16x32_bf16 v[192:195], a[68:71], a[212:215], v[192:195]\n"
    "ds_read_b64_tr_b16 a[84:85], v14 offset:8448\n"
    "buffer_load_dwordx4 v116, s[24:27], s42 offen nt lds\n"
    "v_mfma_f32_16x16x32_bf16 v[224:227], a[68:71], a[220:223], v[224:227]\n"
    "s_add_u32 m0, s55, 0x14000\n"
    "ds_read_b64_tr_b16 a[86:87], v14 offset:12544\n"
    "v_mfma_f32_16x16x32_bf16 v[224:227], a[64:67], a[216:219], v[224:227]\n"
    "s_waitcnt lgkmcnt(4)\n"
    "buffer_load_dwordx4 v20, s[32:35], s70 offen offset:2048 lds\n"
    "v_mfma_f32_16x16x32_bf16 v[132:135], a[72:75], a[192:195], v[132:135]\n"
    "buffer_load_dwordx4 v21, s[32:35], s71 offen offset:3072 lds\n"
    "v_mfma_f32_16x16x32_bf16 v[132:135], a[76:79], a[196:199], v[132:135]\n"
    "v_add_u32_e32 v23, s52, v22\n"
    "v_mfma_f32_16x16x32_bf16 v[164:167], a[76:79], a[204:207], v[164:167]\n"
    "v_xor_b32_e32 v56, 32, v23\n"
    "v_mfma_f32_16x16x32_bf16 v[164:167], a[72:75], a[200:203], v[164:167]\n"
    "s_add_u32 m0, s55, 0x24000\n"
    "ds_read_b64_tr_b16 a[88:89], v15 offset:256\n"
    "v_mfma_f32_16x16x32_bf16 v[196:199], a[72:75], a[208:211], v[196:199]\n"
    "ds_read_b64_tr_b16 a[90:91], v15 offset:4352\n"
    "buffer_load_dwordx4 v23, s[36:39], s72 offen lds\n"
    "v_mfma_f32_16x16x32_bf16 v[196:199], a[76:79], a[212:215], v[196:199]\n"
    "ds_read_b64_tr_b16 a[92:93], v15 offset:8448\n"
    "v_mfma_f32_16x16x32_bf16 v[228:231], a[76:79], a[220:223], v[228:231]\n"
    "ds_read_b64_tr_b16 a[94:95], v15 offset:12544\n"
    "v_mfma_f32_16x16x32_bf16 v[228:231], a[72:75], a[216:219], v[228:231]\n"
    "s_waitcnt lgkmcnt(4)\n"
    "v_mfma_f32_16x16x32_bf16 v[136:139], a[80:83], a[192:195], v[136:139]\n"
    "buffer_load_dwordx4 v56, s[36:39], s73 offen offset:1024 lds\n"
    "v_mfma_f32_16x16x32_bf16 v[136:139], a[84:87], a[196:199], v[136:139]\n"
    "v_mfma_f32_16x16x32_bf16 v[168:171], a[84:87], a[204:207], v[168:171]\n"
    "v_mfma_f32_16x16x32_bf16 v[168:171], a[80:83], a[200:203], v[168:171]\n"
    "ds_read_b64_tr_b16 a[96:97], v16\n"
    "v_mfma_f32_16x16x32_bf16 v[200:203], a[80:83], a[208:211], v[200:203]\n"
    "s_waitcnt vmcnt(16)\n"
    "ds_read_b64_tr_b16 a[98:99], v16 offset:4096\n"
    "buffer_load_dwordx4 v23, s[36:39], s74 offen offset:2048 lds\n"
    "v_mfma_f32_16x16x32_bf16 v[200:203], a[84:87], a[212:215], v[200:203]\n"
    "ds_read_b64_tr_b16 a[100:101], v16 offset:8192\n"
    "ds_read_b128 a[160:163], v8 offset:32768\n"
    "v_mfma_f32_16x16x32_bf16 v[232:235], a[84:87], a[220:223], v[232:235]\n"
    "ds_read_b64_tr_b16 a[102:103], v16 offset:12288\n"
    "ds_read_b128 a[164:167], v9 offset:32768\n"
    "v_mfma_f32_16x16x32_bf16 v[232:235], a[80:83], a[216:219], v[232:235]\n"
    "s_waitcnt lgkmcnt(6)\n"
    "ds_read_b128 a[168:171], v8 offset:34816\n"
    "buffer_load_dwordx4 v56, s[36:39], s75 offen offset:3072 lds\n"
    "v_mfma_f32_16x16x32_bf16 v[140:143], a[88:91], a[192:195], v[140:143]\n"
    "s_add_u32 m0, s55, 0x8000\n"
    "ds_read_b128 a[172:175], v9 offset:34816\n"
    "v_mfma_f32_16x16x32_bf16 v[140:143], a[92:95], a[196:199], v[140:143]\n"
    "ds_read_b64_tr_b16 a[104:105], v17\n"
    "ds_read_b128 a[176:179], v8 offset:49152\n"
    "v_mfma_f32_16x16x32_bf16 v[172:175], a[92:95], a[204:207], v[172:175]\n"
    "ds_read_b64_tr_b16 a[106:107], v17 offset:4096\n"
    "ds_read_b128 a[180:183], v9 offset:49152\n"
    "v_mfma_f32_16x16x32_bf16 v[172:175], a[88:91], a[200:203], v[172:175]\n"
    "ds_read_b64_tr_b16 a[108:109], v17 offset:8192\n"
    "ds_read_b128 a[184:187], v8 offset:51200\n"
    "v_mfma_f32_16x16x32_bf16 v[204:207], a[88:91], a[208:211], v[204:207]\n"
    "ds_read_b64_tr_b16 a[110:111], v17 offset:12288\n"
    "ds_read_b128 a[188:191], v9 offset:51200\n"
    "v_mfma_f32_16x16x32_bf16 v[204:207], a[92:95], a[212:215], v[204:207]\n"
    "v_mfma_f32_16x16x32_bf16 v[236:239], a[92:95], a[220:223], v[236:239]\n"
    "v_mfma_f32_16x16x32_bf16 v[236:239], a[88:91], a[216:219], v[236:239]\n"
    "s_waitcnt lgkmcnt(11)\n"
    "ds_read_b64_tr_b16 a[112:113], v16 offset:256\n"
    "v_mfma_f32_16x16x32_bf16 v[144:147], a[96:99], a[192:195], v[144:147]\n"
    "ds_read_b64_tr_b16 a[114:115], v16 offset:4352\n"
    "buffer_load_dwordx4 v117, s[24:27], s42 offen offset:1024 nt lds\n"
    "v_mfma_f32_16x16x32_bf16 v[144:147], a[100:103], a[196:199], v[144:147]\n"
    "ds_read_b64_tr_b16 a[116:117], v16 offset:8448\n"
    "v_mfma_f32_16x16x32_bf16 v[176:179], a[100:103], a[204:207], v[176:179]\n"
    "ds_read_b64_tr_b16 a[118:119], v16 offset:12544\n"
    "v_mfma_f32_16x16x32_bf16 v[176:179], a[96:99], a[200:203], v[176:179]\n"
    "v_mfma_f32_16x16x32_bf16 v[208:211], a[96:99], a[208:211], v[208:211]\n"
    "v_mfma_f32_16x16x32_bf16 v[208:211], a[100:103], a[212:215], v[208:211]\n"
    "s_waitcnt lgkmcnt(4)\n"
    "ds_read_b64_tr_b16 a[120:121], v17 offset:256\n"
    "v_mfma_f32_16x16x32_bf16 v[148:151], a[108:111], a[196:199], v[148:151]\n"
    "ds_read_b64_tr_b16 a[122:123], v17 offset:4352\n"
    "v_mfma_f32_16x16x32_bf16 v[148:151], a[104:107], a[192:195], v[148:151]\n"
    "ds_read_b64_tr_b16 a[124:125], v17 offset:8448\n"
    "v_mfma_f32_16x16x32_bf16 v[180:183], a[104:107], a[200:203], v[180:183]\n"
    "ds_read_b64_tr_b16 a[126:127], v17 offset:12544\n"
    "v_mfma_f32_16x16x32_bf16 v[180:183], a[108:111], a[204:207], v[180:183]\n"
    "v_mfma_f32_16x16x32_bf16 v[212:215], a[108:111], a[212:215], v[212:215]\n"
    "v_mfma_f32_16x16x32_bf16 v[212:215], a[104:107], a[208:211], v[212:215]\n"
    "s_waitcnt lgkmcnt(4)\n"
    "v_mfma_f32_16x16x32_bf16 v[152:155], a[112:115], a[192:195], v[152:155]\n"
    "v_mfma_f32_16x16x32_bf16 v[152:155], a[116:119], a[196:199], v[152:155]\n"
    "s_waitcnt lgkmcnt(0)\n"
    "v_mfma_f32_16x16x32_bf16 v[184:187], a[116:119], a[204:207], v[184:187]\n"
    "s_waitcnt vmcnt(15)\n"
    "v_mfma_f32_16x16x32_bf16 v[184:187], a[112:115], a[200:203], v[184:187]\n"
    "s_barrier\n"
    "ds_read_b64_tr_b16 a[64:65], v10\n"
    "ds_read_b64_tr_b16 a[66:67], v10 offset:4096\n"
    "v_mfma_f32_16x16x32_bf16 v[216:219], a[112:115], a[208:211], v[216:219]\n"
    "ds_read_b64_tr_b16 a[68:69], v10 offset:8192\n"
    "ds_read_b64_tr_b16 a[70:71], v10 offset:12288\n"
    "v_mfma_f32_16x16x32_bf16 v[216:219], a[116:119], a[212:215], v[216:219]\n"
    "buffer_load_dwordx4 v118, s[24:27], s42 offen offset:2048 nt lds\n"
    "v_mfma_f32_16x16x32_bf16 v[156:159], a[124:127], a[196:199], v[156:159]\n"
    "ds_read_b64_tr_b16 a[72:73], v11\n"
    "v_mfma_f32_16x16x32_bf16 v[156:159], a[120:123], a[192:195], v[156:159]\n"
    "ds_read_b64_tr_b16 a[74:75], v11 offset:4096\n"
    "v_mfma_f32_16x16x32_bf16 v[188:191], a[120:123], a[200:203], v[188:191]\n"
    "ds_read_b64_tr_b16 a[76:77], v11 offset:8192\n"
    "v_mfma_f32_16x16x32_bf16 v[188:191], a[124:127], a[204:207], v[188:191]\n"
    "ds_read_b64_tr_b16 a[78:79], v11 offset:12288\n"
    "v_mfma_f32_16x16x32_bf16 v[220:223], a[124:127], a[212:215], v[220:223]\n"
    "v_mfma_f32_16x16x32_bf16 v[220:223], a[120:123], a[208:211], v[220:223]\n"
    "s_waitcnt lgkmcnt(4)\n"
    "v_mfma_f32_16x16x32_bf16 v[128:131], a[64:67], a[160:163], v[128:131]\n"
    "v_mfma_f32_16x16x32_bf16 v[128:131], a[68:71], a[164:167], v[128:131]\n"
    "v_mfma_f32_16x16x32_bf16 v[160:163], a[68:71], a[172:175], v[160:163]\n"
    "v_mfma_f32_16x16x32_bf16 v[160:163], a[64:67], a[168:171], v[160:163]\n"
    "ds_read_b64_tr_b16 a[80:81], v10 offset:256\n"
    "v_mfma_f32_16x16x32_bf16 v[192:195], a[64:67], a[176:179], v[192:195]\n"
    "ds_read_b64_tr_b16 a[82:83], v10 offset:4352\n"
    "v_mfma_f32_16x16x32_bf16 v[192:195], a[68:71], a[180:183], v[192:195]\n"
    "ds_read_b64_tr_b16 a[84:85], v10 offset:8448\n"
    "v_mfma_f32_16x16x32_bf16 v[224:227], a[68:71], a[188:191], v[224:227]\n"
    "ds_read_b64_tr_b16 a[86:87], v10 offset:12544\n"
    "v_mfma_f32_16x16x32_bf16 v[224:227], a[64:67], a[184:187], v[224:227]\n"
    "s_waitcnt lgkmcnt(4)\n"
    "v_mfma_f32_16x16x32_bf16 v[132:135], a[72:75], a[160:163], v[132:135]\n"
    "v_mfma_f32_16x16x32_bf16 v[132:135], a[76:79], a[164:167], v[132:135]\n"
    "v_mfma_f32_16x16x32_bf16 v[164:167], a[76:79], a[172:175], v[164:167]\n"
    "buffer_load_dwordx4 v119, s[24:27], s42 offen offset:3072 nt lds\n"
    "v_mfma_f32_16x16x32_bf16 v[164:167], a[72:75], a[168:171], v[164:167]\n"
    "ds_read_b64_tr_b16 a[88:89], v11 offset:256\n"
    "v_mfma_f32_16x16x32_bf16 v[196:199], a[72:75], a[176:179], v[196:199]\n"
    "ds_read_b64_tr_b16 a[90:91], v11 offset:4352\n"
    "v_mfma_f32_16x16x32_bf16 v[196:199], a[76:79], a[180:183], v[196:199]\n"
    "ds_read_b64_tr_b16 a[92:93], v11 offset:8448\n"
    "v_mfma_f32_16x16x32_bf16 v[228:231], a[76:79], a[188:191], v[228:231]\n"
    "ds_read_b64_tr_b16 a[94:95], v11 offset:12544\n"
    "v_mfma_f32_16x16x32_bf16 v[228:231], a[72:75], a[184:187], v[228:231]\n"
    "s_waitcnt lgkmcnt(4)\n"
    "v_mfma_f32_16x16x32_bf16 v[136:139], a[80:83], a[160:163], v[136:139]\n"
    "v_mfma_f32_16x16x32_bf16 v[136:139], a[84:87], a[164:167], v[136:139]\n"
    "s_add_u32 s42, s78, 0x4000\n"
    "v_mfma_f32_16x16x32_bf16 v[168:171], a[84:87], a[172:175], v[168:171]\n"
    "s_and_b32 s42, s42, 0x3ffff\n"
    "v_mfma_f32_16x16x32_bf16 v[168:171], a[80:83], a[168:171], v[168:171]\n"
    "s_add_u32 m0, s55, 0xc000\n"
    "ds_read_b64_tr_b16 a[96:97], v12\n"
    "v_mfma_f32_16x16x32_bf16 v[200:203], a[80:83], a[176:179], v[200:203]\n"
    "s_waitcnt vmcnt(7)\n"
    "ds_read_b64_tr_b16 a[98:99], v12 offset:4096\n"
    "v_mfma_f32_16x16x32_bf16 v[200:203], a[84:87], a[180:183], v[200:203]\n"
    "ds_read_b64_tr_b16 a[100:101], v12 offset:8192\n"
    "ds_read_b128 a[192:195], v8\n"
    "v_mfma_f32_16x16x32_bf16 v[232:235], a[84:87], a[188:191], v[232:235]\n"
    "ds_read_b64_tr_b16 a[102:103], v12 offset:12288\n"
    "ds_read_b128 a[196:199], v9\n"
    "v_mfma_f32_16x16x32_bf16 v[232:235], a[80:83], a[184:187], v[232:235]\n"
    "s_waitcnt lgkmcnt(6)\n"
    "ds_read_b128 a[200:203], v8 offset:2048\n"
    "v_mfma_f32_16x16x32_bf16 v[140:143], a[88:91], a[160:163], v[140:143]\n"
    "ds_read_b128 a[204:207], v9 offset:2048\n"
    "v_mfma_f32_16x16x32_bf16 v[140:143], a[92:95], a[164:167], v[140:143]\n"
    "ds_read_b64_tr_b16 a[104:105], v13\n"
    "ds_read_b128 a[208:211], v8 offset:16384\n"
    "v_mfma_f32_16x16x32_bf16 v[172:175], a[92:95], a[172:175], v[172:175]\n"
    "ds_read_b64_tr_b16 a[106:107], v13 offset:4096\n"
    "ds_read_b128 a[212:215], v9 offset:16384\n"
    "v_mfma_f32_16x16x32_bf16 v[172:175], a[88:91], a[168:171], v[172:175]\n"
    "ds_read_b64_tr_b16 a[108:109], v13 offset:8192\n"
    "ds_read_b128 a[216:219], v8 offset:18432\n"
    "v_mfma_f32_16x16x32_bf16 v[204:207], a[88:91], a[176:179], v[204:207]\n"
    "ds_read_b64_tr_b16 a[110:111], v13 offset:12288\n"
    "ds_read_b128 a[220:223], v9 offset:18432\n"
    "v_mfma_f32_16x16x32_bf16 v[204:207], a[92:95], a[180:183], v[204:207]\n"
    "buffer_load_dwordx4 v116, s[28:31], s42 offen nt lds\n"
    "v_mfma_f32_16x16x32_bf16 v[236:239], a[92:95], a[188:191], v[236:239]\n"
    "v_mfma_f32_16x16x32_bf16 v[236:239], a[88:91], a[184:187], v[236:239]\n"
    "s_waitcnt lgkmcnt(11)\n"
    "ds_read_b64_tr_b16 a[112:113], v12 offset:256\n"
    "v_mfma_f32_16x16x32_bf16 v[144:147], a[96:99], a[160:163], v[144:147]\n"
    "ds_read_b64_tr_b16 a[114:115], v12 offset:4352\n"
    "v_mfma_f32_16x16x32_bf16 v[144:147], a[100:103], a[164:167], v[144:147]\n"
    "ds_read_b64_tr_b16 a[116:117], v12 offset:8448\n"
    "v_mfma_f32_16x16x32_bf16 v[176:179], a[100:103], a[172:175], v[176:179]\n"
    "ds_read_b64_tr_b16 a[118:119], v12 offset:12544\n"
    "v_mfma_f32_16x16x32_bf16 v[176:179], a[96:99], a[168:171], v[176:179]\n"
    "v_mfma_f32_16x16x32_bf16 v[208:211], a[96:99], a[176:179], v[208:211]\n"
    "v_mfma_f32_16x16x32_bf16 v[208:211], a[100:103], a[180:183], v[208:211]\n"
    "s_waitcnt lgkmcnt(4)\n"
    "ds_read_b64_tr_b16 a[120:121], v13 offset:256\n"
    "v_mfma_f32_16x16x32_bf16 v[148:151], a[108:111], a[164:167], v[148:151]\n"
    "ds_read_b64_tr_b16 a[122:123], v13 offset:4352\n"
    "v_mfma_f32_16x16x32_bf16 v[148:151], a[104:107], a[160:163], v[148:151]\n"
    "ds_read_b64_tr_b16 a[124:125], v13 offset:8448\n"
    "v_mfma_f32_16x16x32_bf16 v[180:183], a[104:107], a[168:171], v[180:183]\n"
    "ds_read_b64_tr_b16 a[126:127], v13 offset:12544\n"
    "v_mfma_f32_16x16x32_bf16 v[180:183], a[108:111], a[172:175], v[180:183]\n"
    "v_mfma_f32_16x16x32_bf16 v[212:215], a[108:111], a[180:183], v[212:215]\n"
    "v_mfma_f32_16x16x32_bf16 v[212:215], a[104:107], a[176:179], v[212:215]\n"
    "s_waitcnt lgkmcnt(4)\n"
    "v_mfma_f32_16x16x32_bf16 v[152:155], a[112:115], a[160:163], v[152:155]\n"
    "buffer_load_dwordx4 v118, s[28:31], s42 offen offset:1024 nt lds\n"
    "v_mfma_f32_16x16x32_bf16 v[152:155], a[116:119], a[164:167], v[152:155]\n"
    "s_waitcnt lgkmcnt(0)\n"
    "v_mfma_f32_16x16x32_bf16 v[184:187], a[116:119], a[172:175], v[184:187]\n"
    "s_waitcnt vmcnt(5)\n"
    "v_mfma_f32_16x16x32_bf16 v[184:187], a[112:115], a[168:171], v[184:187]\n"
    "s_barrier\n"
    "ds_read_b64_tr_b16 a[64:65], v10 offset:16384\n"
    "ds_read_b64_tr_b16 a[66:67], v10 offset:20480\n"
    "v_mfma_f32_16x16x32_bf16 v[216:219], a[112:115], a[176:179], v[216:219]\n"
    "ds_read_b64_tr_b16 a[68:69], v10 offset:24576\n"
    "ds_read_b64_tr_b16 a[70:71], v10 offset:28672\n"
    "v_mfma_f32_16x16x32_bf16 v[216:219], a[116:119], a[180:183], v[216:219]\n"
    "ds_read_b64_tr_b16 a[72:73], v11 offset:16384\n"
    "ds_read_b64_tr_b16 a[74:75], v11 offset:20480\n"
    "v_mfma_f32_16x16x32_bf16 v[156:159], a[124:127], a[164:167], v[156:159]\n"
    "ds_read_b64_tr_b16 a[76:77], v11 offset:24576\n"
    "ds_read_b64_tr_b16 a[78:79], v11 offset:28672\n"
    "v_mfma_f32_16x16x32_bf16 v[156:159], a[120:123], a[160:163], v[156:159]\n"
    "ds_read_b64_tr_b16 a[80:81], v10 offset:16640\n"
    "ds_read_b64_tr_b16 a[82:83], v10 offset:20736\n"
    "v_mfma_f32_16x16x32_bf16 v[188:191], a[120:123], a[168:171], v[188:191]\n"
    "ds_read_b64_tr_b16 a[84:85], v10 offset:24832\n"
    "ds_read_b64_tr_b16 a[86:87], v10 offset:28928\n"
    "v_mfma_f32_16x16x32_bf16 v[188:191], a[124:127], a[172:175], v[188:191]\n"
    "ds_read_b64_tr_b16 a[88:89], v11 offset:16640\n"
    "ds_read_b64_tr_b16 a[90:91], v11 offset:20736\n"
    "v_mfma_f32_16x16x32_bf16 v[220:223], a[124:127], a[180:183], v[220:223]\n"
    "ds_read_b64_tr_b16 a[92:93], v11 offset:24832\n"
    "ds_read_b64_tr_b16 a[94:95], v11 offset:28928\n"
    "v_mfma_f32_16x16x32_bf16 v[220:223], a[120:123], a[176:179], v[220:223]\n"
    "s_waitcnt lgkmcnt(8)\n"
    "ds_read_b64_tr_b16 a[96:97], v12 offset:16384\n"
    "ds_read_b64_tr_b16 a[98:99], v12 offset:20480\n"
    "v_mfma_f32_16x16x32_bf16 v[224:227], a[64:67], a[216:219], v[224:227]\n"
    "ds_read_b64_tr_b16 a[100:101], v12 offset:24576\n"
    "ds_read_b64_tr_b16 a[102:103], v12 offset:28672\n"
    "v_mfma_f32_16x16x32_bf16 v[224:227], a[68:71], a[220:223], v[224:227]\n"
    "ds_read_b64_tr_b16 a[104:105], v13 offset:16384\n"
    "ds_read_b64_tr_b16 a[106:107], v13 offset:20480\n"
    "v_mfma_f32_16x16x32_bf16 v[228:231], a[76:79], a[220:223], v[228:231]\n"
    "ds_read_b64_tr_b16 a[108:109], v13 offset:24576\n"
    "ds_read_b64_tr_b16 a[110:111], v13 offset:28672\n"
    "v_mfma_f32_16x16x32_bf16 v[228:231], a[72:75], a[216:219], v[228:231]\n"
    "s_waitcnt lgkmcnt(8)\n"
    "ds_read_b64_tr_b16 a[112:113], v12 offset:16640\n"
    "ds_read_b64_tr_b16 a[114:115], v12 offset:20736\n"
    "v_mfma_f32_16x16x32_bf16 v[232:235], a[80:83], a[216:219], v[232:235]\n"
    "ds_read_b64_tr_b16 a[116:117], v12 offset:24832\n"
    "ds_read_b64_tr_b16 a[118:119], v12 offset:28928\n"
    "v_mfma_f32_16x16x32_bf16 v[232:235], a[84:87], a[220:223], v[232:235]\n"
    "ds_read_b64_tr_b16 a[120:121], v13 offset:16640\n"
    "ds_read_b64_tr_b16 a[122:123], v13 offset:20736\n"
    "v_mfma_f32_16x16x32_bf16 v[236:239], a[92:95], a[220:223], v[236:239]\n"
    "ds_read_b64_tr_b16 a[124:125], v13 offset:24832\n"
    "ds_read_b64_tr_b16 a[126:127], v13 offset:28928\n"
    "v_mfma_f32_16x16x32_bf16 v[236:239], a[88:91], a[216:219], v[236:239]\n"
    "buffer_load_dwordx4 v116, s[28:31], s42 offen offset:2048 nt lds\n"
    "v_mul_f32_e32 v224, s54, v224\n"
    "v_mfma_f32_16x16x32_bf16 v[128:131], a[64:67], a[192:195], v[128:131]\n"
    "v_mul_f32_e32 v225, s54, v225\n"
    "v_mul_f32_e32 v226, s54, v226\n"
    "v_mfma_f32_16x16x32_bf16 v[128:131], a[68:71], a[196:199], v[128:131]\n"
    "v_mul_f32_e32 v227, s54, v227\n"
    "v_cvt_pk_bf16_f32 v40, v224, v225\n"
    "v_mfma_f32_16x16x32_bf16 v[132:135], a[76:79], a[196:199], v[132:135]\n"
    "v_cvt_pk_bf16_f32 v41, v226, v227\n"
    "v_mfma_f32_16x16x32_bf16 v[132:135], a[72:75], a[192:195], v[132:135]\n"
    "ds_write_b64 v28, v[40:41]\n"
    "v_mul_f32_e32 v228, s54, v228\n"
    "v_mfma_f32_16x16x32_bf16 v[136:139], a[80:83], a[192:195], v[136:139]\n"
    "v_mul_f32_e32 v229, s54, v229\n"
    "v_mul_f32_e32 v230, s54, v230\n"
    "v_mfma_f32_16x16x32_bf16 v[136:139], a[84:87], a[196:199], v[136:139]\n"
    "v_mul_f32_e32 v231, s54, v231\n"
    "v_cvt_pk_bf16_f32 v42, v228, v229\n"
    "v_mfma_f32_16x16x32_bf16 v[140:143], a[92:95], a[196:199], v[140:143]\n"
    "v_cvt_pk_bf16_f32 v43, v230, v231\n"
    "v_mfma_f32_16x16x32_bf16 v[140:143], a[88:91], a[192:195], v[140:143]\n"
    "s_waitcnt lgkmcnt(9)\n"
    "ds_write_b64 v29, v[42:43]\n"
    "v_mul_f32_e32 v232, s54, v232\n"
    "v_mfma_f32_16x16x32_bf16 v[144:147], a[96:99], a[192:195], v[144:147]\n"
    "v_mul_f32_e32 v233, s54, v233\n"
    "v_mul_f32_e32 v234, s54, v234\n"
    "v_mfma_f32_16x16x32_bf16 v[144:147], a[100:103], a[196:199], v[144:147]\n"
    "v_mul_f32_e32 v235, s54, v235\n"
    "v_cvt_pk_bf16_f32 v44, v232, v233\n"
    "v_mfma_f32_16x16x32_bf16 v[148:151], a[108:111], a[196:199], v[148:151]\n"
    "v_cvt_pk_bf16_f32 v45, v234, v235\n"
    "v_mfma_f32_16x16x32_bf16 v[148:151], a[104:107], a[192:195], v[148:151]\n"
    "s_waitcnt lgkmcnt(2)\n"
    "ds_write_b64 v28, v[44:45] offset:1024\n"
    "v_mul_f32_e32 v236, s54, v236\n"
    "v_mfma_f32_16x16x32_bf16 v[152:155], a[112:115], a[192:195], v[152:155]\n"
    "v_mul_f32_e32 v237, s54, v237\n"
    "v_mul_f32_e32 v238, s54, v238\n"
    "v_mfma_f32_16x16x32_bf16 v[152:155], a[116:119], a[196:199], v[152:155]\n"
    "v_mul_f32_e32 v239, s54, v239\n"
    "v_cvt_pk_bf16_f32 v46, v236, v237\n"
    "v_mfma_f32_16x16x32_bf16 v[156:159], a[124:127], a[196:199], v[156:159]\n"
    "buffer_load_dwordx4 v118, s[28:31], s42 offen offset:3072 nt lds\n"
    "v_cvt_pk_bf16_f32 v47, v238, v239\n"
    "v_mfma_f32_16x16x32_bf16 v[156:159], a[120:123], a[192:195], v[156:159]\n"
    "ds_write_b64 v29, v[46:47] offset:1024\n"
    "v_mul_f32_e32 v128, s54, v128\n"
    "v_mfma_f32_16x16x32_bf16 v[160:163], a[64:67], a[200:203], v[160:163]\n"
    "v_mul_f32_e32 v129, s54, v129\n"
    "v_mul_f32_e32 v130, s54, v130\n"
    "v_mfma_f32_16x16x32_bf16 v[160:163], a[68:71], a[204:207], v[160:163]\n"
    "v_mul_f32_e32 v131, s54, v131\n"
    "v_cvt_pk_bf16_f32 v48, v128, v129\n"
    "v_mfma_f32_16x16x32_bf16 v[164:167], a[76:79], a[204:207], v[164:167]\n"
    "v_cvt_pk_bf16_f32 v49, v130, v131\n"
    "v_mfma_f32_16x16x32_bf16 v[164:167], a[72:75], a[200:203], v[164:167]\n"
    "ds_write_b64 v24, v[48:49]\n"
    "v_mul_f32_e32 v132, s54, v132\n"
    "v_mfma_f32_16x16x32_bf16 v[168:171], a[80:83], a[200:203], v[168:171]\n"
    "v_mul_f32_e32 v133, s54, v133\n"
    "v_mul_f32_e32 v134, s54, v134\n"
    "v_mfma_f32_16x16x32_bf16 v[168:171], a[84:87], a[204:207], v[168:171]\n"
    "s_add_u32 s42, s78, 0x8000\n"
    "v_mul_f32_e32 v135, s54, v135\n"
    "v_cvt_pk_bf16_f32 v50, v132, v133\n"
    "v_mfma_f32_16x16x32_bf16 v[172:175], a[92:95], a[204:207], v[172:175]\n"
    "s_and_b32 s42, s42, 0x3ffff\n"
    "v_cvt_pk_bf16_f32 v51, v134, v135\n"
    "v_mfma_f32_16x16x32_bf16 v[172:175], a[88:91], a[200:203], v[172:175]\n"
    "s_add_u32 m0, s55, 0x18000\n"
    "ds_write_b64 v25, v[50:51]\n"
    "v_mul_f32_e32 v136, s54, v136\n"
    "v_mfma_f32_16x16x32_bf16 v[176:179], a[96:99], a[200:203], v[176:179]\n"
    "v_mul_f32_e32 v137, s54, v137\n"
    "v_mul_f32_e32 v138, s54, v138\n"
    "v_mfma_f32_16x16x32_bf16 v[176:179], a[100:103], a[204:207], v[176:179]\n"
    "v_mul_f32_e32 v139, s54, v139\n"
    "v_cvt_pk_bf16_f32 v52, v136, v137\n"
    "v_mfma_f32_16x16x32_bf16 v[180:183], a[108:111], a[204:207], v[180:183]\n"
    "v_cvt_pk_bf16_f32 v53, v138, v139\n"
    "v_mfma_f32_16x16x32_bf16 v[180:183], a[104:107], a[200:203], v[180:183]\n"
    "ds_write_b64 v24, v[52:53] offset:1024\n"
    "v_mul_f32_e32 v140, s54, v140\n"
    "v_mfma_f32_16x16x32_bf16 v[184:187], a[112:115], a[200:203], v[184:187]\n"
    "v_mul_f32_e32 v141, s54, v141\n"
    "v_mul_f32_e32 v142, s54, v142\n"
    "v_mfma_f32_16x16x32_bf16 v[184:187], a[116:119], a[204:207], v[184:187]\n"
    "v_mul_f32_e32 v143, s54, v143\n"
    "v_cvt_pk_bf16_f32 v54, v140, v141\n"
    "v_mfma_f32_16x16x32_bf16 v[188:191], a[124:127], a[204:207], v[188:191]\n"
    "v_cvt_pk_bf16_f32 v55, v142, v143\n"
    "v_mfma_f32_16x16x32_bf16 v[188:191], a[120:123], a[200:203], v[188:191]\n"
    "buffer_load_dwordx4 v116, s[24:27], s42 offen nt lds\n"
    "ds_write_b64 v25, v[54:55] offset:1024\n"
    "v_mfma_f32_16x16x32_bf16 v[192:195], a[64:67], a[208:211], v[192:195]\n"
    "v_mul_f32_e32 v144, s54, v144\n"
    "v_mul_f32_e32 v145, s54, v145\n"
    "v_mfma_f32_16x16x32_bf16 v[192:195], a[68:71], a[212:215], v[192:195]\n"
    "v_mul_f32_e32 v146, s54, v146\n"
    "v_mul_f32_e32 v147, s54, v147\n"
    "v_mfma_f32_16x16x32_bf16 v[196:199], a[76:79], a[212:215], v[196:199]\n"
    "v_cvt_pk_bf16_f32 v40, v144, v145\n"
    "v_cvt_pk_bf16_f32 v41, v146, v147\n"
    "v_mfma_f32_16x16x32_bf16 v[196:199], a[72:75], a[208:211], v[196:199]\n"
    "ds_write_b64 v26, v[40:41]\n"
    "v_mul_f32_e32 v148, s54, v148\n"
    "v_mfma_f32_16x16x32_bf16 v[200:203], a[80:83], a[208:211], v[200:203]\n"
    "v_mul_f32_e32 v149, s54, v149\n"
    "v_mul_f32_e32 v150, s54, v150\n"
    "v_mfma_f32_16x16x32_bf16 v[200:203], a[84:87], a[212:215], v[200:203]\n"
    "v_mul_f32_e32 v151, s54, v151\n"
    "v_cvt_pk_bf16_f32 v42, v148, v149\n"
    "v_mfma_f32_16x16x32_bf16 v[204:207], a[92:95], a[212:215], v[204:207]\n"
    "v_cvt_pk_bf16_f32 v43, v150, v151\n"
    "v_mfma_f32_16x16x32_bf16 v[204:207], a[88:91], a[208:211], v[204:207]\n"
    "ds_write_b64 v27, v[42:43]\n"
    "v_mul_f32_e32 v152, s54, v152\n"
    "v_mfma_f32_16x16x32_bf16 v[208:211], a[96:99], a[208:211], v[208:211]\n"
    "v_mul_f32_e32 v153, s54, v153\n"
    "v_mul_f32_e32 v154, s54, v154\n"
    "v_mfma_f32_16x16x32_bf16 v[208:211], a[100:103], a[212:215], v[208:211]\n"
    "v_mul_f32_e32 v155, s54, v155\n"
    "v_cvt_pk_bf16_f32 v44, v152, v153\n"
    "v_mfma_f32_16x16x32_bf16 v[212:215], a[108:111], a[212:215], v[212:215]\n"
    "v_cvt_pk_bf16_f32 v45, v154, v155\n"
    "v_mfma_f32_16x16x32_bf16 v[212:215], a[104:107], a[208:211], v[212:215]\n"
    "ds_write_b64 v26, v[44:45] offset:1024\n"
    "v_mul_f32_e32 v156, s54, v156\n"
    "v_mfma_f32_16x16x32_bf16 v[216:219], a[112:115], a[208:211], v[216:219]\n"
    "v_mul_f32_e32 v157, s54, v157\n"
    "v_mul_f32_e32 v158, s54, v158\n"
    "v_mfma_f32_16x16x32_bf16 v[216:219], a[116:119], a[212:215], v[216:219]\n"
    "v_mul_f32_e32 v159, s54, v159\n"
    "v_cvt_pk_bf16_f32 v46, v156, v157\n"
    "v_mfma_f32_16x16x32_bf16 v[220:223], a[124:127], a[212:215], v[220:223]\n"
    "v_cvt_pk_bf16_f32 v47, v158, v159\n"
    "v_mfma_f32_16x16x32_bf16 v[220:223], a[120:123], a[208:211], v[220:223]\n"
    "ds_write_b64 v27, v[46:47] offset:1024\n"
    "ds_read_b128 a[64:67], v108\n"
    "ds_read_b128 a[68:71], v108 offset:256\n"
    "ds_read_b128 a[72:75], v108 offset:512\n"
    "ds_read_b128 a[76:79], v108 offset:768\n"
    "ds_read_b128 a[80:83], v108 offset:4096\n"
    "ds_read_b128 a[84:87], v108 offset:4352\n"
    "ds_read_b128 a[88:91], v108 offset:4608\n"
    "ds_read_b128 a[92:95], v108 offset:4864\n"
    "s_add_u32 m0, s55, 0x18000\n"
    "s_nop 0\n"
    "buffer_load_dwordx4 v117, s[24:27], s42 offen offset:1024 nt lds\n"
    "ds_read_b128 a[0:3], v30\n"
    "ds_read_b128 a[4:7], v30 offset:1024\n"
    "ds_read_b128 a[8:11], v30 offset:2048\n"
    "ds_read_b128 a[12:15], v30 offset:3072\n"
    "v_mul_f32_e32 v160, s54, v160\n"
    "v_mul_f32_e32 v161, s54, v161\n"
    "v_mul_f32_e32 v162, s54, v162\n"
    "v_mul_f32_e32 v163, s54, v163\n"
    "v_cvt_pk_bf16_f32 v48, v160, v161\n"
    "v_cvt_pk_bf16_f32 v49, v162, v163\n"
    "ds_write_b64 v24, v[48:49] offset:16384\n"
    "v_mul_f32_e32 v164, s54, v164\n"
    "v_mul_f32_e32 v165, s54, v165\n"
    "v_mul_f32_e32 v166, s54, v166\n"
    "v_mul_f32_e32 v167, s54, v167\n"
    "v_cvt_pk_bf16_f32 v50, v164, v165\n"
    "v_cvt_pk_bf16_f32 v51, v166, v167\n"
    "ds_write_b64 v25, v[50:51] offset:16384\n"
    "buffer_load_dwordx4 v118, s[24:27], s42 offen offset:2048 nt lds\n"
    "v_mul_f32_e32 v168, s54, v168\n"
    "v_mul_f32_e32 v169, s54, v169\n"
    "v_mul_f32_e32 v170, s54, v170\n"
    "v_mul_f32_e32 v171, s54, v171\n"
    "v_cvt_pk_bf16_f32 v52, v168, v169\n"
    "v_cvt_pk_bf16_f32 v53, v170, v171\n"
    "ds_write_b64 v24, v[52:53] offset:17408\n"
    "v_mul_f32_e32 v172, s54, v172\n"
    "v_mul_f32_e32 v173, s54, v173\n"
    "v_mul_f32_e32 v174, s54, v174\n"
    "v_mul_f32_e32 v175, s54, v175\n"
    "v_cvt_pk_bf16_f32 v54, v172, v173\n"
    "v_cvt_pk_bf16_f32 v55, v174, v175\n"
    "ds_write_b64 v25, v[54:55] offset:17408\n"
    "v_mul_f32_e32 v176, s54, v176\n"
    "v_mul_f32_e32 v177, s54, v177\n"
    "v_mul_f32_e32 v178, s54, v178\n"
    "v_mul_f32_e32 v179, s54, v179\n"
    "v_cvt_pk_bf16_f32 v40, v176, v177\n"
    "v_cvt_pk_bf16_f32 v41, v178, v179\n"
    "buffer_load_dwordx4 v119, s[24:27], s42 offen offset:3072 nt lds\n"
    "ds_write_b64 v26, v[40:41] offset:16384\n"
    "v_mul_f32_e32 v180, s54, v180\n"
    "v_mul_f32_e32 v181, s54, v181\n"
    "v_mul_f32_e32 v182, s54, v182\n"
    "v_mul_f32_e32 v183, s54, v183\n"
    "v_cvt_pk_bf16_f32 v42, v180, v181\n"
    "v_cvt_pk_bf16_f32 v43, v182, v183\n"
    "ds_write_b64 v27, v[42:43] offset:16384\n"
    "v_mul_f32_e32 v184, s54, v184\n"
    "v_mul_f32_e32 v185, s54, v185\n"
    "v_mul_f32_e32 v186, s54, v186\n"
    "v_mul_f32_e32 v187, s54, v187\n"
    "v_cvt_pk_bf16_f32 v44, v184, v185\n"
    "v_cvt_pk_bf16_f32 v45, v186, v187\n"
    "ds_write_b64 v26, v[44:45] offset:17408\n"
    "v_mul_f32_e32 v188, s54, v188\n"
    "v_mul_f32_e32 v189, s54, v189\n"
    "v_mul_f32_e32 v190, s54, v190\n"
    "v_mul_f32_e32 v191, s54, v191\n"
    "v_cvt_pk_bf16_f32 v46, v188, v189\n"
    "v_cvt_pk_bf16_f32 v47, v190, v191\n"
    "ds_write_b64 v27, v[46:47] offset:17408\n"
    "ds_read_b128 a[16:19], v30 offset:16384\n"
    "ds_read_b128 a[20:23], v30 offset:17408\n"
    "ds_read_b128 a[24:27], v30 offset:18432\n"
    "ds_read_b128 a[28:31], v30 offset:19456\n"
    "v_pk_mul_f32 v[192:193], v[192:193], v[34:35]\n"
    "v_pk_mul_f32 v[194:195], v[194:195], v[34:35]\n"
    "v_cvt_pk_bf16_f32 v48, v192, v193\n"
    "v_cvt_pk_bf16_f32 v49, v194, v195\n"
    "v_pk_mul_f32 v[196:197], v[196:197], v[34:35]\n"
    "v_pk_mul_f32 v[198:199], v[198:199], v[34:35]\n"
    "v_cvt_pk_bf16_f32 v50, v196, v197\n"
    "v_cvt_pk_bf16_f32 v51, v198, v199\n"
    "v_pk_mul_f32 v[200:201], v[200:201], v[34:35]\n"
    "v_pk_mul_f32 v[202:203], v[202:203], v[34:35]\n"
    "v_cvt_pk_bf16_f32 v52, v200, v201\n"
    "v_cvt_pk_bf16_f32 v53, v202, v203\n"
    "v_pk_mul_f32 v[204:205], v[204:205], v[34:35]\n"
    "v_pk_mul_f32 v[206:207], v[206:207], v[34:35]\n"
    "v_cvt_pk_bf16_f32 v54, v204, v205\n"
    "v_cvt_pk_bf16_f32 v55, v206, v207\n"
    "ds_write_b64 v24, v[48:49]\n"
    "ds_write_b64 v25, v[50:51]\n"
    "ds_write_b64 v24, v[52:53] offset:1024\n"
    "ds_write_b64 v25, v[54:55] offset:1024\n"
    "v_pk_mul_f32 v[208:209], v[208:209], v[34:35]\n"
    "v_pk_mul_f32 v[210:211], v[210:211], v[34:35]\n"
    "v_cvt_pk_bf16_f32 v40, v208, v209\n"
    "v_cvt_pk_bf16_f32 v41, v210, v211\n"
    "v_pk_mul_f32 v[212:213], v[212:213], v[34:35]\n"
    "v_pk_mul_f32 v[214:215], v[214:215], v[34:35]\n"
    "v_cvt_pk_bf16_f32 v42, v212, v213\n"
    "v_cvt_pk_bf16_f32 v43, v214, v215\n"
    "v_pk_mul_f32 v[216:217], v[216:217], v[34:35]\n"
    "v_pk_mul_f32 v[218:219], v[218:219], v[34:35]\n"
    "v_cvt_pk_bf16_f32 v44, v216, v217\n"
    "v_cvt_pk_bf16_f32 v45, v218, v219\n"
    "v_pk_mul_f32 v[220:221], v[220:221], v[34:35]\n"
    "v_pk_mul_f32 v[222:223], v[222:223], v[34:35]\n"
    "v_cvt_pk_bf16_f32 v46, v220, v221\n"
    "v_cvt_pk_bf16_f32 v47, v222, v223\n"
    "ds_write_b64 v26, v[40:41]\n"
    "ds_write_b64 v27, v[42:43]\n"
    "ds_write_b64 v26, v[44:45] offset:1024\n"
    "ds_write_b64 v27, v[46:47] offset:1024\n"
    "ds_read_b128 a[32:35], v30\n"
    "ds_read_b128 a[36:39], v30 offset:1024\n"
    "ds_read_b128 a[40:43], v30 offset:2048\n"
    "ds_read_b128 a[44:47], v30 offset:3072\n"
    "s_cmp_lg_u32 s44, 0\n"
    "s_cbranch_scc1 L_hs_done%=\n"
    "v_readfirstlane_b32 s76, v33\n"
    "s_nop 1\n"
    "L_hs_try%=:\n"
    "s_add_u32 s76, s76, 1\n"
    "v_cmp_eq_u32_e32 vcc, s76, v32\n"
    "s_nop 4\n"
    "s_cbranch_vccnz L_hs_try%=\n"
    "s_lshl_b32 s45, s47, 5\n"
    "s_add_u32 s45, s45, s48\n"
    "s_lshl_b32 s45, s45, 2\n"
    "v_mov_b32_e32 v1, s76\n"
    "v_mov_b32_e32 v2, 0\n"
    "v_mov_b32_e32 v3, 0x22000\n"
    "s_mov_b64 exec, 1\n"
    "buffer_store_dword v1, v2, s[56:59], s45 offen sc1\n"
    "ds_write_b32 v3, v1\n"
    "s_mov_b64 exec, -1\n"
    "L_hs_done%=:\n"
    "v_mov_b32_e32 v3, 0x22000\n"
    "s_waitcnt vmcnt(25)\n"
    "s_waitcnt lgkmcnt(0)\n"
    "s_barrier\n"
    "ds_read_b128 a[48:51], v31\n"
    "ds_read_b128 a[52:55], v31 offset:1024\n"
    "ds_read_b128 a[56:59], v31 offset:2048\n"
    "ds_read_b128 a[60:63], v31 offset:3072\n"
    "ds_read_b32 v7, v3\n"
    "L_tile0%=:\n"
    "s_waitcnt lgkmcnt(6)\n"
    "v_mfma_f32_16x16x32_bf16 v[8:11], a[64:67], a[0:3], 0\n"
    "s_mov_b32 s40, 0x8000\n"
    "ds_read_b64_tr_b16 a[96:97], v112\n"
    "ds_read_b64_tr_b16 a[98:99], v112 offset:4096\n"
    "v_mfma_f32_16x16x32_bf16 v[8:11], a[68:71], a[4:7], v[8:11]\n"
    "s_mov_b32 s41, 0xc000\n"
    "ds_read_b64_tr_b16 a[100:101], v113\n"
    "ds_read_b64_tr_b16 a[102:103], v113 offset:4096\n"
    "v_mfma_f32_16x16x32_bf16 v[8:11], a[72:75], a[8:11], v[8:11]\n"
    "ds_read_b64_tr_b16 a[104:105], v112 offset:256\n"
    "ds_read_b64_tr_b16 a[106:107], v112 offset:4352\n"
    "v_mfma_f32_16x16x32_bf16 v[8:11], a[76:79], a[12:15], v[8:11]\n"
    "s_mov_b32 s32, s16\n"
    "ds_read_b64_tr_b16 a[108:109], v113 offset:256\n"
    "ds_read_b64_tr_b16 a[110:111], v113 offset:4352\n"
    "v_mfma_f32_16x16x32_bf16 v[24:27], a[92:95], a[12:15], 0\n"
    "s_mov_b32 s33, s17\n"
    "ds_read_b64_tr_b16 a[112:113], v112 offset:512\n"
    "ds_read_b64_tr_b16 a[114:115], v112 offset:4608\n"
    "v_mfma_f32_16x16x32_bf16 v[24:27], a[88:91], a[8:11], v[24:27]\n"
    "s_and_b32 s33, s33, 0xffff\n"
    "ds_read_b64_tr_b16 a[116:117], v113 offset:512\n"
    "ds_read_b64_tr_b16 a[118:119], v113 offset:4608\n"
    "v_mfma_f32_16x16x32_bf16 v[24:27], a[84:87], a[4:7], v[24:27]\n"
    "s_mov_b32 s34, 0x400000\n"
    "ds_read_b64_tr_b16 a[120:121], v112 offset:768\n"
    "ds_read_b64_tr_b16 a[122:123], v112 offset:4864\n"
    "v_mfma_f32_16x16x32_bf16 v[24:27], a[80:83], a[0:3], v[24:27]\n"
    "s_mov_b32 s35, 0x20000\n"
    "ds_read_b64_tr_b16 a[124:125], v113 offset:768\n"
    "ds_read_b64_tr_b16 a[126:127], v113 offset:4864\n"
    "v_mfma_f32_16x16x32_bf16 v[12:15], a[64:67], a[16:19], 0\n"
    "s_add_u32 s45, s47, 0\n"
    "s_add_u32 s42, s78, 0x8000\n"
    "v_mfma_f32_16x16x32_bf16 v[12:15], a[68:71], a[20:23], v[12:15]\n"
    "s_and_b32 s45, s45, 3\n"
    "s_and_b32 s42, s42, 0x3ffff\n"
    "v_mfma_f32_16x16x32_bf16 v[12:15], a[72:75], a[24:27], v[12:15]\n"
    "s_lshl_b32 s69, s45, 15\n"
    "s_add_u32 m0, s55, 0x1c000\n"
    "v_mfma_f32_16x16x32_bf16 v[12:15], a[76:79], a[28:31], v[12:15]\n"
    "s_add_u32 s45, s47, 1\n"
    "buffer_load_dwordx4 v116, s[28:31], s42 offen nt lds\n"
    "v_mfma_f32_16x16x32_bf16 v[28:31], a[92:95], a[28:31], 0\n"
    "s_and_b32 s45, s45, 3\n"
    "v_max3_f32 v80, v8, v9, v10\n"
    "v_max3_f32 v80, v80, v11, v24\n"
    "v_mfma_f32_16x16x32_bf16 v[28:31], a[88:91], a[24:27], v[28:31]\n"
    "s_lshl_b32 s70, s45, 15\n"
    "v_max3_f32 v80, v80, v25, v26\n"
    "v_max_f32_e32 v80, v80, v27\n"
    "v_mfma_f32_16x16x32_bf16 v[28:31], a[84:87], a[20:23], v[28:31]\n"
    "s_add_u32 s45, s47, 2\n"
    "v_mov_b32_e32 v84, v80\n"
    "v_mfma_f32_16x16x32_bf16 v[28:31], a[80:83], a[16:19], v[28:31]\n"
    "s_and_b32 s45, s45, 3\n"
    "v_mfma_f32_16x16x32_bf16 v[16:19], a[64:67], a[32:35], 0\n"
    "s_lshl_b32 s71, s45, 15\n"
    "v_permlane32_swap_b32_e32 v80, v84\n"
    "v_max_f32_e32 v80, v80, v84\n"
    "v_mfma_f32_16x16x32_bf16 v[16:19], a[68:71], a[36:39], v[16:19]\n"
    "s_add_u32 s45, s47, 3\n"
    "v_mov_b32_e32 v84, v80\n"
    "v_mfma_f32_16x16x32_bf16 v[16:19], a[72:75], a[40:43], v[16:19]\n"
    "s_and_b32 s45, s45, 3\n"
    "v_mfma_f32_16x16x32_bf16 v[16:19], a[76:79], a[44:47], v[16:19]\n"
    "s_lshl_b32 s72, s45, 15\n"
    "v_permlane16_swap_b32_e32 v80, v84\n"
    "v_max_f32_e32 v80, v80, v84\n"
    "v_mfma_f32_16x16x32_bf16 v[32:35], a[92:95], a[44:47], 0\n"
    "s_xor_b32 s97, s55, 0x1000\n"
    "v_min_f32_e64 v92, -v80, v92\n"
    "v_mov_b32_e32 v93, v92\n"
    "v_mfma_f32_16x16x32_bf16 v[32:35], a[88:91], a[40:43], v[32:35]\n"
    "v_mov_b32_e32 v94, v92\n"
    "v_mov_b32_e32 v95, v92\n"
    "v_mfma_f32_16x16x32_bf16 v[32:35], a[84:87], a[36:39], v[32:35]\n"
    "s_waitcnt lgkmcnt(15)\n"
    "v_max3_f32 v81, v12, v13, v14\n"
    "v_max3_f32 v81, v81, v15, v28\n"
    "v_mfma_f32_16x16x32_bf16 v[32:35], a[80:83], a[32:35], v[32:35]\n"
    "buffer_load_dwordx4 v118, s[28:31], s42 offen offset:1024 nt lds\n"
    "v_readfirstlane_b32 s76, v7\n"
    "v_mfma_f32_16x16x32_bf16 v[20:23], a[64:67], a[48:51], 0\n"
    "v_max3_f32 v81, v81, v29, v30\n"
    "v_max_f32_e32 v81, v81, v31\n"
    "v_mfma_f32_16x16x32_bf16 v[20:23], a[68:71], a[52:55], v[20:23]\n"
    "v_mov_b32_e32 v85, v81\n"
    "v_add_f32_e32 v8, v92, v8\n"
    "v_mfma_f32_16x16x32_bf16 v[20:23], a[72:75], a[56:59], v[20:23]\n"
    "v_add_f32_e32 v9, v92, v9\n"
    "v_add_f32_e32 v10, v92, v10\n"
    "v_mfma_f32_16x16x32_bf16 v[20:23], a[76:79], a[60:63], v[20:23]\n"
    "v_permlane32_swap_b32_e32 v81, v85\n"
    "v_max_f32_e32 v81, v81, v85\n"
    "v_mfma_f32_16x16x32_bf16 v[36:39], a[92:95], a[60:63], 0\n"
    "v_mov_b32_e32 v85, v81\n"
    "v_max3_f32 v82, v16, v17, v18\n"
    "v_mfma_f32_16x16x32_bf16 v[36:39], a[88:91], a[56:59], v[36:39]\n"
    "v_max3_f32 v82, v82, v19, v32\n"
    "v_max3_f32 v82, v82, v33, v34\n"
    "v_mfma_f32_16x16x32_bf16 v[36:39], a[84:87], a[52:55], v[36:39]\n"
    "v_permlane16_swap_b32_e32 v81, v85\n"
    "v_max_f32_e32 v81, v81, v85\n"
    "v_mfma_f32_16x16x32_bf16 v[36:39], a[80:83], a[48:51], v[36:39]\n"
    "v_min_f32_e64 v96, -v81, v96\n"
    "v_mov_b32_e32 v97, v96\n"
    "ds_read_b128 a[64:67], v109\n"
    "ds_read_b128 a[68:71], v109 offset:256\n"
    "ds_read_b128 a[72:75], v109 offset:512\n"
    "ds_read_b128 a[76:79], v109 offset:768\n"
    "ds_read_b128 a[80:83], v109 offset:4096\n"
    "ds_read_b128 a[84:87], v109 offset:4352\n"
    "ds_read_b128 a[88:91], v109 offset:4608\n"
    "ds_read_b128 a[92:95], v109 offset:4864\n"
    "v_mov_b32_e32 v98, v96\n"
    "v_mov_b32_e32 v99, v96\n"
    "v_max_f32_e32 v82, v82, v35\n"
    "v_mov_b32_e32 v86, v82\n"
    "s_nop 1\n"
    "v_permlane32_swap_b32_e32 v82, v86\n"
    "v_max_f32_e32 v82, v82, v86\n"
    "v_mov_b32_e32 v86, v82\n"
    "s_nop 1\n"
    "v_permlane16_swap_b32_e32 v82, v86\n"
    "v_max_f32_e32 v82, v82, v86\n"
    "v_min_f32_e64 v100, -v82, v100\n"
    "v_mov_b32_e32 v101, v100\n"
    "v_mov_b32_e32 v102, v100\n"
    "v_mov_b32_e32 v103, v100\n"
    "v_add_f32_e32 v11, v92, v11\n"
    "v_exp_f32_e32 v8, v8\n"
    "v_exp_f32_e32 v9, v9\n"
    "v_exp_f32_e32 v10, v10\n"
    "v_exp_f32_e32 v11, v11\n"
    "v_cvt_pk_bf16_f32 v64, v8, v9\n"
    "v_max3_f32 v83, v20, v21, v22\n"
    "v_max3_f32 v83, v83, v23, v36\n"
    "v_max3_f32 v83, v83, v37, v38\n"
    "v_max_f32_e32 v83, v83, v39\n"
    "v_mov_b32_e32 v87, v83\n"
    "s_nop 1\n"
    "v_permlane32_swap_b32_e32 v83, v87\n"
    "v_max_f32_e32 v83, v83, v87\n"
    "v_mov_b32_e32 v87, v83\n"
    "s_nop 1\n"
    "v_permlane16_swap_b32_e32 v83, v87\n"
    "v_max_f32_e32 v83, v83, v87\n"
    "v_min_f32_e64 v104, -v83, v104\n"
    "v_mov_b32_e32 v105, v104\n"
    "v_mov_b32_e32 v106, v104\n"
    "v_mov_b32_e32 v107, v104\n"
    "s_waitcnt lgkmcnt(0)\n"
    "v_mfma_f32_16x16x32_bf16 v[40:43], a[64:67], a[0:3], v[92:95]\n"
    "v_cvt_pk_bf16_f32 v65, v10, v11\n"
    "v_add_f32_e32 v24, v92, v24\n"
    "v_mfma_f32_16x16x32_bf16 v[40:43], a[68:71], a[4:7], v[40:43]\n"
    "buffer_load_dwordx4 v116, s[28:31], s42 offen offset:2048 nt lds\n"
    "v_add_f32_e32 v25, v92, v25\n"
    "v_mfma_f32_16x16x32_bf16 v[40:43], a[72:75], a[8:11], v[40:43]\n"
    "v_add_f32_e32 v26, v92, v26\n"
    "v_add_f32_e32 v27, v92, v27\n"
    "v_mfma_f32_16x16x32_bf16 v[40:43], a[76:79], a[12:15], v[40:43]\n"
    "v_exp_f32_e32 v24, v24\n"
    "v_mfma_f32_16x16x32_bf16 v[52:55], a[92:95], a[12:15], v[92:95]\n"
    "v_exp_f32_e32 v25, v25\n"
    "v_mfma_f32_16x16x32_bf16 v[52:55], a[88:91], a[8:11], v[52:55]\n"
    "v_exp_f32_e32 v26, v26\n"
    "v_mfma_f32_16x16x32_bf16 v[52:55], a[84:87], a[4:7], v[52:55]\n"
    "v_exp_f32_e32 v27, v27\n"
    "v_mfma_f32_16x16x32_bf16 v[52:55], a[80:83], a[0:3], v[52:55]\n"
    "v_cvt_pk_bf16_f32 v66, v24, v25\n"
    "v_cvt_pk_bf16_f32 v67, v26, v27\n"
    "v_mfma_f32_16x16x32_bf16 v[44:47], a[64:67], a[16:19], v[96:99]\n"
    "v_add_f32_e32 v12, v96, v12\n"
    "v_add_f32_e32 v13, v96, v13\n"
    "v_mfma_f32_16x16x32_bf16 v[44:47], a[68:71], a[20:23], v[44:47]\n"
    "buffer_load_dwordx4 v118, s[28:31], s42 offen offset:3072 nt lds\n"
    "v_add_f32_e32 v14, v96, v14\n"
    "v_mfma_f32_16x16x32_bf16 v[44:47], a[72:75], a[24:27], v[44:47]\n"
    "v_add_f32_e32 v15, v96, v15\n"
    "v_mfma_f32_16x16x32_bf16 v[44:47], a[76:79], a[28:31], v[44:47]\n"
    "v_exp_f32_e32 v12, v12\n"
    "v_mfma_f32_16x16x32_bf16 v[56:59], a[92:95], a[28:31], v[96:99]\n"
    "v_exp_f32_e32 v13, v13\n"
    "v_mfma_f32_16x16x32_bf16 v[56:59], a[88:91], a[24:27], v[56:59]\n"
    "v_exp_f32_e32 v14, v14\n"
    "v_mfma_f32_16x16x32_bf16 v[56:59], a[84:87], a[20:23], v[56:59]\n"
    "v_exp_f32_e32 v15, v15\n"
    "v_mfma_f32_16x16x32_bf16 v[56:59], a[80:83], a[16:19], v[56:59]\n"
    "v_cvt_pk_bf16_f32 v68, v12, v13\n"
    "v_cvt_pk_bf16_f32 v69, v14, v15\n"
    "v_mfma_f32_16x16x32_bf16 v[48:51], a[64:67], a[32:35], v[100:103]\n"
    "s_add_u32 s42, s78, 0xc000\n"
    "v_add_f32_e32 v28, v96, v28\n"
    "v_add_f32_e32 v29, v96, v29\n"
    "v_mfma_f32_16x16x32_bf16 v[48:51], a[68:71], a[36:39], v[48:51]\n"
    "s_and_b32 s42, s42, 0x3ffff\n"
    "v_add_f32_e32 v30, v96, v30\n"
    "v_add_f32_e32 v31, v96, v31\n"
    "v_mfma_f32_16x16x32_bf16 v[48:51], a[72:75], a[40:43], v[48:51]\n"
    "s_add_u32 m0, s55, 0x10000\n"
    "v_exp_f32_e32 v28, v28\n"
    "v_mfma_f32_16x16x32_bf16 v[48:51], a[76:79], a[44:47], v[48:51]\n"
    "v_exp_f32_e32 v29, v29\n"
    "v_mfma_f32_16x16x32_bf16 v[60:63], a[92:95], a[44:47], v[100:103]\n"
    "s_waitcnt vmcnt(9)\n"
    "v_exp_f32_e32 v30, v30\n"
    "v_mfma_f32_16x16x32_bf16 v[60:63], a[88:91], a[40:43], v[60:63]\n"
    "s_barrier\n"
    "v_exp_f32_e32 v31, v31\n"
    "v_mfma_f32_16x16x32_bf16 v[60:63], a[84:87], a[36:39], v[60:63]\n"
    "v_cvt_pk_bf16_f32 v70, v28, v29\n"
    "v_cvt_pk_bf16_f32 v71, v30, v31\n"
    "v_mfma_f32_16x16x32_bf16 v[60:63], a[80:83], a[32:35], v[60:63]\n"
    "v_add_f32_e32 v16, v100, v16\n"
    "v_add_f32_e32 v17, v100, v17\n"
    "v_xor_b32_e32 v108, s40, v108\n"
    "v_xor_b32_e32 v109, s40, v109\n"
    "ds_read_b128 a[64:67], v108\n"
    "ds_read_b128 a[68:71], v108 offset:256\n"
    "ds_read_b128 a[72:75], v108 offset:512\n"
    "ds_read_b128 a[76:79], v108 offset:768\n"
    "ds_read_b128 a[80:83], v108 offset:4096\n"
    "ds_read_b128 a[84:87], v108 offset:4352\n"
    "ds_read_b128 a[88:91], v108 offset:4608\n"
    "ds_read_b128 a[92:95], v108 offset:4864\n"
    "v_add_f32_e32 v18, v100, v18\n"
    "v_add_f32_e32 v19, v100, v19\n"
    "v_exp_f32_e32 v16, v16\n"
    "v_exp_f32_e32 v17, v17\n"
    "v_exp_f32_e32 v18, v18\n"
    "v_exp_f32_e32 v19, v19\n"
    "v_cvt_pk_bf16_f32 v72, v16, v17\n"
    "v_cvt_pk_bf16_f32 v73, v18, v19\n"
    "v_add_f32_e32 v32, v100, v32\n"
    "v_add_f32_e32 v33, v100, v33\n"
    "v_add_f32_e32 v34, v100, v34\n"
    "v_add_f32_e32 v35, v100, v35\n"
    "v_exp_f32_e32 v32, v32\n"
    "v_exp_f32_e32 v33, v33\n"
    "v_exp_f32_e32 v34, v34\n"
    "v_exp_f32_e32 v35, v35\n"
    "v_cvt_pk_bf16_f32 v74, v32, v33\n"
    "v_cvt_pk_bf16_f32 v75, v34, v35\n"
    "v_add_f32_e32 v20, v104, v20\n"
    "v_add_f32_e32 v21, v104, v21\n"
    "v_add_f32_e32 v22, v104, v22\n"
    "v_add_f32_e32 v23, v104, v23\n"
    "v_exp_f32_e32 v20, v20\n"
    "v_exp_f32_e32 v21, v21\n"
    "v_exp_f32_e32 v22, v22\n"
    "v_exp_f32_e32 v23, v23\n"
    "v_cvt_pk_bf16_f32 v76, v20, v21\n"
    "v_cvt_pk_bf16_f32 v77, v22, v23\n"
    "v_add_f32_e32 v36, v104, v36\n"
    "v_add_f32_e32 v37, v104, v37\n"
    "v_add_f32_e32 v38, v104, v38\n"
    "v_add_f32_e32 v39, v104, v39\n"
    "v_exp_f32_e32 v36, v36\n"
    "v_exp_f32_e32 v37, v37\n"
    "v_exp_f32_e32 v38, v38\n"
    "v_exp_f32_e32 v39, v39\n"
    "v_cvt_pk_bf16_f32 v78, v36, v37\n"
    "v_cvt_pk_bf16_f32 v79, v38, v39\n"
    "s_waitcnt lgkmcnt(15)\n"
    "v_mfma_f32_16x16x32_bf16 a[128:131], a[144:147], v[64:67], 0\n"
    "v_exp_f32_e32 v40, v40\n"
    "v_mfma_f32_16x16x32_bf16 a[132:135], a[144:147], v[68:71], 0\n"
    "s_mov_b32 s43, 11\n"
    "v_exp_f32_e32 v41, v41\n"
    "v_mfma_f32_16x16x32_bf16 a[136:139], a[144:147], v[72:75], 0\n"
    "buffer_load_dwordx4 v116, s[24:27], s42 offen nt lds\n"
    "v_cvt_pk_bf16_f32 v80, v40, v41\n"
    "v_mfma_f32_16x16x32_bf16 a[140:143], a[144:147], v[76:79], 0\n"
    "v_exp_f32_e32 v42, v42\n"
    "v_mfma_f32_16x16x32_bf16 v[128:131], a[96:99], v[64:67], 0\n"
    "v_exp_f32_e32 v43, v43\n"
    "v_mfma_f32_16x16x32_bf16 v[160:163], a[96:99], v[68:71], 0\n"
    "buffer_load_dwordx4 v117, s[24:27], s42 offen offset:1024 nt lds\n"
    "v_cvt_pk_bf16_f32 v81, v42, v43\n"
    "v_mfma_f32_16x16x32_bf16 v[192:195], a[96:99], v[72:75], 0\n"
    "v_exp_f32_e32 v44, v44\n"
    "v_mfma_f32_16x16x32_bf16 v[224:227], a[96:99], v[76:79], 0\n"
    "v_exp_f32_e32 v45, v45\n"
    "v_mfma_f32_16x16x32_bf16 v[132:135], a[100:103], v[64:67], 0\n"
    "buffer_load_dwordx4 v118, s[24:27], s42 offen offset:2048 nt lds\n"
    "v_cvt_pk_bf16_f32 v84, v44, v45\n"
    "v_mfma_f32_16x16x32_bf16 v[164:167], a[100:103], v[68:71], 0\n"
    "v_exp_f32_e32 v46, v46\n"
    "v_mfma_f32_16x16x32_bf16 v[196:199], a[100:103], v[72:75], 0\n"
    "v_exp_f32_e32 v47, v47\n"
    "v_mfma_f32_16x16x32_bf16 v[228:231], a[100:103], v[76:79], 0\n"
    "buffer_load_dwordx4 v119, s[24:27], s42 offen offset:3072 nt lds\n"
    "v_cvt_pk_bf16_f32 v85, v46, v47\n"
    "v_mfma_f32_16x16x32_bf16 v[136:139], a[104:107], v[64:67], 0\n"
    "s_xor_b32 m0, m0, 0x4000\n"
    "v_exp_f32_e32 v48, v48\n"
    "v_mfma_f32_16x16x32_bf16 v[168:171], a[104:107], v[68:71], 0\n"
    "v_exp_f32_e32 v49, v49\n"
    "v_mfma_f32_16x16x32_bf16 v[200:203], a[104:107], v[72:75], 0\n"
    "s_memrealtime s[74:75]\n"
    "buffer_load_dwordx4 v116, s[28:31], s42 offen nt lds\n"
    "v_cvt_pk_bf16_f32 v88, v48, v49\n"
    "v_mfma_f32_16x16x32_bf16 v[232:235], a[104:107], v[76:79], 0\n"
    "s_lshl_b32 s45, s46, 2\n"
    "v_exp_f32_e32 v50, v50\n"
    "v_mfma_f32_16x16x32_bf16 v[140:143], a[108:111], v[64:67], 0\n"
    "s_add_u32 s45, s45, s44\n"
    "v_exp_f32_e32 v51, v51\n"
    "v_mfma_f32_16x16x32_bf16 v[172:175], a[108:111], v[68:71], 0\n"
    "s_lshl_b32 s68, s45, 12\n"
    "buffer_load_dwordx4 v118, s[28:31], s42 offen offset:1024 nt lds\n"
    "v_cvt_pk_bf16_f32 v89, v50, v51\n"
    "v_mfma_f32_16x16x32_bf16 v[204:207], a[108:111], v[72:75], 0\n"
    "s_mov_b32 s67, s35\n"
    "v_exp_f32_e32 v52, v52\n"
    "v_mfma_f32_16x16x32_bf16 v[236:239], a[108:111], v[76:79], 0\n"
    "v_exp_f32_e32 v53, v53\n"
    "v_mfma_f32_16x16x32_bf16 v[144:147], a[112:115], v[64:67], 0\n"
    "buffer_load_dwordx4 v116, s[28:31], s42 offen offset:2048 nt lds\n"
    "v_cvt_pk_bf16_f32 v82, v52, v53\n"
    "v_mfma_f32_16x16x32_bf16 v[176:179], a[112:115], v[68:71], 0\n"
    "v_exp_f32_e32 v54, v54\n"
    "v_mfma_f32_16x16x32_bf16 v[208:211], a[112:115], v[72:75], 0\n"
    "v_exp_f32_e32 v55, v55\n"
    "v_mfma_f32_16x16x32_bf16 v[240:243], a[112:115], v[76:79], 0\n"
    "buffer_load_dwordx4 v118, s[28:31], s42 offen offset:3072 nt lds\n"
    "v_cvt_pk_bf16_f32 v83, v54, v55\n"
    "v_mfma_f32_16x16x32_bf16 v[148:151], a[116:119], v[64:67], 0\n"
    "v_exp_f32_e32 v56, v56\n"
    "v_mfma_f32_16x16x32_bf16 v[180:183], a[116:119], v[68:71], 0\n"
    "v_exp_f32_e32 v57, v57\n"
    "v_mfma_f32_16x16x32_bf16 v[212:215], a[116:119], v[72:75], 0\n"
    "v_cvt_pk_bf16_f32 v86, v56, v57\n"
    "v_lshlrev_b32_e32 v1, 7, v0\n"
    "v_mfma_f32_16x16x32_bf16 v[244:247], a[116:119], v[76:79], 0\n"
    "v_exp_f32_e32 v58, v58\n"
    "v_mfma_f32_16x16x32_bf16 v[152:155], a[120:123], v[64:67], 0\n"
    "v_exp_f32_e32 v59, v59\n"
    "v_mfma_f32_16x16x32_bf16 v[184:187], a[120:123], v[68:71], 0\n"
    "v_cvt_pk_bf16_f32 v87, v58, v59\n"
    "v_mfma_f32_16x16x32_bf16 v[216:219], a[120:123], v[72:75], 0\n"
    "v_exp_f32_e32 v60, v60\n"
    "v_mfma_f32_16x16x32_bf16 v[248:251], a[120:123], v[76:79], 0\n"
    "v_exp_f32_e32 v61, v61\n"
    "v_mfma_f32_16x16x32_bf16 v[156:159], a[124:127], v[64:67], 0\n"
    "v_cvt_pk_bf16_f32 v90, v60, v61\n"
    "v_mfma_f32_16x16x32_bf16 v[188:191], a[124:127], v[68:71], 0\n"
    "v_exp_f32_e32 v62, v62\n"
    "v_mfma_f32_16x16x32_bf16 v[220:223], a[124:127], v[72:75], 0\n"
    "s_waitcnt lgkmcnt(0)\n"
    "v_exp_f32_e32 v63, v63\n"
    "v_mfma_f32_16x16x32_bf16 v[252:255], a[124:127], v[76:79], 0\n"
    "s_add_u32 s73, s74, 0x7d\n"
    "v_cvt_pk_bf16_f32 v91, v62, v63\n"
    ".p2align 6\n"
    "L_loop%=:\n"
    "v_mfma_f32_16x16x32_bf16 v[8:11], a[64:67], a[0:3], v[92:95]\n"
    "ds_read_b64_tr_b16 a[96:97], v114\n"
    "ds_read_b64_tr_b16 a[98:99], v114 offset:4096\n"
    "v_mfma_f32_16x16x32_bf16 v[8:11], a[68:71], a[4:7], v[8:11]\n"
    "ds_read_b64_tr_b16 a[100:101], v115\n"
    "v_mfma_f32_16x16x32_bf16 v[8:11], a[72:75], a[8:11], v[8:11]\n"
    "s_sub_u32 s43, s43, 1\n"
    "ds_read_b64_tr_b16 a[102:103], v115 offset:4096\n"
    "ds_read_b64_tr_b16 a[104:105], v114 offset:256\n"
    "v_mfma_f32_16x16x32_bf16 v[8:11], a[76:79], a[12:15], v[8:11]\n"
    "ds_read_b64_tr_b16 a[106:107], v114 offset:4352\n"
    "v_mfma_f32_16x16x32_bf16 v[12:15], a[76:79], a[28:31], v[96:99]\n"
    "ds_read_b64_tr_b16 a[108:109], v115 offset:256\n"
    "ds_read_b64_tr_b16 a[110:111], v115 offset:4352\n"
    "v_mfma_f32_16x16x32_bf16 v[12:15], a[72:75], a[24:27], v[12:15]\n"
    "ds_read_b64_tr_b16 a[112:113], v114 offset:512\n"
    "v_mfma_f32_16x16x32_bf16 v[12:15], a[68:71], a[20:23], v[12:15]\n"
    "ds_read_b64_tr_b16 a[114:115], v114 offset:4608\n"
    "ds_read_b64_tr_b16 a[116:117], v115 offset:512\n"
    "v_mfma_f32_16x16x32_bf16 v[12:15], a[64:67], a[16:19], v[12:15]\n"
    "ds_read_b64_tr_b16 a[118:119], v115 offset:4608\n"
    "v_mfma_f32_16x16x32_bf16 v[16:19], a[64:67], a[32:35], v[100:103]\n"
    "v_exp_f32_e32 v8, v8\n"
    "v_mfma_f32_16x16x32_bf16 v[16:19], a[68:71], a[36:39], v[16:19]\n"
    "v_exp_f32_e32 v9, v9\n"
    "v_mfma_f32_16x16x32_bf16 v[16:19], a[72:75], a[40:43], v[16:19]\n"
    "v_exp_f32_e32 v10, v10\n"
    "v_mfma_f32_16x16x32_bf16 v[16:19], a[76:79], a[44:47], v[16:19]\n"
    "v_cvt_pk_bf16_f32 v64, v8, v9\n"
    "v_mfma_f32_16x16x32_bf16 v[20:23], a[76:79], a[60:63], v[104:107]\n"
    "v_exp_f32_e32 v11, v11\n"
    "v_mfma_f32_16x16x32_bf16 v[20:23], a[72:75], a[56:59], v[20:23]\n"
    "ds_read_b64_tr_b16 a[120:121], v114 offset:768\n"
    "v_cvt_pk_bf16_f32 v65, v10, v11\n"
    "v_mfma_f32_16x16x32_bf16 v[20:23], a[68:71], a[52:55], v[20:23]\n"
    "v_exp_f32_e32 v12, v12\n"
    "v_mfma_f32_16x16x32_bf16 v[20:23], a[64:67], a[48:51], v[20:23]\n"
    "s_add_u32 s64, s32, s68\n"
    "s_addc_u32 s65, s33, 0\n"
    "v_mfma_f32_16x16x32_bf16 v[24:27], a[80:83], a[0:3], v[92:95]\n"
    "v_exp_f32_e32 v13, v13\n"
    "v_mfma_f32_16x16x32_bf16 v[24:27], a[84:87], a[4:7], v[24:27]\n"
    "ds_read_b64_tr_b16 a[122:123], v114 offset:4864\n"
    "v_cvt_pk_bf16_f32 v68, v12, v13\n"
    "v_mfma_f32_16x16x32_bf16 v[24:27], a[88:91], a[8:11], v[24:27]\n"
    "v_exp_f32_e32 v14, v14\n"
    "v_mfma_f32_16x16x32_bf16 v[24:27], a[92:95], a[12:15], v[24:27]\n"
    "v_mfma_f32_16x16x32_bf16 v[28:31], a[92:95], a[28:31], v[96:99]\n"
    "s_sub_u32 s66, 0x400000, s68\n"
    "v_exp_f32_e32 v15, v15\n"
    "v_mfma_f32_16x16x32_bf16 v[28:31], a[88:91], a[24:27], v[28:31]\n"
    "ds_read_b64_tr_b16 a[124:125], v115 offset:768\n"
    "v_cvt_pk_bf16_f32 v69, v14, v15\n"
    "v_mfma_f32_16x16x32_bf16 v[28:31], a[84:87], a[20:23], v[28:31]\n"
    "s_min_u32 s66, s66, 0x180\n"
    "v_exp_f32_e32 v16, v16\n"
    "v_mfma_f32_16x16x32_bf16 v[28:31], a[80:83], a[16:19], v[28:31]\n"
    "ds_read_b64_tr_b16 a[126:127], v115 offset:4864\n"
    "ds_read_b128 a[64:67], v109\n"
    "v_mfma_f32_16x16x32_bf16 v[32:35], a[80:83], a[32:35], v[100:103]\n"
    "v_exp_f32_e32 v17, v17\n"
    "v_mfma_f32_16x16x32_bf16 v[32:35], a[84:87], a[36:39], v[32:35]\n"
    "ds_read_b128 a[68:71], v109 offset:256\n"
    "v_cvt_pk_bf16_f32 v72, v16, v17\n"
    "v_mfma_f32_16x16x32_bf16 v[32:35], a[88:91], a[40:43], v[32:35]\n"
    "v_exp_f32_e32 v18, v18\n"
    "v_mfma_f32_16x16x32_bf16 v[32:35], a[92:95], a[44:47], v[32:35]\n"
    "ds_read_b128 a[72:75], v109 offset:512\n"
    "v_xor_b32_e32 v112, s40, v112\n"
    "v_mfma_f32_16x16x32_bf16 v[36:39], a[92:95], a[60:63], v[104:107]\n"
    "v_exp_f32_e32 v19, v19\n"
    "v_mfma_f32_16x16x32_bf16 v[36:39], a[88:91], a[56:59], v[36:39]\n"
    "ds_read_b128 a[76:79], v109 offset:768\n"
    "v_cvt_pk_bf16_f32 v73, v18, v19\n"
    "v_mfma_f32_16x16x32_bf16 v[36:39], a[84:87], a[52:55], v[36:39]\n"
    "v_exp_f32_e32 v20, v20\n"
    "v_mfma_f32_16x16x32_bf16 v[36:39], a[80:83], a[48:51], v[36:39]\n"
    "v_xor_b32_e32 v113, s40, v113\n"
    "v_mfma_f32_16x16x32_bf16 a[128:131], a[144:147], v[80:83], a[128:131]\n"
    "v_exp_f32_e32 v21, v21\n"
    "v_mfma_f32_16x16x32_bf16 a[132:135], a[144:147], v[84:87], a[132:135]\n"
    "ds_read_b128 a[80:83], v109 offset:4096\n"
    "v_cvt_pk_bf16_f32 v76, v20, v21\n"
    "v_mfma_f32_16x16x32_bf16 a[136:139], a[144:147], v[88:91], a[136:139]\n"
    "s_waitcnt lgkmcnt(5)\n"
    "v_exp_f32_e32 v22, v22\n"
    "v_mfma_f32_16x16x32_bf16 v[128:131], a[96:99], v[80:83], v[128:131]\n"
    "v_xor_b32_e32 v114, s40, v114\n"
    "v_mfma_f32_16x16x32_bf16 v[160:163], a[96:99], v[84:87], v[160:163]\n"
    "v_exp_f32_e32 v23, v23\n"
    "v_mfma_f32_16x16x32_bf16 v[192:195], a[96:99], v[88:91], v[192:195]\n"
    "ds_read_b128 a[84:87], v109 offset:4352\n"
    "v_cvt_pk_bf16_f32 v77, v22, v23\n"
    "v_mfma_f32_16x16x32_bf16 v[132:135], a[100:103], v[80:83], v[132:135]\n"
    "v_exp_f32_e32 v24, v24\n"
    "v_mfma_f32_16x16x32_bf16 v[164:167], a[100:103], v[84:87], v[164:167]\n"
    "ds_read_b128 a[88:91], v109 offset:4608\n"
    "v_xor_b32_e32 v115, s40, v115\n"
    "v_mfma_f32_16x16x32_bf16 v[196:199], a[100:103], v[88:91], v[196:199]\n"
    "v_exp_f32_e32 v25, v25\n"
    "v_mfma_f32_16x16x32_bf16 v[136:139], a[104:107], v[80:83], v[136:139]\n"
    "ds_read_b128 a[92:95], v109 offset:4864\n"
    "v_cvt_pk_bf16_f32 v66, v24, v25\n"
    "v_mfma_f32_16x16x32_bf16 v[168:171], a[104:107], v[84:87], v[168:171]\n"
    "v_exp_f32_e32 v26, v26\n"
    "v_mfma_f32_16x16x32_bf16 v[200:203], a[104:107], v[88:91], v[200:203]\n"
    "s_xor_b32 s40, s40, 0x18000\n"
    "v_mfma_f32_16x16x32_bf16 v[140:143], a[108:111], v[80:83], v[140:143]\n"
    "s_xor_b32 s41, s41, 0x18000\n"
    "v_exp_f32_e32 v27, v27\n"
    "v_mfma_f32_16x16x32_bf16 v[172:175], a[108:111], v[84:87], v[172:175]\n"
    "ds_read_b64_tr_b16 a[96:97], v112\n"
    "v_cvt_pk_bf16_f32 v67, v26, v27\n"
    "v_mfma_f32_16x16x32_bf16 v[204:207], a[108:111], v[88:91], v[204:207]\n"
    "v_exp_f32_e32 v28, v28\n"
    "v_mfma_f32_16x16x32_bf16 v[144:147], a[112:115], v[80:83], v[144:147]\n"
    "ds_read_b64_tr_b16 a[98:99], v112 offset:4096\n"
    "ds_read_b64_tr_b16 a[100:101], v113\n"
    "v_mfma_f32_16x16x32_bf16 v[176:179], a[112:115], v[84:87], v[176:179]\n"
    "v_exp_f32_e32 v29, v29\n"
    "v_mfma_f32_16x16x32_bf16 v[208:211], a[112:115], v[88:91], v[208:211]\n"
    "ds_read_b64_tr_b16 a[102:103], v113 offset:4096\n"
    "v_cvt_pk_bf16_f32 v70, v28, v29\n"
    "v_mfma_f32_16x16x32_bf16 v[148:151], a[116:119], v[80:83], v[148:151]\n"
    "v_exp_f32_e32 v30, v30\n"
    "v_mfma_f32_16x16x32_bf16 v[180:183], a[116:119], v[84:87], v[180:183]\n"
    "ds_read_b64_tr_b16 a[104:105], v112 offset:256\n"
    "ds_read_b64_tr_b16 a[106:107], v112 offset:4352\n"
    "v_mfma_f32_16x16x32_bf16 v[212:215], a[116:119], v[88:91], v[212:215]\n"
    "v_exp_f32_e32 v31, v31\n"
    "v_mfma_f32_16x16x32_bf16 v[152:155], a[120:123], v[80:83], v[152:155]\n"
    "ds_read_b64_tr_b16 a[108:109], v113 offset:256\n"
    "v_cvt_pk_bf16_f32 v71, v30, v31\n"
    "v_mfma_f32_16x16x32_bf16 v[184:187], a[120:123], v[84:87], v[184:187]\n"
    "v_exp_f32_e32 v32, v32\n"
    "v_mfma_f32_16x16x32_bf16 v[216:219], a[120:123], v[88:91], v[216:219]\n"
    "ds_read_b64_tr_b16 a[110:111], v113 offset:4352\n"
    "v_xor_b32_e32 v108, s40, v108\n"
    "v_mfma_f32_16x16x32_bf16 v[156:159], a[124:127], v[80:83], v[156:159]\n"
    "v_exp_f32_e32 v33, v33\n"
    "v_mfma_f32_16x16x32_bf16 v[188:191], a[124:127], v[84:87], v[188:191]\n"
    "ds_read_b64_tr_b16 a[112:113], v112 offset:512\n"
    "v_cvt_pk_bf16_f32 v74, v32, v33\n"
    "v_mfma_f32_16x16x32_bf16 v[220:223], a[124:127], v[88:91], v[220:223]\n"
    "s_waitcnt lgkmcnt(9)\n"
    "v_exp_f32_e32 v34, v34\n"
    "v_mfma_f32_16x16x32_bf16 v[40:43], a[64:67], a[0:3], v[92:95]\n"
    "ds_read_b64_tr_b16 a[114:115], v112 offset:4608\n"
    "v_mfma_f32_16x16x32_bf16 v[40:43], a[68:71], a[4:7], v[40:43]\n"
    "v_exp_f32_e32 v35, v35\n"
    "v_mfma_f32_16x16x32_bf16 v[40:43], a[72:75], a[8:11], v[40:43]\n"
    "ds_read_b64_tr_b16 a[116:117], v113 offset:512\n"
    "v_cvt_pk_bf16_f32 v75, v34, v35\n"
    "v_mfma_f32_16x16x32_bf16 v[40:43], a[76:79], a[12:15], v[40:43]\n"
    "v_exp_f32_e32 v36, v36\n"
    "v_mfma_f32_16x16x32_bf16 v[44:47], a[76:79], a[28:31], v[96:99]\n"
    "ds_read_b64_tr_b16 a[118:119], v113 offset:4608\n"
    "v_xor_b32_e32 v109, s40, v109\n"
    "v_mfma_f32_16x16x32_bf16 v[44:47], a[72:75], a[24:27], v[44:47]\n"
    "v_exp_f32_e32 v37, v37\n"
    "v_mfma_f32_16x16x32_bf16 v[44:47], a[68:71], a[20:23], v[44:47]\n"
    "s_add_u32 s42, s42, 0x4000\n"
    "ds_read_b64_tr_b16 a[120:121], v112 offset:768\n"
    "v_cvt_pk_bf16_f32 v78, v36, v37\n"
    "v_mfma_f32_16x16x32_bf16 v[44:47], a[64:67], a[16:19], v[44:47]\n"
    "s_and_b32 s42, s42, 0x3ffff\n"
    "v_exp_f32_e32 v38, v38\n"
    "v_mfma_f32_16x16x32_bf16 v[48:51], a[64:67], a[32:35], v[100:103]\n"
    "s_xor_b32 m0, m0, s41\n"
    "ds_read_b64_tr_b16 a[122:123], v112 offset:4864\n"
    "v_mfma_f32_16x16x32_bf16 v[48:51], a[68:71], a[36:39], v[48:51]\n"
    "v_exp_f32_e32 v39, v39\n"
    "v_mfma_f32_16x16x32_bf16 v[48:51], a[72:75], a[40:43], v[48:51]\n"
    "ds_read_b64_tr_b16 a[124:125], v113 offset:768\n"
    "v_cvt_pk_bf16_f32 v79, v38, v39\n"
    "v_mfma_f32_16x16x32_bf16 v[48:51], a[76:79], a[44:47], v[48:51]\n"
    "v_exp_f32_e32 v40, v40\n"
    "v_mfma_f32_16x16x32_bf16 v[52:55], a[80:83], a[0:3], v[92:95]\n"
    "buffer_load_dword v127, v1, s[64:67], 0 offen\n"
    "v_mfma_f32_16x16x32_bf16 v[52:55], a[84:87], a[4:7], v[52:55]\n"
    "v_exp_f32_e32 v41, v41\n"
    "v_mfma_f32_16x16x32_bf16 v[52:55], a[88:91], a[8:11], v[52:55]\n"
    "ds_read_b64_tr_b16 a[126:127], v113 offset:4864\n"
    "v_cvt_pk_bf16_f32 v80, v40, v41\n"
    "v_mfma_f32_16x16x32_bf16 v[52:55], a[92:95], a[12:15], v[52:55]\n"
    "s_waitcnt vmcnt(9)\n"
    "v_exp_f32_e32 v42, v42\n"
    "v_mfma_f32_16x16x32_bf16 v[56:59], a[92:95], a[28:31], v[96:99]\n"
    "s_barrier\n"
    "ds_read_b128 a[64:67], v108\n"
    "v_mfma_f32_16x16x32_bf16 v[56:59], a[88:91], a[24:27], v[56:59]\n"
    "v_exp_f32_e32 v43, v43\n"
    "v_mfma_f32_16x16x32_bf16 v[56:59], a[84:87], a[20:23], v[56:59]\n"
    "ds_read_b128 a[68:71], v108 offset:256\n"
    "v_cvt_pk_bf16_f32 v81, v42, v43\n"
    "v_mfma_f32_16x16x32_bf16 v[56:59], a[80:83], a[16:19], v[56:59]\n"
    "v_exp_f32_e32 v44, v44\n"
    "v_mfma_f32_16x16x32_bf16 v[60:63], a[80:83], a[32:35], v[100:103]\n"
    "ds_read_b128 a[72:75], v108 offset:512\n"
    "ds_read_b128 a[76:79], v108 offset:768\n"
    "v_mfma_f32_16x16x32_bf16 v[60:63], a[84:87], a[36:39], v[60:63]\n"
    "v_exp_f32_e32 v45, v45\n"
    "v_mfma_f32_16x16x32_bf16 v[60:63], a[88:91], a[40:43], v[60:63]\n"
    "buffer_load_dwordx4 v116, s[24:27], s42 offen nt lds\n"
    "v_cvt_pk_bf16_f32 v84, v44, v45\n"
    "v_mfma_f32_16x16x32_bf16 v[60:63], a[92:95], a[44:47], v[60:63]\n"
    "v_exp_f32_e32 v46, v46\n"
    "v_mfma_f32_16x16x32_bf16 a[128:131], a[144:147], v[64:67], a[128:131]\n"
    "ds_read_b128 a[80:83], v108 offset:4096\n"
    "v_mfma_f32_16x16x32_bf16 a[132:135], a[144:147], v[68:71], a[132:135]\n"
    "v_exp_f32_e32 v47, v47\n"
    "v_mfma_f32_16x16x32_bf16 a[136:139], a[144:147], v[72:75], a[136:139]\n"
    "ds_read_b128 a[84:87], v108 offset:4352\n"
    "v_cvt_pk_bf16_f32 v85, v46, v47\n"
    "v_mfma_f32_16x16x32_bf16 a[140:143], a[144:147], v[76:79], a[140:143]\n"
    "s_waitcnt lgkmcnt(6)\n"
    "v_exp_f32_e32 v48, v48\n"
    "v_mfma_f32_16x16x32_bf16 v[128:131], a[96:99], v[64:67], v[128:131]\n"
    "ds_read_b128 a[88:91], v108 offset:4608\n"
    "buffer_load_dwordx4 v117, s[24:27], s42 offen offset:1024 nt lds\n"
    "v_mfma_f32_16x16x32_bf16 v[160:163], a[96:99], v[68:71], v[160:163]\n"
    "v_exp_f32_e32 v49, v49\n"
    "v_mfma_f32_16x16x32_bf16 v[192:195], a[96:99], v[72:75], v[192:195]\n"
    "ds_read_b128 a[92:95], v108 offset:4864\n"
    "v_cvt_pk_bf16_f32 v88, v48, v49\n"
    "v_mfma_f32_16x16x32_bf16 v[224:227], a[96:99], v[76:79], v[224:227]\n"
    "s_add_u32 s68, s68, 0x180\n"
    "v_exp_f32_e32 v50, v50\n"
    "v_mfma_f32_16x16x32_bf16 v[132:135], a[100:103], v[64:67], v[132:135]\n"
    "buffer_load_dwordx4 v118, s[24:27], s42 offen offset:2048 nt lds\n"
    "v_mfma_f32_16x16x32_bf16 v[164:167], a[100:103], v[68:71], v[164:167]\n"
    "v_exp_f32_e32 v51, v51\n"
    "v_mfma_f32_16x16x32_bf16 v[196:199], a[100:103], v[72:75], v[196:199]\n"
    "buffer_load_dwordx4 v119, s[24:27], s42 offen offset:3072 nt lds\n"
    "v_cvt_pk_bf16_f32 v89, v50, v51\n"
    "v_mfma_f32_16x16x32_bf16 v[228:231], a[100:103], v[76:79], v[228:231]\n"
    "s_xor_b32 m0, m0, 0x4000\n"
    "v_exp_f32_e32 v52, v52\n"
    "v_mfma_f32_16x16x32_bf16 v[136:139], a[104:107], v[64:67], v[136:139]\n"
    "v_mfma_f32_16x16x32_bf16 v[168:171], a[104:107], v[68:71], v[168:171]\n"
    "v_exp_f32_e32 v53, v53\n"
    "v_mfma_f32_16x16x32_bf16 v[200:203], a[104:107], v[72:75], v[200:203]\n"
    "buffer_load_dwordx4 v116, s[28:31], s42 offen nt lds\n"
    "v_cvt_pk_bf16_f32 v82, v52, v53\n"
    "v_mfma_f32_16x16x32_bf16 v[232:235], a[104:107], v[76:79], v[232:235]\n"
    "v_exp_f32_e32 v54, v54\n"
    "v_mfma_f32_16x16x32_bf16 v[140:143], a[108:111], v[64:67], v[140:143]\n"
    "v_mfma_f32_16x16x32_bf16 v[172:175], a[108:111], v[68:71], v[172:175]\n"
    "v_exp_f32_e32 v55, v55\n"
    "v_mfma_f32_16x16x32_bf16 v[204:207], a[108:111], v[72:75], v[204:207]\n"
    "buffer_load_dwordx4 v118, s[28:31], s42 offen offset:1024 nt lds\n"
    "v_cvt_pk_bf16_f32 v83, v54, v55\n"
    "v_mfma_f32_16x16x32_bf16 v[236:239], a[108:111], v[76:79], v[236:239]\n"
    "v_exp_f32_e32 v56, v56\n"
    "v_mfma_f32_16x16x32_bf16 v[144:147], a[112:115], v[64:67], v[144:147]\n"
    "v_mfma_f32_16x16x32_bf16 v[176:179], a[112:115], v[68:71], v[176:179]\n"
    "s_memrealtime s[74:75]\n"
    "v_exp_f32_e32 v57, v57\n"
    "v_mfma_f32_16x16x32_bf16 v[208:211], a[112:115], v[72:75], v[208:211]\n"
    "buffer_load_dwordx4 v116, s[28:31], s42 offen offset:2048 nt lds\n"
    "v_cvt_pk_bf16_f32 v86, v56, v57\n"
    "v_mfma_f32_16x16x32_bf16 v[240:243], a[112:115], v[76:79], v[240:243]\n"
    "v_exp_f32_e32 v58, v58\n"
    "v_mfma_f32_16x16x32_bf16 v[148:151], a[116:119], v[64:67], v[148:151]\n"
    "v_mfma_f32_16x16x32_bf16 v[180:183], a[116:119], v[68:71], v[180:183]\n"
    "v_exp_f32_e32 v59, v59\n"
    "v_mfma_f32_16x16x32_bf16 v[212:215], a[116:119], v[72:75], v[212:215]\n"
    "buffer_load_dwordx4 v118, s[28:31], s42 offen offset:3072 nt lds\n"
    "v_cvt_pk_bf16_f32 v87, v58, v59\n"
    "v_mfma_f32_16x16x32_bf16 v[244:247], a[116:119], v[76:79], v[244:247]\n"
    "v_exp_f32_e32 v60, v60\n"
    "v_mfma_f32_16x16x32_bf16 v[152:155], a[120:123], v[64:67], v[152:155]\n"
    "v_mfma_f32_16x16x32_bf16 v[184:187], a[120:123], v[68:71], v[184:187]\n"
    "v_exp_f32_e32 v61, v61\n"
    "v_mfma_f32_16x16x32_bf16 v[216:219], a[120:123], v[72:75], v[216:219]\n"
    "v_cvt_pk_bf16_f32 v90, v60, v61\n"
    "v_mfma_f32_16x16x32_bf16 v[248:251], a[120:123], v[76:79], v[248:251]\n"
    "v_exp_f32_e32 v62, v62\n"
    "v_mfma_f32_16x16x32_bf16 v[156:159], a[124:127], v[64:67], v[156:159]\n"
    "v_mfma_f32_16x16x32_bf16 v[188:191], a[124:127], v[68:71], v[188:191]\n"
    "s_waitcnt lgkmcnt(0)\n"
    "v_exp_f32_e32 v63, v63\n"
    "v_mfma_f32_16x16x32_bf16 v[220:223], a[124:127], v[72:75], v[220:223]\n"
    "v_cvt_pk_bf16_f32 v91, v62, v63\n"
    "v_mfma_f32_16x16x32_bf16 v[252:255], a[124:127], v[76:79], v[252:255]\n"
    "s_sub_u32 s52, s73, s74\n"
    "s_cmp_gt_i32 s52, 0\n"
    "s_cbranch_scc0 L_npl%=\n"
    "L_pwl%=:\n"
    "s_nop 15\n"
    "s_sub_u32 s52, s52, 3\n"
    "s_cmp_gt_i32 s52, 0\n"
    "s_cbranch_scc1 L_pwl%=\n"
    "s_mov_b32 s74, s73\n"
    "L_npl%=:\n"
    "s_add_u32 s73, s74, 0x7d\n"
    "s_cmp_lg_u32 s43, 0\n"
    "s_cbranch_scc1 L_loop%=\n"
    "v_mfma_f32_16x16x32_bf16 v[8:11], a[64:67], a[0:3], v[92:95]\n"
    "ds_read_b64_tr_b16 a[96:97], v114\n"
    "ds_read_b64_tr_b16 a[98:99], v114 offset:4096\n"
    "v_mfma_f32_16x16x32_bf16 v[8:11], a[68:71], a[4:7], v[8:11]\n"
    "ds_read_b64_tr_b16 a[100:101], v115\n"
    "v_mfma_f32_16x16x32_bf16 v[8:11], a[72:75], a[8:11], v[8:11]\n"
    "ds_read_b64_tr_b16 a[102:103], v115 offset:4096\n"
    "ds_read_b64_tr_b16 a[104:105], v114 offset:256\n"
    "v_mfma_f32_16x16x32_bf16 v[8:11], a[76:79], a[12:15], v[8:11]\n"
    "ds_read_b64_tr_b16 a[106:107], v114 offset:4352\n"
    "v_mfma_f32_16x16x32_bf16 v[12:15], a[76:79], a[28:31], v[96:99]\n"
    "ds_read_b64_tr_b16 a[108:109], v115 offset:256\n"
    "ds_read_b64_tr_b16 a[110:111], v115 offset:4352\n"
    "v_mfma_f32_16x16x32_bf16 v[12:15], a[72:75], a[24:27], v[12:15]\n"
    "ds_read_b64_tr_b16 a[112:113], v114 offset:512\n"
    "v_mfma_f32_16x16x32_bf16 v[12:15], a[68:71], a[20:23], v[12:15]\n"
    "ds_read_b64_tr_b16 a[114:115], v114 offset:4608\n"
    "ds_read_b64_tr_b16 a[116:117], v115 offset:512\n"
    "v_mfma_f32_16x16x32_bf16 v[12:15], a[64:67], a[16:19], v[12:15]\n"
    "ds_read_b64_tr_b16 a[118:119], v115 offset:4608\n"
    "v_mfma_f32_16x16x32_bf16 v[16:19], a[64:67], a[32:35], v[100:103]\n"
    "v_exp_f32_e32 v8, v8\n"
    "v_mfma_f32_16x16x32_bf16 v[16:19], a[68:71], a[36:39], v[16:19]\n"
    "v_exp_f32_e32 v9, v9\n"
    "v_mfma_f32_16x16x32_bf16 v[16:19], a[72:75], a[40:43], v[16:19]\n"
    "v_exp_f32_e32 v10, v10\n"
    "v_mfma_f32_16x16x32_bf16 v[16:19], a[76:79], a[44:47], v[16:19]\n"
    "s_mov_b32 s51, m0\n"
    "v_cvt_pk_bf16_f32 v64, v8, v9\n"
    "v_mfma_f32_16x16x32_bf16 v[20:23], a[76:79], a[60:63], v[104:107]\n"
    "s_add_u32 s45, s69, 0\n"
    "v_exp_f32_e32 v11, v11\n"
    "v_mfma_f32_16x16x32_bf16 v[20:23], a[72:75], a[56:59], v[20:23]\n"
    "s_add_u32 m0, s55, 0x20000\n"
    "ds_read_b64_tr_b16 a[120:121], v114 offset:768\n"
    "v_cvt_pk_bf16_f32 v65, v10, v11\n"
    "v_mfma_f32_16x16x32_bf16 v[20:23], a[68:71], a[52:55], v[20:23]\n"
    "v_exp_f32_e32 v12, v12\n"
    "v_mfma_f32_16x16x32_bf16 v[20:23], a[64:67], a[48:51], v[20:23]\n"
    "buffer_load_dwordx4 v122, s[32:35], s45 offen lds\n"
    "v_mfma_f32_16x16x32_bf16 v[24:27], a[80:83], a[0:3], v[92:95]\n"
    "v_exp_f32_e32 v13, v13\n"
    "v_mfma_f32_16x16x32_bf16 v[24:27], a[84:87], a[4:7], v[24:27]\n"
    "ds_read_b64_tr_b16 a[122:123], v114 offset:4864\n"
    "v_cvt_pk_bf16_f32 v68, v12, v13\n"
    "v_mfma_f32_16x16x32_bf16 v[24:27], a[88:91], a[8:11], v[24:27]\n"
    "v_exp_f32_e32 v14, v14\n"
    "v_mfma_f32_16x16x32_bf16 v[24:27], a[92:95], a[12:15], v[24:27]\n"
    "buffer_load_dwordx4 v123, s[32:35], s45 offen offset:1024 lds\n"
    "v_mfma_f32_16x16x32_bf16 v[28:31], a[92:95], a[28:31], v[96:99]\n"
    "v_exp_f32_e32 v15, v15\n"
    "v_mfma_f32_16x16x32_bf16 v[28:31], a[88:91], a[24:27], v[28:31]\n"
    "ds_read_b64_tr_b16 a[124:125], v115 offset:768\n"
    "v_cvt_pk_bf16_f32 v69, v14, v15\n"
    "v_mfma_f32_16x16x32_bf16 v[28:31], a[84:87], a[20:23], v[28:31]\n"
    "v_exp_f32_e32 v16, v16\n"
    "v_mfma_f32_16x16x32_bf16 v[28:31], a[80:83], a[16:19], v[28:31]\n"
    "ds_read_b64_tr_b16 a[126:127], v115 offset:4864\n"
    "ds_read_b128 a[64:67], v109\n"
    "v_mfma_f32_16x16x32_bf16 v[32:35], a[80:83], a[32:35], v[100:103]\n"
    "v_exp_f32_e32 v17, v17\n"
    "v_mfma_f32_16x16x32_bf16 v[32:35], a[84:87], a[36:39], v[32:35]\n"
    "ds_read_b128 a[68:71], v109 offset:256\n"
    "v_cvt_pk_bf16_f32 v72, v16, v17\n"
    "v_mfma_f32_16x16x32_bf16 v[32:35], a[88:91], a[40:43], v[32:35]\n"
    "v_exp_f32_e32 v18, v18\n"
    "v_mfma_f32_16x16x32_bf16 v[32:35], a[92:95], a[44:47], v[32:35]\n"
    "ds_read_b128 a[72:75], v109 offset:512\n"
    "buffer_load_dwordx4 v124, s[32:35], s45 offen offset:2048 lds\n"
    "v_mfma_f32_16x16x32_bf16 v[36:39], a[92:95], a[60:63], v[104:107]\n"
    "v_exp_f32_e32 v19, v19\n"
    "v_mfma_f32_16x16x32_bf16 v[36:39], a[88:91], a[56:59], v[36:39]\n"
    "ds_read_b128 a[76:79], v109 offset:768\n"
    "v_cvt_pk_bf16_f32 v73, v18, v19\n"
    "v_mfma_f32_16x16x32_bf16 v[36:39], a[84:87], a[52:55], v[36:39]\n"
    "v_exp_f32_e32 v20, v20\n"
    "v_mfma_f32_16x16x32_bf16 v[36:39], a[80:83], a[48:51], v[36:39]\n"
    "v_xor_b32_e32 v112, s40, v112\n"
    "v_xor_b32_e32 v113, s40, v113\n"
    "v_mfma_f32_16x16x32_bf16 a[128:131], a[144:147], v[80:83], a[128:131]\n"
    "v_exp_f32_e32 v21, v21\n"
    "v_mfma_f32_16x16x32_bf16 a[132:135], a[144:147], v[84:87], a[132:135]\n"
    "ds_read_b128 a[80:83], v109 offset:4096\n"
    "v_cvt_pk_bf16_f32 v76, v20, v21\n"
    "v_mfma_f32_16x16x32_bf16 a[136:139], a[144:147], v[88:91], a[136:139]\n"
    "s_waitcnt lgkmcnt(5)\n"
    "v_exp_f32_e32 v22, v22\n"
    "v_mfma_f32_16x16x32_bf16 v[128:131], a[96:99], v[80:83], v[128:131]\n"
    "buffer_load_dwordx4 v125, s[32:35], s45 offen offset:3072 lds\n"
    "v_xor_b32_e32 v114, s40, v114\n"
    "v_mfma_f32_16x16x32_bf16 v[160:163], a[96:99], v[84:87], v[160:163]\n"
    "s_add_u32 s45, s70, 0\n"
    "v_exp_f32_e32 v23, v23\n"
    "v_mfma_f32_16x16x32_bf16 v[192:195], a[96:99], v[88:91], v[192:195]\n"
    "s_add_u32 m0, s55, 0x24000\n"
    "ds_read_b128 a[84:87], v109 offset:4352\n"
    "v_cvt_pk_bf16_f32 v77, v22, v23\n"
    "v_mfma_f32_16x16x32_bf16 v[132:135], a[100:103], v[80:83], v[132:135]\n"
    "v_exp_f32_e32 v24, v24\n"
    "v_mfma_f32_16x16x32_bf16 v[164:167], a[100:103], v[84:87], v[164:167]\n"
    "ds_read_b128 a[88:91], v109 offset:4608\n"
    "v_xor_b32_e32 v115, s40, v115\n"
    "v_mfma_f32_16x16x32_bf16 v[196:199], a[100:103], v[88:91], v[196:199]\n"
    "v_exp_f32_e32 v25, v25\n"
    "v_mfma_f32_16x16x32_bf16 v[136:139], a[104:107], v[80:83], v[136:139]\n"
    "ds_read_b128 a[92:95], v109 offset:4864\n"
    "v_cvt_pk_bf16_f32 v66, v24, v25\n"
    "v_mfma_f32_16x16x32_bf16 v[168:171], a[104:107], v[84:87], v[168:171]\n"
    "v_exp_f32_e32 v26, v26\n"
    "v_mfma_f32_16x16x32_bf16 v[200:203], a[104:107], v[88:91], v[200:203]\n"
    "s_xor_b32 s40, s40, 0x18000\n"
    "buffer_load_dwordx4 v122, s[32:35], s45 offen lds\n"
    "v_mfma_f32_16x16x32_bf16 v[140:143], a[108:111], v[80:83], v[140:143]\n"
    "s_xor_b32 s41, s41, 0x18000\n"
    "v_exp_f32_e32 v27, v27\n"
    "v_mfma_f32_16x16x32_bf16 v[172:175], a[108:111], v[84:87], v[172:175]\n"
    "ds_read_b64_tr_b16 a[96:97], v112\n"
    "v_cvt_pk_bf16_f32 v67, v26, v27\n"
    "v_mfma_f32_16x16x32_bf16 v[204:207], a[108:111], v[88:91], v[204:207]\n"
    "v_exp_f32_e32 v28, v28\n"
    "v_mfma_f32_16x16x32_bf16 v[144:147], a[112:115], v[80:83], v[144:147]\n"
    "ds_read_b64_tr_b16 a[98:99], v112 offset:4096\n"
    "ds_read_b64_tr_b16 a[100:101], v113\n"
    "v_mfma_f32_16x16x32_bf16 v[176:179], a[112:115], v[84:87], v[176:179]\n"
    "v_exp_f32_e32 v29, v29\n"
    "v_mfma_f32_16x16x32_bf16 v[208:211], a[112:115], v[88:91], v[208:211]\n"
    "ds_read_b64_tr_b16 a[102:103], v113 offset:4096\n"
    "v_cvt_pk_bf16_f32 v70, v28, v29\n"
    "v_mfma_f32_16x16x32_bf16 v[148:151], a[116:119], v[80:83], v[148:151]\n"
    "v_exp_f32_e32 v30, v30\n"
    "v_mfma_f32_16x16x32_bf16 v[180:183], a[116:119], v[84:87], v[180:183]\n"
    "ds_read_b64_tr_b16 a[104:105], v112 offset:256\n"
    "ds_read_b64_tr_b16 a[106:107], v112 offset:4352\n"
    "v_mfma_f32_16x16x32_bf16 v[212:215], a[116:119], v[88:91], v[212:215]\n"
    "v_exp_f32_e32 v31, v31\n"
    "v_mfma_f32_16x16x32_bf16 v[152:155], a[120:123], v[80:83], v[152:155]\n"
    "ds_read_b64_tr_b16 a[108:109], v113 offset:256\n"
    "v_cvt_pk_bf16_f32 v71, v30, v31\n"
    "v_mfma_f32_16x16x32_bf16 v[184:187], a[120:123], v[84:87], v[184:187]\n"
    "v_exp_f32_e32 v32, v32\n"
    "v_mfma_f32_16x16x32_bf16 v[216:219], a[120:123], v[88:91], v[216:219]\n"
    "buffer_load_dwordx4 v123, s[32:35], s45 offen offset:1024 lds\n"
    "ds_read_b64_tr_b16 a[110:111], v113 offset:4352\n"
    "v_mfma_f32_16x16x32_bf16 v[156:159], a[124:127], v[80:83], v[156:159]\n"
    "v_exp_f32_e32 v33, v33\n"
    "v_mfma_f32_16x16x32_bf16 v[188:191], a[124:127], v[84:87], v[188:191]\n"
    "ds_read_b64_tr_b16 a[112:113], v112 offset:512\n"
    "v_cvt_pk_bf16_f32 v74, v32, v33\n"
    "v_mfma_f32_16x16x32_bf16 v[220:223], a[124:127], v[88:91], v[220:223]\n"
    "s_waitcnt lgkmcnt(9)\n"
    "v_exp_f32_e32 v34, v34\n"
    "v_mfma_f32_16x16x32_bf16 v[40:43], a[64:67], a[0:3], v[92:95]\n"
    "buffer_load_dwordx4 v124, s[32:35], s45 offen offset:2048 lds\n"
    "ds_read_b64_tr_b16 a[114:115], v112 offset:4608\n"
    "v_mfma_f32_16x16x32_bf16 v[40:43], a[68:71], a[4:7], v[40:43]\n"
    "v_exp_f32_e32 v35, v35\n"
    "v_mfma_f32_16x16x32_bf16 v[40:43], a[72:75], a[8:11], v[40:43]\n"
    "ds_read_b64_tr_b16 a[116:117], v113 offset:512\n"
    "v_cvt_pk_bf16_f32 v75, v34, v35\n"
    "v_mfma_f32_16x16x32_bf16 v[40:43], a[76:79], a[12:15], v[40:43]\n"
    "v_exp_f32_e32 v36, v36\n"
    "v_mfma_f32_16x16x32_bf16 v[44:47], a[76:79], a[28:31], v[96:99]\n"
    "ds_read_b64_tr_b16 a[118:119], v113 offset:4608\n"
    "buffer_load_dwordx4 v125, s[32:35], s45 offen offset:3072 lds\n"
    "v_mfma_f32_16x16x32_bf16 v[44:47], a[72:75], a[24:27], v[44:47]\n"
    "s_mov_b32 m0, s51\n"
    "v_exp_f32_e32 v37, v37\n"
    "v_mfma_f32_16x16x32_bf16 v[44:47], a[68:71], a[20:23], v[44:47]\n"
    "s_add_u32 s42, s42, 0x4000\n"
    "ds_read_b64_tr_b16 a[120:121], v112 offset:768\n"
    "v_cvt_pk_bf16_f32 v78, v36, v37\n"
    "v_mfma_f32_16x16x32_bf16 v[44:47], a[64:67], a[16:19], v[44:47]\n"
    "s_and_b32 s42, s42, 0x3ffff\n"
    "v_exp_f32_e32 v38, v38\n"
    "v_mfma_f32_16x16x32_bf16 v[48:51], a[64:67], a[32:35], v[100:103]\n"
    "s_xor_b32 m0, m0, s41\n"
    "ds_read_b64_tr_b16 a[122:123], v112 offset:4864\n"
    "v_xor_b32_e32 v108, s40, v108\n"
    "v_mfma_f32_16x16x32_bf16 v[48:51], a[68:71], a[36:39], v[48:51]\n"
    "v_exp_f32_e32 v39, v39\n"
    "v_mfma_f32_16x16x32_bf16 v[48:51], a[72:75], a[40:43], v[48:51]\n"
    "ds_read_b64_tr_b16 a[124:125], v113 offset:768\n"
    "v_cvt_pk_bf16_f32 v79, v38, v39\n"
    "v_mfma_f32_16x16x32_bf16 v[48:51], a[76:79], a[44:47], v[48:51]\n"
    "v_exp_f32_e32 v40, v40\n"
    "v_mfma_f32_16x16x32_bf16 v[52:55], a[80:83], a[0:3], v[92:95]\n"
    "v_xor_b32_e32 v109, s40, v109\n"
    "v_lshrrev_b32_e32 v1, 5, v0\n"
    "v_mfma_f32_16x16x32_bf16 v[52:55], a[84:87], a[4:7], v[52:55]\n"
    "v_exp_f32_e32 v41, v41\n"
    "v_mfma_f32_16x16x32_bf16 v[52:55], a[88:91], a[8:11], v[52:55]\n"
    "ds_read_b64_tr_b16 a[126:127], v113 offset:4864\n"
    "v_cvt_pk_bf16_f32 v80, v40, v41\n"
    "v_mfma_f32_16x16x32_bf16 v[52:55], a[92:95], a[12:15], v[52:55]\n"
    "s_waitcnt vmcnt(17)\n"
    "v_exp_f32_e32 v42, v42\n"
    "v_mfma_f32_16x16x32_bf16 v[56:59], a[92:95], a[28:31], v[96:99]\n"
    "s_barrier\n"
    "ds_read_b128 a[64:67], v108\n"
    "v_mul_u32_u24_e32 v1, 0x400, v1\n"
    "v_mfma_f32_16x16x32_bf16 v[56:59], a[88:91], a[24:27], v[56:59]\n"
    "v_exp_f32_e32 v43, v43\n"
    "v_mfma_f32_16x16x32_bf16 v[56:59], a[84:87], a[20:23], v[56:59]\n"
    "ds_read_b128 a[68:71], v108 offset:256\n"
    "v_cvt_pk_bf16_f32 v81, v42, v43\n"
    "v_mfma_f32_16x16x32_bf16 v[56:59], a[80:83], a[16:19], v[56:59]\n"
    "v_exp_f32_e32 v44, v44\n"
    "v_mfma_f32_16x16x32_bf16 v[60:63], a[80:83], a[32:35], v[100:103]\n"
    "ds_read_b128 a[72:75], v108 offset:512\n"
    "ds_read_b128 a[76:79], v108 offset:768\n"
    "v_mfma_f32_16x16x32_bf16 v[60:63], a[84:87], a[36:39], v[60:63]\n"
    "v_exp_f32_e32 v45, v45\n"
    "v_mfma_f32_16x16x32_bf16 v[60:63], a[88:91], a[40:43], v[60:63]\n"
    "buffer_load_dwordx4 v116, s[24:27], s42 offen nt lds\n"
    "v_cvt_pk_bf16_f32 v84, v44, v45\n"
    "v_mfma_f32_16x16x32_bf16 v[60:63], a[92:95], a[44:47], v[60:63]\n"
    "v_exp_f32_e32 v46, v46\n"
    "v_mfma_f32_16x16x32_bf16 a[128:131], a[144:147], v[64:67], a[128:131]\n"
    "ds_read_b128 a[80:83], v108 offset:4096\n"
    "v_bfe_u32 v2, v0, 2, 3\n"
    "v_mfma_f32_16x16x32_bf16 a[132:135], a[144:147], v[68:71], a[132:135]\n"
    "v_exp_f32_e32 v47, v47\n"
    "v_mfma_f32_16x16x32_bf16 a[136:139], a[144:147], v[72:75], a[136:139]\n"
    "ds_read_b128 a[84:87], v108 offset:4352\n"
    "v_cvt_pk_bf16_f32 v85, v46, v47\n"
    "v_mfma_f32_16x16x32_bf16 a[140:143], a[144:147], v[76:79], a[140:143]\n"
    "s_waitcnt lgkmcnt(6)\n"
    "v_exp_f32_e32 v48, v48\n"
    "v_mfma_f32_16x16x32_bf16 v[128:131], a[96:99], v[64:67], v[128:131]\n"
    "ds_read_b128 a[88:91], v108 offset:4608\n"
    "buffer_load_dwordx4 v117, s[24:27], s42 offen offset:1024 nt lds\n"
    "v_mfma_f32_16x16x32_bf16 v[160:163], a[96:99], v[68:71], v[160:163]\n"
    "v_exp_f32_e32 v49, v49\n"
    "v_mfma_f32_16x16x32_bf16 v[192:195], a[96:99], v[72:75], v[192:195]\n"
    "ds_read_b128 a[92:95], v108 offset:4864\n"
    "v_cvt_pk_bf16_f32 v88, v48, v49\n"
    "v_mfma_f32_16x16x32_bf16 v[224:227], a[96:99], v[76:79], v[224:227]\n"
    "v_exp_f32_e32 v50, v50\n"
    "v_mfma_f32_16x16x32_bf16 v[132:135], a[100:103], v[64:67], v[132:135]\n"
    "buffer_load_dwordx4 v118, s[24:27], s42 offen offset:2048 nt lds\n"
    "v_lshlrev_b32_e32 v2, 6, v2\n"
    "v_mfma_f32_16x16x32_bf16 v[164:167], a[100:103], v[68:71], v[164:167]\n"
    "v_exp_f32_e32 v51, v51\n"
    "v_mfma_f32_16x16x32_bf16 v[196:199], a[100:103], v[72:75], v[196:199]\n"
    "buffer_load_dwordx4 v119, s[24:27], s42 offen offset:3072 nt lds\n"
    "v_cvt_pk_bf16_f32 v89, v50, v51\n"
    "v_mfma_f32_16x16x32_bf16 v[228:231], a[100:103], v[76:79], v[228:231]\n"
    "s_xor_b32 m0, m0, 0x4000\n"
    "v_exp_f32_e32 v52, v52\n"
    "v_mfma_f32_16x16x32_bf16 v[136:139], a[104:107], v[64:67], v[136:139]\n"
    "v_bfe_u32 v3, v0, 4, 1\n"
    "v_lshlrev_b32_e32 v3, 5, v3\n"
    "v_mfma_f32_16x16x32_bf16 v[168:171], a[104:107], v[68:71], v[168:171]\n"
    "v_exp_f32_e32 v53, v53\n"
    "v_mfma_f32_16x16x32_bf16 v[200:203], a[104:107], v[72:75], v[200:203]\n"
    "buffer_load_dwordx4 v116, s[28:31], s42 offen nt lds\n"
    "v_cvt_pk_bf16_f32 v82, v52, v53\n"
    "v_mfma_f32_16x16x32_bf16 v[232:235], a[104:107], v[76:79], v[232:235]\n"
    "v_exp_f32_e32 v54, v54\n"
    "v_mfma_f32_16x16x32_bf16 v[140:143], a[108:111], v[64:67], v[140:143]\n"
    "v_add3_u32 v1, v1, v2, v3\n"
    "v_and_b32_e32 v2, 3, v0\n"
    "v_mfma_f32_16x16x32_bf16 v[172:175], a[108:111], v[68:71], v[172:175]\n"
    "v_exp_f32_e32 v55, v55\n"
    "v_mfma_f32_16x16x32_bf16 v[204:207], a[108:111], v[72:75], v[204:207]\n"
    "buffer_load_dwordx4 v118, s[28:31], s42 offen offset:1024 nt lds\n"
    "v_cvt_pk_bf16_f32 v83, v54, v55\n"
    "v_mfma_f32_16x16x32_bf16 v[236:239], a[108:111], v[76:79], v[236:239]\n"
    "v_exp_f32_e32 v56, v56\n"
    "v_mfma_f32_16x16x32_bf16 v[144:147], a[112:115], v[64:67], v[144:147]\n"
    "v_lshl_add_u32 v3, v2, 3, v1\n"
    "v_add_u32_e32 v3, s55, v3\n"
    "v_mfma_f32_16x16x32_bf16 v[176:179], a[112:115], v[68:71], v[176:179]\n"
    "s_memrealtime s[74:75]\n"
    "v_exp_f32_e32 v57, v57\n"
    "v_mfma_f32_16x16x32_bf16 v[208:211], a[112:115], v[72:75], v[208:211]\n"
    "buffer_load_dwordx4 v116, s[28:31], s42 offen offset:2048 nt lds\n"
    "v_cvt_pk_bf16_f32 v86, v56, v57\n"
    "v_mfma_f32_16x16x32_bf16 v[240:243], a[112:115], v[76:79], v[240:243]\n"
    "v_exp_f32_e32 v58, v58\n"
    "v_mfma_f32_16x16x32_bf16 v[148:151], a[116:119], v[64:67], v[148:151]\n"
    "v_xor_b32_e32 v4, 32, v3\n"
    "v_add_u32_e32 v5, 0x20000, v3\n"
    "v_mfma_f32_16x16x32_bf16 v[180:183], a[116:119], v[68:71], v[180:183]\n"
    "v_exp_f32_e32 v59, v59\n"
    "v_mfma_f32_16x16x32_bf16 v[212:215], a[116:119], v[72:75], v[212:215]\n"
    "buffer_load_dwordx4 v118, s[28:31], s42 offen offset:3072 nt lds\n"
    "v_cvt_pk_bf16_f32 v87, v58, v59\n"
    "v_mfma_f32_16x16x32_bf16 v[244:247], a[116:119], v[76:79], v[244:247]\n"
    "v_exp_f32_e32 v60, v60\n"
    "v_mfma_f32_16x16x32_bf16 v[152:155], a[120:123], v[64:67], v[152:155]\n"
    "v_add_u32_e32 v6, 0x20000, v4\n"
    "v_mfma_f32_16x16x32_bf16 v[184:187], a[120:123], v[68:71], v[184:187]\n"
    "v_exp_f32_e32 v61, v61\n"
    "v_mfma_f32_16x16x32_bf16 v[216:219], a[120:123], v[72:75], v[216:219]\n"
    "v_cvt_pk_bf16_f32 v90, v60, v61\n"
    "v_mfma_f32_16x16x32_bf16 v[248:251], a[120:123], v[76:79], v[248:251]\n"
    "v_exp_f32_e32 v62, v62\n"
    "v_mfma_f32_16x16x32_bf16 v[156:159], a[124:127], v[64:67], v[156:159]\n"
    "v_mfma_f32_16x16x32_bf16 v[188:191], a[124:127], v[68:71], v[188:191]\n"
    "s_waitcnt lgkmcnt(0)\n"
    "v_exp_f32_e32 v63, v63\n"
    "v_mfma_f32_16x16x32_bf16 v[220:223], a[124:127], v[72:75], v[220:223]\n"
    "v_cvt_pk_bf16_f32 v91, v62, v63\n"
    "v_mfma_f32_16x16x32_bf16 v[252:255], a[124:127], v[76:79], v[252:255]\n"
    "s_sub_u32 s52, s73, s74\n"
    "s_cmp_gt_i32 s52, 0\n"
    "s_cbranch_scc0 L_np12%=\n"
    "L_pw12%=:\n"
    "s_nop 15\n"
    "s_sub_u32 s52, s52, 3\n"
    "s_cmp_gt_i32 s52, 0\n"
    "s_cbranch_scc1 L_pw12%=\n"
    "s_mov_b32 s74, s73\n"
    "L_np12%=:\n"
    "s_add_u32 s73, s74, 0x7d\n"
    "v_mfma_f32_16x16x32_bf16 v[8:11], a[64:67], a[0:3], v[92:95]\n"
    "ds_read_b64_tr_b16 a[96:97], v114\n"
    "ds_read_b64_tr_b16 a[98:99], v114 offset:4096\n"
    "v_mfma_f32_16x16x32_bf16 v[8:11], a[68:71], a[4:7], v[8:11]\n"
    "s_waitcnt vmcnt(8)\n"
    "ds_read_b64_tr_b16 a[100:101], v115\n"
    "v_mfma_f32_16x16x32_bf16 v[8:11], a[72:75], a[8:11], v[8:11]\n"
    "ds_read_b64_tr_b16 a[102:103], v115 offset:4096\n"
    "ds_read_b64_tr_b16 a[104:105], v114 offset:256\n"
    "v_mfma_f32_16x16x32_bf16 v[8:11], a[76:79], a[12:15], v[8:11]\n"
    "ds_read_b64_tr_b16 a[106:107], v114 offset:4352\n"
    "ds_read_b64_tr_b16 a[160:161], v5\n"
    "v_mfma_f32_16x16x32_bf16 v[12:15], a[76:79], a[28:31], v[96:99]\n"
    "ds_read_b64_tr_b16 a[108:109], v115 offset:256\n"
    "ds_read_b64_tr_b16 a[110:111], v115 offset:4352\n"
    "v_mfma_f32_16x16x32_bf16 v[12:15], a[72:75], a[24:27], v[12:15]\n"
    "ds_read_b64_tr_b16 a[112:113], v114 offset:512\n"
    "ds_read_b64_tr_b16 a[162:163], v5 offset:2048\n"
    "v_mfma_f32_16x16x32_bf16 v[12:15], a[68:71], a[20:23], v[12:15]\n"
    "ds_read_b64_tr_b16 a[114:115], v114 offset:4608\n"
    "ds_read_b64_tr_b16 a[116:117], v115 offset:512\n"
    "v_mfma_f32_16x16x32_bf16 v[12:15], a[64:67], a[16:19], v[12:15]\n"
    "ds_read_b64_tr_b16 a[118:119], v115 offset:4608\n"
    "ds_read_b64_tr_b16 a[164:165], v6\n"
    "v_mfma_f32_16x16x32_bf16 v[16:19], a[64:67], a[32:35], v[100:103]\n"
    "v_exp_f32_e32 v8, v8\n"
    "v_mfma_f32_16x16x32_bf16 v[16:19], a[68:71], a[36:39], v[16:19]\n"
    "v_exp_f32_e32 v9, v9\n"
    "v_mfma_f32_16x16x32_bf16 v[16:19], a[72:75], a[40:43], v[16:19]\n"
    "v_exp_f32_e32 v10, v10\n"
    "v_mfma_f32_16x16x32_bf16 v[16:19], a[76:79], a[44:47], v[16:19]\n"
    "ds_read_b64_tr_b16 a[166:167], v6 offset:2048\n"
    "v_cvt_pk_bf16_f32 v64, v8, v9\n"
    "v_mfma_f32_16x16x32_bf16 v[20:23], a[76:79], a[60:63], v[104:107]\n"
    "v_exp_f32_e32 v11, v11\n"
    "v_mfma_f32_16x16x32_bf16 v[20:23], a[72:75], a[56:59], v[20:23]\n"
    "ds_read_b64_tr_b16 a[120:121], v114 offset:768\n"
    "v_cvt_pk_bf16_f32 v65, v10, v11\n"
    "v_mfma_f32_16x16x32_bf16 v[20:23], a[68:71], a[52:55], v[20:23]\n"
    "v_exp_f32_e32 v12, v12\n"
    "v_mfma_f32_16x16x32_bf16 v[20:23], a[64:67], a[48:51], v[20:23]\n"
    "ds_read_b64_tr_b16 a[168:169], v5 offset:512\n"
    "ds_read_b64_tr_b16 a[170:171], v5 offset:2560\n"
    "v_mfma_f32_16x16x32_bf16 v[24:27], a[80:83], a[0:3], v[92:95]\n"
    "v_exp_f32_e32 v13, v13\n"
    "v_mfma_f32_16x16x32_bf16 v[24:27], a[84:87], a[4:7], v[24:27]\n"
    "ds_read_b64_tr_b16 a[122:123], v114 offset:4864\n"
    "v_cvt_pk_bf16_f32 v68, v12, v13\n"
    "v_mfma_f32_16x16x32_bf16 v[24:27], a[88:91], a[8:11], v[24:27]\n"
    "v_exp_f32_e32 v14, v14\n"
    "v_mfma_f32_16x16x32_bf16 v[24:27], a[92:95], a[12:15], v[24:27]\n"
    "ds_read_b64_tr_b16 a[172:173], v6 offset:512\n"
    "ds_read_b64_tr_b16 a[174:175], v6 offset:2560\n"
    "v_mfma_f32_16x16x32_bf16 v[28:31], a[92:95], a[28:31], v[96:99]\n"
    "v_exp_f32_e32 v15, v15\n"
    "v_mfma_f32_16x16x32_bf16 v[28:31], a[88:91], a[24:27], v[28:31]\n"
    "ds_read_b64_tr_b16 a[124:125], v115 offset:768\n"
    "v_cvt_pk_bf16_f32 v69, v14, v15\n"
    "v_mfma_f32_16x16x32_bf16 v[28:31], a[84:87], a[20:23], v[28:31]\n"
    "v_exp_f32_e32 v16, v16\n"
    "v_mfma_f32_16x16x32_bf16 v[28:31], a[80:83], a[16:19], v[28:31]\n"
    "ds_read_b64_tr_b16 a[126:127], v115 offset:4864\n"
    "ds_read_b128 a[64:67], v109\n"
    "v_mfma_f32_16x16x32_bf16 v[32:35], a[80:83], a[32:35], v[100:103]\n"
    "v_exp_f32_e32 v17, v17\n"
    "v_mfma_f32_16x16x32_bf16 v[32:35], a[84:87], a[36:39], v[32:35]\n"
    "ds_read_b128 a[68:71], v109 offset:256\n"
    "v_cvt_pk_bf16_f32 v72, v16, v17\n"
    "v_mfma_f32_16x16x32_bf16 v[32:35], a[88:91], a[40:43], v[32:35]\n"
    "v_exp_f32_e32 v18, v18\n"
    "v_mfma_f32_16x16x32_bf16 v[32:35], a[92:95], a[44:47], v[32:35]\n"
    "ds_read_b128 a[72:75], v109 offset:512\n"
    "ds_read_b64_tr_b16 a[176:177], v5 offset:16384\n"
    "v_mfma_f32_16x16x32_bf16 v[36:39], a[92:95], a[60:63], v[104:107]\n"
    "v_exp_f32_e32 v19, v19\n"
    "v_mfma_f32_16x16x32_bf16 v[36:39], a[88:91], a[56:59], v[36:39]\n"
    "ds_read_b128 a[76:79], v109 offset:768\n"
    "v_cvt_pk_bf16_f32 v73, v18, v19\n"
    "v_mfma_f32_16x16x32_bf16 v[36:39], a[84:87], a[52:55], v[36:39]\n"
    "s_waitcnt lgkmcnt(7)\n"
    "v_exp_f32_e32 v20, v20\n"
    "v_mfma_f32_16x16x32_bf16 v[36:39], a[80:83], a[48:51], v[36:39]\n"
    "ds_read_b64_tr_b16 a[178:179], v5 offset:18432\n"
    "ds_read_b64_tr_b16 a[180:181], v6 offset:16384\n"
    "v_mfma_f32_16x16x32_bf16 a[128:131], a[144:147], v[80:83], a[128:131]\n"
    "s_add_u32 s42, s71, 0\n"
    "v_exp_f32_e32 v21, v21\n"
    "v_mfma_f32_16x16x32_bf16 a[132:135], a[144:147], v[84:87], a[132:135]\n"
    "s_add_u32 m0, s55, 0x20000\n"
    "ds_read_b128 a[80:83], v109 offset:4096\n"
    "v_cvt_pk_bf16_f32 v76, v20, v21\n"
    "v_mfma_f32_16x16x32_bf16 a[136:139], a[144:147], v[88:91], a[136:139]\n"
    "s_waitcnt lgkmcnt(8)\n"
    "v_exp_f32_e32 v22, v22\n"
    "v_mfma_f32_16x16x32_bf16 v[128:131], a[96:99], v[80:83], v[128:131]\n"
    "v_xor_b32_e32 v112, s40, v112\n"
    "v_xor_b32_e32 v113, s40, v113\n"
    "v_mfma_f32_16x16x32_bf16 v[160:163], a[96:99], v[84:87], v[160:163]\n"
    "v_exp_f32_e32 v23, v23\n"
    "v_mfma_f32_16x16x32_bf16 v[192:195], a[96:99], v[88:91], v[192:195]\n"
    "ds_read_b128 a[84:87], v109 offset:4352\n"
    "v_cvt_pk_bf16_f32 v77, v22, v23\n"
    "v_mfma_f32_16x16x32_bf16 v[132:135], a[100:103], v[80:83], v[132:135]\n"
    "v_exp_f32_e32 v24, v24\n"
    "v_mfma_f32_16x16x32_bf16 v[164:167], a[100:103], v[84:87], v[164:167]\n"
    "ds_read_b128 a[88:91], v109 offset:4608\n"
    "v_xor_b32_e32 v114, s40, v114\n"
    "v_mfma_f32_16x16x32_bf16 v[196:199], a[100:103], v[88:91], v[196:199]\n"
    "v_exp_f32_e32 v25, v25\n"
    "v_mfma_f32_16x16x32_bf16 v[136:139], a[104:107], v[80:83], v[136:139]\n"
    "v_cvt_pk_bf16_f32 v66, v24, v25\n"
    "v_xor_b32_e32 v115, s40, v115\n"
    "v_mfma_f32_16x16x32_bf16 v[168:171], a[104:107], v[84:87], v[168:171]\n"
    "v_exp_f32_e32 v26, v26\n"
    "v_mfma_f32_16x16x32_bf16 v[200:203], a[104:107], v[88:91], v[200:203]\n"
    "s_xor_b32 s40, s40, 0x18000\n"
    "ds_read_b128 a[92:95], v109 offset:4864\n"
    "ds_read_b64_tr_b16 a[182:183], v6 offset:18432\n"
    "ds_read_b64_tr_b16 a[184:185], v5 offset:16896\n"
    "v_mfma_f32_16x16x32_bf16 v[140:143], a[108:111], v[80:83], v[140:143]\n"
    "v_exp_f32_e32 v27, v27\n"
    "v_mfma_f32_16x16x32_bf16 v[172:175], a[108:111], v[84:87], v[172:175]\n"
    "ds_read_b64_tr_b16 a[96:97], v112\n"
    "v_cvt_pk_bf16_f32 v67, v26, v27\n"
    "v_mfma_f32_16x16x32_bf16 v[204:207], a[108:111], v[88:91], v[204:207]\n"
    "v_exp_f32_e32 v28, v28\n"
    "v_mfma_f32_16x16x32_bf16 v[144:147], a[112:115], v[80:83], v[144:147]\n"
    "ds_read_b64_tr_b16 a[98:99], v112 offset:4096\n"
    "ds_read_b64_tr_b16 a[100:101], v113\n"
    "v_mfma_f32_16x16x32_bf16 v[176:179], a[112:115], v[84:87], v[176:179]\n"
    "v_exp_f32_e32 v29, v29\n"
    "v_mfma_f32_16x16x32_bf16 v[208:211], a[112:115], v[88:91], v[208:211]\n"
    "ds_read_b64_tr_b16 a[102:103], v113 offset:4096\n"
    "v_cvt_pk_bf16_f32 v70, v28, v29\n"
    "v_mfma_f32_16x16x32_bf16 v[148:151], a[116:119], v[80:83], v[148:151]\n"
    "v_exp_f32_e32 v30, v30\n"
    "v_mfma_f32_16x16x32_bf16 v[180:183], a[116:119], v[84:87], v[180:183]\n"
    "ds_read_b64_tr_b16 a[104:105], v112 offset:256\n"
    "ds_read_b64_tr_b16 a[186:187], v5 offset:18944\n"
    "ds_read_b64_tr_b16 a[188:189], v6 offset:16896\n"
    "ds_read_b64_tr_b16 a[190:191], v6 offset:18944\n"
    "buffer_load_dwordx4 v122, s[32:35], s42 offen lds\n"
    "v_mfma_f32_16x16x32_bf16 v[212:215], a[116:119], v[88:91], v[212:215]\n"
    "v_exp_f32_e32 v31, v31\n"
    "v_mfma_f32_16x16x32_bf16 v[152:155], a[120:123], v[80:83], v[152:155]\n"
    "ds_read_b64_tr_b16 a[106:107], v112 offset:4352\n"
    "v_cvt_pk_bf16_f32 v71, v30, v31\n"
    "v_mfma_f32_16x16x32_bf16 v[184:187], a[120:123], v[84:87], v[184:187]\n"
    "v_exp_f32_e32 v32, v32\n"
    "v_mfma_f32_16x16x32_bf16 v[216:219], a[120:123], v[88:91], v[216:219]\n"
    "ds_read_b64_tr_b16 a[108:109], v113 offset:256\n"
    "buffer_load_dwordx4 v123, s[32:35], s42 offen offset:1024 lds\n"
    "v_mfma_f32_16x16x32_bf16 v[156:159], a[124:127], v[80:83], v[156:159]\n"
    "s_waitcnt lgkmcnt(2)\n"
    "v_exp_f32_e32 v33, v33\n"
    "v_mfma_f32_16x16x32_bf16 v[188:191], a[124:127], v[84:87], v[188:191]\n"
    "ds_read_b64_tr_b16 a[110:111], v113 offset:4352\n"
    "v_cvt_pk_bf16_f32 v74, v32, v33\n"
    "v_mfma_f32_16x16x32_bf16 v[220:223], a[124:127], v[88:91], v[220:223]\n"
    "s_waitcnt lgkmcnt(13)\n"
    "v_exp_f32_e32 v34, v34\n"
    "v_mfma_f32_16x16x32_bf16 v[40:43], a[64:67], a[0:3], v[92:95]\n"
    "ds_read_b64_tr_b16 a[112:113], v112 offset:512\n"
    "buffer_load_dwordx4 v124, s[32:35], s42 offen offset:2048 lds\n"
    "v_mfma_f32_16x16x32_bf16 v[40:43], a[68:71], a[4:7], v[40:43]\n"
    "v_exp_f32_e32 v35, v35\n"
    "v_mfma_f32_16x16x32_bf16 v[40:43], a[72:75], a[8:11], v[40:43]\n"
    "ds_read_b64_tr_b16 a[114:115], v112 offset:4608\n"
    "v_cvt_pk_bf16_f32 v75, v34, v35\n"
    "v_mfma_f32_16x16x32_bf16 v[40:43], a[76:79], a[12:15], v[40:43]\n"
    "v_exp_f32_e32 v36, v36\n"
    "v_mfma_f32_16x16x32_bf16 v[44:47], a[76:79], a[28:31], v[96:99]\n"
    "ds_read_b64_tr_b16 a[116:117], v113 offset:512\n"
    "buffer_load_dwordx4 v125, s[32:35], s42 offen offset:3072 lds\n"
    "v_mfma_f32_16x16x32_bf16 v[44:47], a[72:75], a[24:27], v[44:47]\n"
    "s_add_u32 s42, s72, 0\n"
    "v_exp_f32_e32 v37, v37\n"
    "v_mfma_f32_16x16x32_bf16 v[44:47], a[68:71], a[20:23], v[44:47]\n"
    "s_add_u32 m0, s55, 0x24000\n"
    "ds_read_b64_tr_b16 a[118:119], v113 offset:4608\n"
    "v_cvt_pk_bf16_f32 v78, v36, v37\n"
    "v_mfma_f32_16x16x32_bf16 v[44:47], a[64:67], a[16:19], v[44:47]\n"
    "v_exp_f32_e32 v38, v38\n"
    "v_mfma_f32_16x16x32_bf16 v[48:51], a[64:67], a[32:35], v[100:103]\n"
    "ds_read_b64_tr_b16 a[120:121], v112 offset:768\n"
    "ds_read_b64_tr_b16 a[122:123], v112 offset:4864\n"
    "v_mfma_f32_16x16x32_bf16 v[48:51], a[68:71], a[36:39], v[48:51]\n"
    "v_exp_f32_e32 v39, v39\n"
    "v_mfma_f32_16x16x32_bf16 v[48:51], a[72:75], a[40:43], v[48:51]\n"
    "ds_read_b64_tr_b16 a[124:125], v113 offset:768\n"
    "v_cvt_pk_bf16_f32 v79, v38, v39\n"
    "v_mfma_f32_16x16x32_bf16 v[48:51], a[76:79], a[44:47], v[48:51]\n"
    "v_exp_f32_e32 v40, v40\n"
    "v_mfma_f32_16x16x32_bf16 v[52:55], a[80:83], a[0:3], v[92:95]\n"
    "v_xor_b32_e32 v108, s40, v108\n"
    "v_xor_b32_e32 v109, s40, v109\n"
    "v_mfma_f32_16x16x32_bf16 v[52:55], a[84:87], a[4:7], v[52:55]\n"
    "v_exp_f32_e32 v41, v41\n"
    "v_mfma_f32_16x16x32_bf16 v[52:55], a[88:91], a[8:11], v[52:55]\n"
    "ds_read_b64_tr_b16 a[126:127], v113 offset:4864\n"
    "v_cvt_pk_bf16_f32 v80, v40, v41\n"
    "v_mfma_f32_16x16x32_bf16 v[52:55], a[92:95], a[12:15], v[52:55]\n"
    "s_waitcnt vmcnt(20)\n"
    "v_exp_f32_e32 v42, v42\n"
    "v_mfma_f32_16x16x32_bf16 v[56:59], a[92:95], a[28:31], v[96:99]\n"
    "s_barrier\n"
    "buffer_load_dwordx4 v122, s[32:35], s42 offen lds\n"
    "ds_read_b128 a[64:67], v108\n"
    "v_mfma_f32_16x16x32_bf16 v[56:59], a[88:91], a[24:27], v[56:59]\n"
    "v_exp_f32_e32 v43, v43\n"
    "v_mfma_f32_16x16x32_bf16 v[56:59], a[84:87], a[20:23], v[56:59]\n"
    "ds_read_b128 a[68:71], v108 offset:256\n"
    "v_cvt_pk_bf16_f32 v81, v42, v43\n"
    "v_mfma_f32_16x16x32_bf16 v[56:59], a[80:83], a[16:19], v[56:59]\n"
    "v_exp_f32_e32 v44, v44\n"
    "v_mfma_f32_16x16x32_bf16 v[60:63], a[80:83], a[32:35], v[100:103]\n"
    "ds_read_b128 a[72:75], v108 offset:512\n"
    "buffer_load_dwordx4 v123, s[32:35], s42 offen offset:1024 lds\n"
    "v_mfma_f32_16x16x32_bf16 v[60:63], a[84:87], a[36:39], v[60:63]\n"
    "v_exp_f32_e32 v45, v45\n"
    "v_mfma_f32_16x16x32_bf16 v[60:63], a[88:91], a[40:43], v[60:63]\n"
    "ds_read_b128 a[76:79], v108 offset:768\n"
    "v_cvt_pk_bf16_f32 v84, v44, v45\n"
    "v_mfma_f32_16x16x32_bf16 v[60:63], a[92:95], a[44:47], v[60:63]\n"
    "v_exp_f32_e32 v46, v46\n"
    "v_mfma_f32_16x16x32_bf16 a[128:131], a[144:147], v[64:67], a[128:131]\n"
    "buffer_load_dwordx4 v124, s[32:35], s42 offen offset:2048 lds\n"
    "ds_read_b128 a[80:83], v108 offset:4096\n"
    "v_mfma_f32_16x16x32_bf16 a[132:135], a[144:147], v[68:71], a[132:135]\n"
    "v_exp_f32_e32 v47, v47\n"
    "v_mfma_f32_16x16x32_bf16 a[136:139], a[144:147], v[72:75], a[136:139]\n"
    "ds_read_b128 a[84:87], v108 offset:4352\n"
    "v_cvt_pk_bf16_f32 v85, v46, v47\n"
    "v_mfma_f32_16x16x32_bf16 a[140:143], a[144:147], v[76:79], a[140:143]\n"
    "s_waitcnt lgkmcnt(6)\n"
    "v_exp_f32_e32 v48, v48\n"
    "v_mfma_f32_16x16x32_bf16 v[128:131], a[96:99], v[64:67], v[128:131]\n"
    "buffer_load_dwordx4 v125, s[32:35], s42 offen offset:3072 lds\n"
    "ds_read_b128 a[88:91], v108 offset:4608\n"
    "v_mfma_f32_16x16x32_bf16 v[160:163], a[96:99], v[68:71], v[160:163]\n"
    "s_add_u32 s42, s69, 0x20000\n"
    "v_exp_f32_e32 v49, v49\n"
    "v_mfma_f32_16x16x32_bf16 v[192:195], a[96:99], v[72:75], v[192:195]\n"
    "s_add_u32 m0, s55, 0\n"
    "ds_read_b128 a[92:95], v108 offset:4864\n"
    "v_cvt_pk_bf16_f32 v88, v48, v49\n"
    "v_mfma_f32_16x16x32_bf16 v[224:227], a[96:99], v[76:79], v[224:227]\n"
    "v_exp_f32_e32 v50, v50\n"
    "v_mfma_f32_16x16x32_bf16 v[132:135], a[100:103], v[64:67], v[132:135]\n"
    "buffer_load_dwordx4 v122, s[32:35], s42 offen lds\n"
    "v_mfma_f32_16x16x32_bf16 v[164:167], a[100:103], v[68:71], v[164:167]\n"
    "v_exp_f32_e32 v51, v51\n"
    "v_mfma_f32_16x16x32_bf16 v[196:199], a[100:103], v[72:75], v[196:199]\n"
    "buffer_load_dwordx4 v123, s[32:35], s42 offen offset:1024 lds\n"
    "v_cvt_pk_bf16_f32 v89, v50, v51\n"
    "v_mfma_f32_16x16x32_bf16 v[228:231], a[100:103], v[76:79], v[228:231]\n"
    "v_exp_f32_e32 v52, v52\n"
    "v_mfma_f32_16x16x32_bf16 v[136:139], a[104:107], v[64:67], v[136:139]\n"
    "buffer_load_dwordx4 v124, s[32:35], s42 offen offset:2048 lds\n"
    "v_mfma_f32_16x16x32_bf16 v[168:171], a[104:107], v[68:71], v[168:171]\n"
    "v_exp_f32_e32 v53, v53\n"
    "v_mfma_f32_16x16x32_bf16 v[200:203], a[104:107], v[72:75], v[200:203]\n"
    "buffer_load_dwordx4 v125, s[32:35], s42 offen offset:3072 lds\n"
    "v_cvt_pk_bf16_f32 v82, v52, v53\n"
    "v_mfma_f32_16x16x32_bf16 v[232:235], a[104:107], v[76:79], v[232:235]\n"
    "s_add_u32 s42, s70, 0x20000\n"
    "v_exp_f32_e32 v54, v54\n"
    "v_mfma_f32_16x16x32_bf16 v[140:143], a[108:111], v[64:67], v[140:143]\n"
    "s_add_u32 m0, s55, 0x4000\n"
    "v_mfma_f32_16x16x32_bf16 v[172:175], a[108:111], v[68:71], v[172:175]\n"
    "v_exp_f32_e32 v55, v55\n"
    "v_mfma_f32_16x16x32_bf16 v[204:207], a[108:111], v[72:75], v[204:207]\n"
    "buffer_load_dwordx4 v122, s[32:35], s42 offen lds\n"
    "v_cvt_pk_bf16_f32 v83, v54, v55\n"
    "v_mfma_f32_16x16x32_bf16 v[236:239], a[108:111], v[76:79], v[236:239]\n"
    "v_exp_f32_e32 v56, v56\n"
    "v_mfma_f32_16x16x32_bf16 v[144:147], a[112:115], v[64:67], v[144:147]\n"
    "buffer_load_dwordx4 v123, s[32:35], s42 offen offset:1024 lds\n"
    "v_mfma_f32_16x16x32_bf16 v[176:179], a[112:115], v[68:71], v[176:179]\n"
    "s_memrealtime s[74:75]\n"
    "v_exp_f32_e32 v57, v57\n"
    "v_mfma_f32_16x16x32_bf16 v[208:211], a[112:115], v[72:75], v[208:211]\n"
    "buffer_load_dwordx4 v124, s[32:35], s42 offen offset:2048 lds\n"
    "v_cvt_pk_bf16_f32 v86, v56, v57\n"
    "v_mfma_f32_16x16x32_bf16 v[240:243], a[112:115], v[76:79], v[240:243]\n"
    "v_exp_f32_e32 v58, v58\n"
    "v_mfma_f32_16x16x32_bf16 v[148:151], a[116:119], v[64:67], v[148:151]\n"
    "v_mfma_f32_16x16x32_bf16 v[180:183], a[116:119], v[68:71], v[180:183]\n"
    "v_exp_f32_e32 v59, v59\n"
    "v_mfma_f32_16x16x32_bf16 v[212:215], a[116:119], v[72:75], v[212:215]\n"
    "buffer_load_dwordx4 v125, s[32:35], s42 offen offset:3072 lds\n"
    "v_cvt_pk_bf16_f32 v87, v58, v59\n"
    "v_mfma_f32_16x16x32_bf16 v[244:247], a[116:119], v[76:79], v[244:247]\n"
    "v_exp_f32_e32 v60, v60\n"
    "v_mfma_f32_16x16x32_bf16 v[152:155], a[120:123], v[64:67], v[152:155]\n"
    "v_mfma_f32_16x16x32_bf16 v[184:187], a[120:123], v[68:71], v[184:187]\n"
    "v_exp_f32_e32 v61, v61\n"
    "v_mfma_f32_16x16x32_bf16 v[216:219], a[120:123], v[72:75], v[216:219]\n"
    "v_cvt_pk_bf16_f32 v90, v60, v61\n"
    "v_mfma_f32_16x16x32_bf16 v[248:251], a[120:123], v[76:79], v[248:251]\n"
    "v_exp_f32_e32 v62, v62\n"
    "v_mfma_f32_16x16x32_bf16 v[156:159], a[124:127], v[64:67], v[156:159]\n"
    "v_mfma_f32_16x16x32_bf16 v[188:191], a[124:127], v[68:71], v[188:191]\n"
    "s_waitcnt lgkmcnt(0)\n"
    "v_exp_f32_e32 v63, v63\n"
    "v_mfma_f32_16x16x32_bf16 v[220:223], a[124:127], v[72:75], v[220:223]\n"
    "v_cvt_pk_bf16_f32 v91, v62, v63\n"
    "v_mfma_f32_16x16x32_bf16 v[252:255], a[124:127], v[76:79], v[252:255]\n"
    "s_sub_u32 s52, s73, s74\n"
    "s_cmp_gt_i32 s52, 0\n"
    "s_cbranch_scc0 L_np13%=\n"
    "L_pw13%=:\n"
    "s_nop 15\n"
    "s_sub_u32 s52, s52, 3\n"
    "s_cmp_gt_i32 s52, 0\n"
    "s_cbranch_scc1 L_pw13%=\n"
    "s_mov_b32 s74, s73\n"
    "L_np13%=:\n"
    "s_add_u32 s73, s74, 0x7d\n"
    "v_mfma_f32_16x16x32_bf16 v[8:11], a[64:67], a[0:3], v[92:95]\n"
    "ds_read_b64_tr_b16 a[96:97], v114\n"
    "ds_read_b64_tr_b16 a[98:99], v114 offset:4096\n"
    "v_mfma_f32_16x16x32_bf16 v[8:11], a[68:71], a[4:7], v[8:11]\n"
    "s_waitcnt vmcnt(8)\n"
    "ds_read_b64_tr_b16 a[100:101], v115\n"
    "v_mfma_f32_16x16x32_bf16 v[8:11], a[72:75], a[8:11], v[8:11]\n"
    "ds_read_b64_tr_b16 a[102:103], v115 offset:4096\n"
    "ds_read_b64_tr_b16 a[104:105], v114 offset:256\n"
    "v_mfma_f32_16x16x32_bf16 v[8:11], a[76:79], a[12:15], v[8:11]\n"
    "ds_read_b64_tr_b16 a[106:107], v114 offset:4352\n"
    "ds_read_b64_tr_b16 a[192:193], v5\n"
    "v_mfma_f32_16x16x32_bf16 v[12:15], a[76:79], a[28:31], v[96:99]\n"
    "ds_read_b64_tr_b16 a[108:109], v115 offset:256\n"
    "ds_read_b64_tr_b16 a[110:111], v115 offset:4352\n"
    "v_mfma_f32_16x16x32_bf16 v[12:15], a[72:75], a[24:27], v[12:15]\n"
    "ds_read_b64_tr_b16 a[112:113], v114 offset:512\n"
    "ds_read_b64_tr_b16 a[194:195], v5 offset:2048\n"
    "v_mfma_f32_16x16x32_bf16 v[12:15], a[68:71], a[20:23], v[12:15]\n"
    "ds_read_b64_tr_b16 a[114:115], v114 offset:4608\n"
    "ds_read_b64_tr_b16 a[116:117], v115 offset:512\n"
    "v_mfma_f32_16x16x32_bf16 v[12:15], a[64:67], a[16:19], v[12:15]\n"
    "ds_read_b64_tr_b16 a[118:119], v115 offset:4608\n"
    "ds_read_b64_tr_b16 a[196:197], v6\n"
    "v_mfma_f32_16x16x32_bf16 v[16:19], a[64:67], a[32:35], v[100:103]\n"
    "v_exp_f32_e32 v8, v8\n"
    "v_mfma_f32_16x16x32_bf16 v[16:19], a[68:71], a[36:39], v[16:19]\n"
    "v_exp_f32_e32 v9, v9\n"
    "v_mfma_f32_16x16x32_bf16 v[16:19], a[72:75], a[40:43], v[16:19]\n"
    "v_exp_f32_e32 v10, v10\n"
    "v_mfma_f32_16x16x32_bf16 v[16:19], a[76:79], a[44:47], v[16:19]\n"
    "ds_read_b64_tr_b16 a[198:199], v6 offset:2048\n"
    "v_cvt_pk_bf16_f32 v64, v8, v9\n"
    "v_mfma_f32_16x16x32_bf16 v[20:23], a[76:79], a[60:63], v[104:107]\n"
    "v_exp_f32_e32 v11, v11\n"
    "v_mfma_f32_16x16x32_bf16 v[20:23], a[72:75], a[56:59], v[20:23]\n"
    "ds_read_b64_tr_b16 a[120:121], v114 offset:768\n"
    "v_cvt_pk_bf16_f32 v65, v10, v11\n"
    "v_mfma_f32_16x16x32_bf16 v[20:23], a[68:71], a[52:55], v[20:23]\n"
    "v_exp_f32_e32 v12, v12\n"
    "v_mfma_f32_16x16x32_bf16 v[20:23], a[64:67], a[48:51], v[20:23]\n"
    "ds_read_b64_tr_b16 a[200:201], v5 offset:512\n"
    "ds_read_b64_tr_b16 a[202:203], v5 offset:2560\n"
    "v_mfma_f32_16x16x32_bf16 v[24:27], a[80:83], a[0:3], v[92:95]\n"
    "v_exp_f32_e32 v13, v13\n"
    "v_mfma_f32_16x16x32_bf16 v[24:27], a[84:87], a[4:7], v[24:27]\n"
    "ds_read_b64_tr_b16 a[122:123], v114 offset:4864\n"
    "v_cvt_pk_bf16_f32 v68, v12, v13\n"
    "v_mfma_f32_16x16x32_bf16 v[24:27], a[88:91], a[8:11], v[24:27]\n"
    "v_exp_f32_e32 v14, v14\n"
    "v_mfma_f32_16x16x32_bf16 v[24:27], a[92:95], a[12:15], v[24:27]\n"
    "ds_read_b64_tr_b16 a[204:205], v6 offset:512\n"
    "ds_read_b64_tr_b16 a[206:207], v6 offset:2560\n"
    "v_mfma_f32_16x16x32_bf16 v[28:31], a[92:95], a[28:31], v[96:99]\n"
    "v_exp_f32_e32 v15, v15\n"
    "v_mfma_f32_16x16x32_bf16 v[28:31], a[88:91], a[24:27], v[28:31]\n"
    "ds_read_b64_tr_b16 a[124:125], v115 offset:768\n"
    "v_cvt_pk_bf16_f32 v69, v14, v15\n"
    "v_mfma_f32_16x16x32_bf16 v[28:31], a[84:87], a[20:23], v[28:31]\n"
    "v_exp_f32_e32 v16, v16\n"
    "v_mfma_f32_16x16x32_bf16 v[28:31], a[80:83], a[16:19], v[28:31]\n"
    "ds_read_b64_tr_b16 a[126:127], v115 offset:4864\n"
    "ds_read_b128 a[64:67], v109\n"
    "v_mfma_f32_16x16x32_bf16 v[32:35], a[80:83], a[32:35], v[100:103]\n"
    "v_exp_f32_e32 v17, v17\n"
    "v_mfma_f32_16x16x32_bf16 v[32:35], a[84:87], a[36:39], v[32:35]\n"
    "ds_read_b128 a[68:71], v109 offset:256\n"
    "v_cvt_pk_bf16_f32 v72, v16, v17\n"
    "v_mfma_f32_16x16x32_bf16 v[32:35], a[88:91], a[40:43], v[32:35]\n"
    "v_exp_f32_e32 v18, v18\n"
    "v_mfma_f32_16x16x32_bf16 v[32:35], a[92:95], a[44:47], v[32:35]\n"
    "ds_read_b128 a[72:75], v109 offset:512\n"
    "ds_read_b64_tr_b16 a[208:209], v5 offset:16384\n"
    "v_mfma_f32_16x16x32_bf16 v[36:39], a[92:95], a[60:63], v[104:107]\n"
    "v_exp_f32_e32 v19, v19\n"
    "v_mfma_f32_16x16x32_bf16 v[36:39], a[88:91], a[56:59], v[36:39]\n"
    "ds_read_b128 a[76:79], v109 offset:768\n"
    "v_cvt_pk_bf16_f32 v73, v18, v19\n"
    "v_mfma_f32_16x16x32_bf16 v[36:39], a[84:87], a[52:55], v[36:39]\n"
    "s_waitcnt lgkmcnt(7)\n"
    "v_exp_f32_e32 v20, v20\n"
    "v_mfma_f32_16x16x32_bf16 v[36:39], a[80:83], a[48:51], v[36:39]\n"
    "ds_read_b64_tr_b16 a[210:211], v5 offset:18432\n"
    "ds_read_b64_tr_b16 a[212:213], v6 offset:16384\n"
    "v_mfma_f32_16x16x32_bf16 a[128:131], a[144:147], v[80:83], a[128:131]\n"
    "s_add_u32 s42, s71, 0x20000\n"
    "v_exp_f32_e32 v21, v21\n"
    "v_mfma_f32_16x16x32_bf16 a[132:135], a[144:147], v[84:87], a[132:135]\n"
    "s_add_u32 m0, s55, 0x20000\n"
    "ds_read_b128 a[80:83], v109 offset:4096\n"
    "v_cvt_pk_bf16_f32 v76, v20, v21\n"
    "v_mfma_f32_16x16x32_bf16 a[136:139], a[144:147], v[88:91], a[136:139]\n"
    "s_waitcnt lgkmcnt(8)\n"
    "v_exp_f32_e32 v22, v22\n"
    "v_mfma_f32_16x16x32_bf16 v[128:131], a[96:99], v[80:83], v[128:131]\n"
    "v_xor_b32_e32 v112, s40, v112\n"
    "v_xor_b32_e32 v113, s40, v113\n"
    "v_mfma_f32_16x16x32_bf16 v[160:163], a[96:99], v[84:87], v[160:163]\n"
    "v_exp_f32_e32 v23, v23\n"
    "v_mfma_f32_16x16x32_bf16 v[192:195], a[96:99], v[88:91], v[192:195]\n"
    "ds_read_b128 a[84:87], v109 offset:4352\n"
    "v_cvt_pk_bf16_f32 v77, v22, v23\n"
    "v_mfma_f32_16x16x32_bf16 v[132:135], a[100:103], v[80:83], v[132:135]\n"
    "v_exp_f32_e32 v24, v24\n"
    "v_mfma_f32_16x16x32_bf16 v[164:167], a[100:103], v[84:87], v[164:167]\n"
    "ds_read_b128 a[88:91], v109 offset:4608\n"
    "v_xor_b32_e32 v114, s40, v114\n"
    "v_mfma_f32_16x16x32_bf16 v[196:199], a[100:103], v[88:91], v[196:199]\n"
    "v_exp_f32_e32 v25, v25\n"
    "v_mfma_f32_16x16x32_bf16 v[136:139], a[104:107], v[80:83], v[136:139]\n"
    "v_cvt_pk_bf16_f32 v66, v24, v25\n"
    "v_xor_b32_e32 v115, s40, v115\n"
    "v_mfma_f32_16x16x32_bf16 v[168:171], a[104:107], v[84:87], v[168:171]\n"
    "v_exp_f32_e32 v26, v26\n"
    "v_mfma_f32_16x16x32_bf16 v[200:203], a[104:107], v[88:91], v[200:203]\n"
    "s_xor_b32 s40, s40, 0x18000\n"
    "ds_read_b128 a[92:95], v109 offset:4864\n"
    "ds_read_b64_tr_b16 a[214:215], v6 offset:18432\n"
    "ds_read_b64_tr_b16 a[216:217], v5 offset:16896\n"
    "v_mfma_f32_16x16x32_bf16 v[140:143], a[108:111], v[80:83], v[140:143]\n"
    "v_exp_f32_e32 v27, v27\n"
    "v_mfma_f32_16x16x32_bf16 v[172:175], a[108:111], v[84:87], v[172:175]\n"
    "ds_read_b64_tr_b16 a[96:97], v112\n"
    "v_cvt_pk_bf16_f32 v67, v26, v27\n"
    "v_mfma_f32_16x16x32_bf16 v[204:207], a[108:111], v[88:91], v[204:207]\n"
    "v_exp_f32_e32 v28, v28\n"
    "v_mfma_f32_16x16x32_bf16 v[144:147], a[112:115], v[80:83], v[144:147]\n"
    "ds_read_b64_tr_b16 a[98:99], v112 offset:4096\n"
    "ds_read_b64_tr_b16 a[100:101], v113\n"
    "v_mfma_f32_16x16x32_bf16 v[176:179], a[112:115], v[84:87], v[176:179]\n"
    "v_exp_f32_e32 v29, v29\n"
    "v_mfma_f32_16x16x32_bf16 v[208:211], a[112:115], v[88:91], v[208:211]\n"
    "ds_read_b64_tr_b16 a[102:103], v113 offset:4096\n"
    "v_cvt_pk_bf16_f32 v70, v28, v29\n"
    "v_mfma_f32_16x16x32_bf16 v[148:151], a[116:119], v[80:83], v[148:151]\n"
    "v_exp_f32_e32 v30, v30\n"
    "v_mfma_f32_16x16x32_bf16 v[180:183], a[116:119], v[84:87], v[180:183]\n"
    "ds_read_b64_tr_b16 a[104:105], v112 offset:256\n"
    "ds_read_b64_tr_b16 a[218:219], v5 offset:18944\n"
    "ds_read_b64_tr_b16 a[220:221], v6 offset:16896\n"
    "ds_read_b64_tr_b16 a[222:223], v6 offset:18944\n"
    "buffer_load_dwordx4 v122, s[32:35], s42 offen lds\n"
    "v_mfma_f32_16x16x32_bf16 v[212:215], a[116:119], v[88:91], v[212:215]\n"
    "v_exp_f32_e32 v31, v31\n"
    "v_mfma_f32_16x16x32_bf16 v[152:155], a[120:123], v[80:83], v[152:155]\n"
    "ds_read_b64_tr_b16 a[106:107], v112 offset:4352\n"
    "v_cvt_pk_bf16_f32 v71, v30, v31\n"
    "v_mfma_f32_16x16x32_bf16 v[184:187], a[120:123], v[84:87], v[184:187]\n"
    "v_exp_f32_e32 v32, v32\n"
    "v_mfma_f32_16x16x32_bf16 v[216:219], a[120:123], v[88:91], v[216:219]\n"
    "ds_read_b64_tr_b16 a[108:109], v113 offset:256\n"
    "buffer_load_dwordx4 v123, s[32:35], s42 offen offset:1024 lds\n"
    "v_mfma_f32_16x16x32_bf16 v[156:159], a[124:127], v[80:83], v[156:159]\n"
    "s_waitcnt lgkmcnt(2)\n"
    "v_exp_f32_e32 v33, v33\n"
    "v_mfma_f32_16x16x32_bf16 v[188:191], a[124:127], v[84:87], v[188:191]\n"
    "ds_read_b64_tr_b16 a[110:111], v113 offset:4352\n"
    "v_cvt_pk_bf16_f32 v74, v32, v33\n"
    "v_mfma_f32_16x16x32_bf16 v[220:223], a[124:127], v[88:91], v[220:223]\n"
    "s_waitcnt lgkmcnt(13)\n"
    "v_exp_f32_e32 v34, v34\n"
    "v_mfma_f32_16x16x32_bf16 v[40:43], a[64:67], a[0:3], v[92:95]\n"
    "ds_read_b64_tr_b16 a[112:113], v112 offset:512\n"
    "buffer_load_dwordx4 v124, s[32:35], s42 offen offset:2048 lds\n"
    "v_mfma_f32_16x16x32_bf16 v[40:43], a[68:71], a[4:7], v[40:43]\n"
    "v_exp_f32_e32 v35, v35\n"
    "v_mfma_f32_16x16x32_bf16 v[40:43], a[72:75], a[8:11], v[40:43]\n"
    "ds_read_b64_tr_b16 a[114:115], v112 offset:4608\n"
    "v_cvt_pk_bf16_f32 v75, v34, v35\n"
    "v_mfma_f32_16x16x32_bf16 v[40:43], a[76:79], a[12:15], v[40:43]\n"
    "v_exp_f32_e32 v36, v36\n"
    "v_mfma_f32_16x16x32_bf16 v[44:47], a[76:79], a[28:31], v[96:99]\n"
    "ds_read_b64_tr_b16 a[116:117], v113 offset:512\n"
    "buffer_load_dwordx4 v125, s[32:35], s42 offen offset:3072 lds\n"
    "v_mfma_f32_16x16x32_bf16 v[44:47], a[72:75], a[24:27], v[44:47]\n"
    "s_add_u32 s42, s72, 0x20000\n"
    "v_exp_f32_e32 v37, v37\n"
    "v_mfma_f32_16x16x32_bf16 v[44:47], a[68:71], a[20:23], v[44:47]\n"
    "s_add_u32 m0, s55, 0x24000\n"
    "ds_read_b64_tr_b16 a[118:119], v113 offset:4608\n"
    "v_cvt_pk_bf16_f32 v78, v36, v37\n"
    "v_mfma_f32_16x16x32_bf16 v[44:47], a[64:67], a[16:19], v[44:47]\n"
    "v_exp_f32_e32 v38, v38\n"
    "v_mfma_f32_16x16x32_bf16 v[48:51], a[64:67], a[32:35], v[100:103]\n"
    "ds_read_b64_tr_b16 a[120:121], v112 offset:768\n"
    "ds_read_b64_tr_b16 a[122:123], v112 offset:4864\n"
    "v_mfma_f32_16x16x32_bf16 v[48:51], a[68:71], a[36:39], v[48:51]\n"
    "v_exp_f32_e32 v39, v39\n"
    "v_mfma_f32_16x16x32_bf16 v[48:51], a[72:75], a[40:43], v[48:51]\n"
    "ds_read_b64_tr_b16 a[124:125], v113 offset:768\n"
    "v_cvt_pk_bf16_f32 v79, v38, v39\n"
    "v_mfma_f32_16x16x32_bf16 v[48:51], a[76:79], a[44:47], v[48:51]\n"
    "v_exp_f32_e32 v40, v40\n"
    "v_mfma_f32_16x16x32_bf16 v[52:55], a[80:83], a[0:3], v[92:95]\n"
    "v_xor_b32_e32 v108, s40, v108\n"
    "v_xor_b32_e32 v109, s40, v109\n"
    "v_mfma_f32_16x16x32_bf16 v[52:55], a[84:87], a[4:7], v[52:55]\n"
    "v_exp_f32_e32 v41, v41\n"
    "v_mfma_f32_16x16x32_bf16 v[52:55], a[88:91], a[8:11], v[52:55]\n"
    "ds_read_b64_tr_b16 a[126:127], v113 offset:4864\n"
    "v_cvt_pk_bf16_f32 v80, v40, v41\n"
    "v_mfma_f32_16x16x32_bf16 v[52:55], a[92:95], a[12:15], v[52:55]\n"
    "s_waitcnt vmcnt(20)\n"
    "v_exp_f32_e32 v42, v42\n"
    "v_mfma_f32_16x16x32_bf16 v[56:59], a[92:95], a[28:31], v[96:99]\n"
    "s_barrier\n"
    "buffer_load_dwordx4 v122, s[32:35], s42 offen lds\n"
    "ds_read_b128 a[64:67], v108\n"
    "v_mfma_f32_16x16x32_bf16 v[56:59], a[88:91], a[24:27], v[56:59]\n"
    "v_exp_f32_e32 v43, v43\n"
    "v_mfma_f32_16x16x32_bf16 v[56:59], a[84:87], a[20:23], v[56:59]\n"
    "ds_read_b128 a[68:71], v108 offset:256\n"
    "v_cvt_pk_bf16_f32 v81, v42, v43\n"
    "v_mfma_f32_16x16x32_bf16 v[56:59], a[80:83], a[16:19], v[56:59]\n"
    "v_exp_f32_e32 v44, v44\n"
    "v_mfma_f32_16x16x32_bf16 v[60:63], a[80:83], a[32:35], v[100:103]\n"
    "ds_read_b128 a[72:75], v108 offset:512\n"
    "buffer_load_dwordx4 v123, s[32:35], s42 offen offset:1024 lds\n"
    "v_mfma_f32_16x16x32_bf16 v[60:63], a[84:87], a[36:39], v[60:63]\n"
    "v_exp_f32_e32 v45, v45\n"
    "v_mfma_f32_16x16x32_bf16 v[60:63], a[88:91], a[40:43], v[60:63]\n"
    "ds_read_b128 a[76:79], v108 offset:768\n"
    "v_cvt_pk_bf16_f32 v84, v44, v45\n"
    "v_mfma_f32_16x16x32_bf16 v[60:63], a[92:95], a[44:47], v[60:63]\n"
    "v_exp_f32_e32 v46, v46\n"
    "v_mfma_f32_16x16x32_bf16 a[128:131], a[144:147], v[64:67], a[128:131]\n"
    "buffer_load_dwordx4 v124, s[32:35], s42 offen offset:2048 lds\n"
    "ds_read_b128 a[80:83], v108 offset:4096\n"
    "v_mfma_f32_16x16x32_bf16 a[132:135], a[144:147], v[68:71], a[132:135]\n"
    "v_exp_f32_e32 v47, v47\n"
    "v_mfma_f32_16x16x32_bf16 a[136:139], a[144:147], v[72:75], a[136:139]\n"
    "ds_read_b128 a[84:87], v108 offset:4352\n"
    "v_cvt_pk_bf16_f32 v85, v46, v47\n"
    "v_mfma_f32_16x16x32_bf16 a[140:143], a[144:147], v[76:79], a[140:143]\n"
    "s_waitcnt lgkmcnt(6)\n"
    "v_exp_f32_e32 v48, v48\n"
    "v_mfma_f32_16x16x32_bf16 v[128:131], a[96:99], v[64:67], v[128:131]\n"
    "buffer_load_dwordx4 v125, s[32:35], s42 offen offset:3072 lds\n"
    "ds_read_b128 a[88:91], v108 offset:4608\n"
    "v_mfma_f32_16x16x32_bf16 v[160:163], a[96:99], v[68:71], v[160:163]\n"
    "s_add_u32 s42, s69, 0x40000\n"
    "v_exp_f32_e32 v49, v49\n"
    "v_mfma_f32_16x16x32_bf16 v[192:195], a[96:99], v[72:75], v[192:195]\n"
    "s_add_u32 m0, s55, 0x8000\n"
    "ds_read_b128 a[92:95], v108 offset:4864\n"
    "v_cvt_pk_bf16_f32 v88, v48, v49\n"
    "v_mfma_f32_16x16x32_bf16 v[224:227], a[96:99], v[76:79], v[224:227]\n"
    "s_lshl_b32 s45, s47, 7\n"
    "v_exp_f32_e32 v50, v50\n"
    "v_mfma_f32_16x16x32_bf16 v[132:135], a[100:103], v[64:67], v[132:135]\n"
    "buffer_load_dwordx4 v122, s[32:35], s42 offen lds\n"
    "v_and_b32_e32 v126, 31, v0\n"
    "v_mfma_f32_16x16x32_bf16 v[164:167], a[100:103], v[68:71], v[164:167]\n"
    "v_exp_f32_e32 v51, v51\n"
    "v_mfma_f32_16x16x32_bf16 v[196:199], a[100:103], v[72:75], v[196:199]\n"
    "buffer_load_dwordx4 v123, s[32:35], s42 offen offset:1024 lds\n"
    "v_cvt_pk_bf16_f32 v89, v50, v51\n"
    "v_mfma_f32_16x16x32_bf16 v[228:231], a[100:103], v[76:79], v[228:231]\n"
    "v_exp_f32_e32 v52, v52\n"
    "v_mfma_f32_16x16x32_bf16 v[136:139], a[104:107], v[64:67], v[136:139]\n"
    "buffer_load_dwordx4 v124, s[32:35], s42 offen offset:2048 lds\n"
    "v_lshlrev_b32_e32 v126, 2, v126\n"
    "v_mfma_f32_16x16x32_bf16 v[168:171], a[104:107], v[68:71], v[168:171]\n"
    "v_exp_f32_e32 v53, v53\n"
    "v_mfma_f32_16x16x32_bf16 v[200:203], a[104:107], v[72:75], v[200:203]\n"
    "buffer_load_dword v126, v126, s[56:59], s45 offen sc1\n"
    "v_cvt_pk_bf16_f32 v82, v52, v53\n"
    "v_mfma_f32_16x16x32_bf16 v[232:235], a[104:107], v[76:79], v[232:235]\n"
    "s_lshr_b32 s45, s46, 3\n"
    "v_exp_f32_e32 v54, v54\n"
    "v_mfma_f32_16x16x32_bf16 v[140:143], a[108:111], v[64:67], v[140:143]\n"
    "s_lshl_b32 s45, s45, 2\n"
    "buffer_load_dwordx4 v125, s[32:35], s42 offen offset:3072 lds\n"
    "v_mfma_f32_16x16x32_bf16 v[172:175], a[108:111], v[68:71], v[172:175]\n"
    "s_add_u32 s42, s70, 0x40000\n"
    "v_exp_f32_e32 v55, v55\n"
    "v_mfma_f32_16x16x32_bf16 v[204:207], a[108:111], v[72:75], v[204:207]\n"
    "s_add_u32 m0, s55, 0xc000\n"
    "s_add_u32 s45, s45, s44\n"
    "v_cvt_pk_bf16_f32 v83, v54, v55\n"
    "v_mfma_f32_16x16x32_bf16 v[236:239], a[108:111], v[76:79], v[236:239]\n"
    "s_lshl_b32 s45, s45, 8\n"
    "v_exp_f32_e32 v56, v56\n"
    "v_mfma_f32_16x16x32_bf16 v[144:147], a[112:115], v[64:67], v[144:147]\n"
    "s_add_u32 s45, s45, L_drain%=-L_pc%=\n"
    "buffer_load_dwordx4 v122, s[32:35], s42 offen lds\n"
    "v_mfma_f32_16x16x32_bf16 v[176:179], a[112:115], v[68:71], v[176:179]\n"
    "s_memrealtime s[74:75]\n"
    "v_exp_f32_e32 v57, v57\n"
    "v_mfma_f32_16x16x32_bf16 v[208:211], a[112:115], v[72:75], v[208:211]\n"
    "v_cvt_pk_bf16_f32 v86, v56, v57\n"
    "v_lshl_add_u32 v2, v0, 2, s45\n"
    "v_mfma_f32_16x16x32_bf16 v[240:243], a[112:115], v[76:79], v[240:243]\n"
    "v_exp_f32_e32 v58, v58\n"
    "v_mfma_f32_16x16x32_bf16 v[148:151], a[116:119], v[64:67], v[148:151]\n"
    "buffer_load_dword v127, v2, s[60:63], 0 offen\n"
    "buffer_load_dwordx4 v123, s[32:35], s42 offen offset:1024 lds\n"
    "v_mfma_f32_16x16x32_bf16 v[180:183], a[116:119], v[68:71], v[180:183]\n"
    "v_exp_f32_e32 v59, v59\n"
    "v_mfma_f32_16x16x32_bf16 v[212:215], a[116:119], v[72:75], v[212:215]\n"
    "buffer_load_dwordx4 v124, s[32:35], s42 offen offset:2048 lds\n"
    "v_cvt_pk_bf16_f32 v87, v58, v59\n"
    "v_mfma_f32_16x16x32_bf16 v[244:247], a[116:119], v[76:79], v[244:247]\n"
    "v_exp_f32_e32 v60, v60\n"
    "v_mfma_f32_16x16x32_bf16 v[152:155], a[120:123], v[64:67], v[152:155]\n"
    "buffer_load_dwordx4 v125, s[32:35], s42 offen offset:3072 lds\n"
    "v_mfma_f32_16x16x32_bf16 v[184:187], a[120:123], v[68:71], v[184:187]\n"
    "v_exp_f32_e32 v61, v61\n"
    "v_mfma_f32_16x16x32_bf16 v[216:219], a[120:123], v[72:75], v[216:219]\n"
    "v_cvt_pk_bf16_f32 v90, v60, v61\n"
    "v_mfma_f32_16x16x32_bf16 v[248:251], a[120:123], v[76:79], v[248:251]\n"
    "v_exp_f32_e32 v62, v62\n"
    "v_mfma_f32_16x16x32_bf16 v[156:159], a[124:127], v[64:67], v[156:159]\n"
    "v_mfma_f32_16x16x32_bf16 v[188:191], a[124:127], v[68:71], v[188:191]\n"
    "s_waitcnt lgkmcnt(0)\n"
    "v_exp_f32_e32 v63, v63\n"
    "v_mfma_f32_16x16x32_bf16 v[220:223], a[124:127], v[72:75], v[220:223]\n"
    "v_cvt_pk_bf16_f32 v91, v62, v63\n"
    "v_mfma_f32_16x16x32_bf16 v[252:255], a[124:127], v[76:79], v[252:255]\n"
    "s_sub_u32 s52, s73, s74\n"
    "s_cmp_gt_i32 s52, 0\n"
    "s_cbranch_scc0 L_np14%=\n"
    "L_pw14%=:\n"
    "s_nop 15\n"
    "s_sub_u32 s52, s52, 3\n"
    "s_cmp_gt_i32 s52, 0\n"
    "s_cbranch_scc1 L_pw14%=\n"
    "s_mov_b32 s74, s73\n"
    "L_np14%=:\n"
    "s_add_u32 s73, s74, 0x7d\n"
    "v_mfma_f32_16x16x32_bf16 v[8:11], a[64:67], a[0:3], v[92:95]\n"
    "ds_read_b64_tr_b16 a[96:97], v114\n"
    "ds_read_b64_tr_b16 a[98:99], v114 offset:4096\n"
    "v_mfma_f32_16x16x32_bf16 v[8:11], a[68:71], a[4:7], v[8:11]\n"
    "s_waitcnt vmcnt(18)\n"
    "ds_read_b64_tr_b16 a[100:101], v115\n"
    "v_mfma_f32_16x16x32_bf16 v[8:11], a[72:75], a[8:11], v[8:11]\n"
    "ds_read_b64_tr_b16 a[102:103], v115 offset:4096\n"
    "ds_read_b64_tr_b16 a[104:105], v114 offset:256\n"
    "v_mfma_f32_16x16x32_bf16 v[8:11], a[76:79], a[12:15], v[8:11]\n"
    "ds_read_b64_tr_b16 a[106:107], v114 offset:4352\n"
    "ds_read_b64_tr_b16 a[224:225], v3\n"
    "v_mfma_f32_16x16x32_bf16 v[12:15], a[76:79], a[28:31], v[96:99]\n"
    "ds_read_b64_tr_b16 a[108:109], v115 offset:256\n"
    "ds_read_b64_tr_b16 a[110:111], v115 offset:4352\n"
    "v_mfma_f32_16x16x32_bf16 v[12:15], a[72:75], a[24:27], v[12:15]\n"
    "ds_read_b64_tr_b16 a[112:113], v114 offset:512\n"
    "ds_read_b64_tr_b16 a[226:227], v3 offset:2048\n"
    "v_mfma_f32_16x16x32_bf16 v[12:15], a[68:71], a[20:23], v[12:15]\n"
    "ds_read_b64_tr_b16 a[114:115], v114 offset:4608\n"
    "ds_read_b64_tr_b16 a[116:117], v115 offset:512\n"
    "v_mfma_f32_16x16x32_bf16 v[12:15], a[64:67], a[16:19], v[12:15]\n"
    "ds_read_b64_tr_b16 a[118:119], v115 offset:4608\n"
    "ds_read_b64_tr_b16 a[228:229], v4\n"
    "v_mfma_f32_16x16x32_bf16 v[16:19], a[64:67], a[32:35], v[100:103]\n"
    "v_exp_f32_e32 v8, v8\n"
    "v_mfma_f32_16x16x32_bf16 v[16:19], a[68:71], a[36:39], v[16:19]\n"
    "v_exp_f32_e32 v9, v9\n"
    "v_mfma_f32_16x16x32_bf16 v[16:19], a[72:75], a[40:43], v[16:19]\n"
    "v_exp_f32_e32 v10, v10\n"
    "v_mfma_f32_16x16x32_bf16 v[16:19], a[76:79], a[44:47], v[16:19]\n"
    "ds_read_b64_tr_b16 a[230:231], v4 offset:2048\n"
    "v_cvt_pk_bf16_f32 v64, v8, v9\n"
    "v_mfma_f32_16x16x32_bf16 v[20:23], a[76:79], a[60:63], v[104:107]\n"
    "v_exp_f32_e32 v11, v11\n"
    "v_mfma_f32_16x16x32_bf16 v[20:23], a[72:75], a[56:59], v[20:23]\n"
    "ds_read_b64_tr_b16 a[120:121], v114 offset:768\n"
    "v_cvt_pk_bf16_f32 v65, v10, v11\n"
    "v_mfma_f32_16x16x32_bf16 v[20:23], a[68:71], a[52:55], v[20:23]\n"
    "v_exp_f32_e32 v12, v12\n"
    "v_mfma_f32_16x16x32_bf16 v[20:23], a[64:67], a[48:51], v[20:23]\n"
    "ds_read_b64_tr_b16 a[232:233], v3 offset:512\n"
    "ds_read_b64_tr_b16 a[234:235], v3 offset:2560\n"
    "v_mfma_f32_16x16x32_bf16 v[24:27], a[80:83], a[0:3], v[92:95]\n"
    "v_exp_f32_e32 v13, v13\n"
    "v_mfma_f32_16x16x32_bf16 v[24:27], a[84:87], a[4:7], v[24:27]\n"
    "ds_read_b64_tr_b16 a[122:123], v114 offset:4864\n"
    "v_cvt_pk_bf16_f32 v68, v12, v13\n"
    "v_mfma_f32_16x16x32_bf16 v[24:27], a[88:91], a[8:11], v[24:27]\n"
    "v_exp_f32_e32 v14, v14\n"
    "v_mfma_f32_16x16x32_bf16 v[24:27], a[92:95], a[12:15], v[24:27]\n"
    "ds_read_b64_tr_b16 a[236:237], v4 offset:512\n"
    "ds_read_b64_tr_b16 a[238:239], v4 offset:2560\n"
    "v_mfma_f32_16x16x32_bf16 v[28:31], a[92:95], a[28:31], v[96:99]\n"
    "v_exp_f32_e32 v15, v15\n"
    "v_mfma_f32_16x16x32_bf16 v[28:31], a[88:91], a[24:27], v[28:31]\n"
    "ds_read_b64_tr_b16 a[124:125], v115 offset:768\n"
    "v_cvt_pk_bf16_f32 v69, v14, v15\n"
    "v_mfma_f32_16x16x32_bf16 v[28:31], a[84:87], a[20:23], v[28:31]\n"
    "v_exp_f32_e32 v16, v16\n"
    "v_mfma_f32_16x16x32_bf16 v[28:31], a[80:83], a[16:19], v[28:31]\n"
    "ds_read_b64_tr_b16 a[126:127], v115 offset:4864\n"
    "ds_read_b128 a[64:67], v109\n"
    "v_mfma_f32_16x16x32_bf16 v[32:35], a[80:83], a[32:35], v[100:103]\n"
    "v_exp_f32_e32 v17, v17\n"
    "v_mfma_f32_16x16x32_bf16 v[32:35], a[84:87], a[36:39], v[32:35]\n"
    "ds_read_b128 a[68:71], v109 offset:256\n"
    "v_cvt_pk_bf16_f32 v72, v16, v17\n"
    "v_mfma_f32_16x16x32_bf16 v[32:35], a[88:91], a[40:43], v[32:35]\n"
    "v_exp_f32_e32 v18, v18\n"
    "v_mfma_f32_16x16x32_bf16 v[32:35], a[92:95], a[44:47], v[32:35]\n"
    "ds_read_b128 a[72:75], v109 offset:512\n"
    "ds_read_b64_tr_b16 a[240:241], v3 offset:16384\n"
    "v_mfma_f32_16x16x32_bf16 v[36:39], a[92:95], a[60:63], v[104:107]\n"
    "v_exp_f32_e32 v19, v19\n"
    "v_mfma_f32_16x16x32_bf16 v[36:39], a[88:91], a[56:59], v[36:39]\n"
    "ds_read_b128 a[76:79], v109 offset:768\n"
    "v_cvt_pk_bf16_f32 v73, v18, v19\n"
    "v_mfma_f32_16x16x32_bf16 v[36:39], a[84:87], a[52:55], v[36:39]\n"
    "s_waitcnt lgkmcnt(7)\n"
    "v_exp_f32_e32 v20, v20\n"
    "v_mfma_f32_16x16x32_bf16 v[36:39], a[80:83], a[48:51], v[36:39]\n"
    "ds_read_b64_tr_b16 a[242:243], v3 offset:18432\n"
    "ds_read_b64_tr_b16 a[244:245], v4 offset:16384\n"
    "v_mfma_f32_16x16x32_bf16 a[128:131], a[144:147], v[80:83], a[128:131]\n"
    "s_add_u32 s42, s71, 0x40000\n"
    "v_exp_f32_e32 v21, v21\n"
    "v_mfma_f32_16x16x32_bf16 a[132:135], a[144:147], v[84:87], a[132:135]\n"
    "s_add_u32 m0, s55, 0\n"
    "ds_read_b128 a[80:83], v109 offset:4096\n"
    "v_cvt_pk_bf16_f32 v76, v20, v21\n"
    "v_mfma_f32_16x16x32_bf16 a[136:139], a[144:147], v[88:91], a[136:139]\n"
    "s_waitcnt lgkmcnt(8)\n"
    "v_exp_f32_e32 v22, v22\n"
    "v_mfma_f32_16x16x32_bf16 v[128:131], a[96:99], v[80:83], v[128:131]\n"
    "v_xor_b32_e32 v112, s40, v112\n"
    "v_xor_b32_e32 v113, s40, v113\n"
    "v_mfma_f32_16x16x32_bf16 v[160:163], a[96:99], v[84:87], v[160:163]\n"
    "v_exp_f32_e32 v23, v23\n"
    "v_mfma_f32_16x16x32_bf16 v[192:195], a[96:99], v[88:91], v[192:195]\n"
    "ds_read_b128 a[84:87], v109 offset:4352\n"
    "v_cvt_pk_bf16_f32 v77, v22, v23\n"
    "v_mfma_f32_16x16x32_bf16 v[132:135], a[100:103], v[80:83], v[132:135]\n"
    "v_exp_f32_e32 v24, v24\n"
    "v_mfma_f32_16x16x32_bf16 v[164:167], a[100:103], v[84:87], v[164:167]\n"
    "ds_read_b128 a[88:91], v109 offset:4608\n"
    "v_xor_b32_e32 v114, s40, v114\n"
    "v_mfma_f32_16x16x32_bf16 v[196:199], a[100:103], v[88:91], v[196:199]\n"
    "v_exp_f32_e32 v25, v25\n"
    "v_mfma_f32_16x16x32_bf16 v[136:139], a[104:107], v[80:83], v[136:139]\n"
    "v_cvt_pk_bf16_f32 v66, v24, v25\n"
    "v_xor_b32_e32 v115, s40, v115\n"
    "v_mfma_f32_16x16x32_bf16 v[168:171], a[104:107], v[84:87], v[168:171]\n"
    "v_exp_f32_e32 v26, v26\n"
    "v_mfma_f32_16x16x32_bf16 v[200:203], a[104:107], v[88:91], v[200:203]\n"
    "s_xor_b32 s40, s40, 0x18000\n"
    "ds_read_b128 a[92:95], v109 offset:4864\n"
    "ds_read_b64_tr_b16 a[246:247], v4 offset:18432\n"
    "ds_read_b64_tr_b16 a[248:249], v3 offset:16896\n"
    "v_mfma_f32_16x16x32_bf16 v[140:143], a[108:111], v[80:83], v[140:143]\n"
    "v_exp_f32_e32 v27, v27\n"
    "v_mfma_f32_16x16x32_bf16 v[172:175], a[108:111], v[84:87], v[172:175]\n"
    "ds_read_b64_tr_b16 a[96:97], v112\n"
    "v_cvt_pk_bf16_f32 v67, v26, v27\n"
    "v_mfma_f32_16x16x32_bf16 v[204:207], a[108:111], v[88:91], v[204:207]\n"
    "v_exp_f32_e32 v28, v28\n"
    "v_mfma_f32_16x16x32_bf16 v[144:147], a[112:115], v[80:83], v[144:147]\n"
    "ds_read_b64_tr_b16 a[98:99], v112 offset:4096\n"
    "ds_read_b64_tr_b16 a[100:101], v113\n"
    "v_mfma_f32_16x16x32_bf16 v[176:179], a[112:115], v[84:87], v[176:179]\n"
    "v_exp_f32_e32 v29, v29\n"
    "v_mfma_f32_16x16x32_bf16 v[208:211], a[112:115], v[88:91], v[208:211]\n"
    "ds_read_b64_tr_b16 a[102:103], v113 offset:4096\n"
    "v_cvt_pk_bf16_f32 v70, v28, v29\n"
    "v_mfma_f32_16x16x32_bf16 v[148:151], a[116:119], v[80:83], v[148:151]\n"
    "v_exp_f32_e32 v30, v30\n"
    "v_mfma_f32_16x16x32_bf16 v[180:183], a[116:119], v[84:87], v[180:183]\n"
    "ds_read_b64_tr_b16 a[104:105], v112 offset:256\n"
    "ds_read_b64_tr_b16 a[250:251], v3 offset:18944\n"
    "ds_read_b64_tr_b16 a[252:253], v4 offset:16896\n"
    "ds_read_b64_tr_b16 a[254:255], v4 offset:18944\n"
    "buffer_load_dwordx4 v122, s[32:35], s42 offen lds\n"
    "v_mfma_f32_16x16x32_bf16 v[212:215], a[116:119], v[88:91], v[212:215]\n"
    "v_exp_f32_e32 v31, v31\n"
    "v_mfma_f32_16x16x32_bf16 v[152:155], a[120:123], v[80:83], v[152:155]\n"
    "ds_read_b64_tr_b16 a[106:107], v112 offset:4352\n"
    "v_cvt_pk_bf16_f32 v71, v30, v31\n"
    "v_mfma_f32_16x16x32_bf16 v[184:187], a[120:123], v[84:87], v[184:187]\n"
    "v_exp_f32_e32 v32, v32\n"
    "v_mfma_f32_16x16x32_bf16 v[216:219], a[120:123], v[88:91], v[216:219]\n"
    "ds_read_b64_tr_b16 a[108:109], v113 offset:256\n"
    "buffer_load_dwordx4 v123, s[32:35], s42 offen offset:1024 lds\n"
    "v_mfma_f32_16x16x32_bf16 v[156:159], a[124:127], v[80:83], v[156:159]\n"
    "s_waitcnt lgkmcnt(2)\n"
    "v_exp_f32_e32 v33, v33\n"
    "v_mfma_f32_16x16x32_bf16 v[188:191], a[124:127], v[84:87], v[188:191]\n"
    "ds_read_b64_tr_b16 a[110:111], v113 offset:4352\n"
    "v_cvt_pk_bf16_f32 v74, v32, v33\n"
    "v_mfma_f32_16x16x32_bf16 v[220:223], a[124:127], v[88:91], v[220:223]\n"
    "s_waitcnt lgkmcnt(13)\n"
    "v_exp_f32_e32 v34, v34\n"
    "v_mfma_f32_16x16x32_bf16 v[40:43], a[64:67], a[0:3], v[92:95]\n"
    "ds_read_b64_tr_b16 a[112:113], v112 offset:512\n"
    "buffer_load_dwordx4 v124, s[32:35], s42 offen offset:2048 lds\n"
    "v_mfma_f32_16x16x32_bf16 v[40:43], a[68:71], a[4:7], v[40:43]\n"
    "v_exp_f32_e32 v35, v35\n"
    "v_mfma_f32_16x16x32_bf16 v[40:43], a[72:75], a[8:11], v[40:43]\n"
    "ds_read_b64_tr_b16 a[114:115], v112 offset:4608\n"
    "v_cvt_pk_bf16_f32 v75, v34, v35\n"
    "v_mfma_f32_16x16x32_bf16 v[40:43], a[76:79], a[12:15], v[40:43]\n"
    "v_exp_f32_e32 v36, v36\n"
    "v_mfma_f32_16x16x32_bf16 v[44:47], a[76:79], a[28:31], v[96:99]\n"
    "ds_read_b64_tr_b16 a[116:117], v113 offset:512\n"
    "buffer_load_dwordx4 v125, s[32:35], s42 offen offset:3072 lds\n"
    "v_mfma_f32_16x16x32_bf16 v[44:47], a[72:75], a[24:27], v[44:47]\n"
    "s_add_u32 s42, s72, 0x40000\n"
    "v_exp_f32_e32 v37, v37\n"
    "v_mfma_f32_16x16x32_bf16 v[44:47], a[68:71], a[20:23], v[44:47]\n"
    "s_add_u32 m0, s55, 0x4000\n"
    "ds_read_b64_tr_b16 a[118:119], v113 offset:4608\n"
    "v_cvt_pk_bf16_f32 v78, v36, v37\n"
    "v_mfma_f32_16x16x32_bf16 v[44:47], a[64:67], a[16:19], v[44:47]\n"
    "v_exp_f32_e32 v38, v38\n"
    "v_mfma_f32_16x16x32_bf16 v[48:51], a[64:67], a[32:35], v[100:103]\n"
    "ds_read_b64_tr_b16 a[120:121], v112 offset:768\n"
    "ds_read_b64_tr_b16 a[122:123], v112 offset:4864\n"
    "v_mfma_f32_16x16x32_bf16 v[48:51], a[68:71], a[36:39], v[48:51]\n"
    "v_exp_f32_e32 v39, v39\n"
    "v_mfma_f32_16x16x32_bf16 v[48:51], a[72:75], a[40:43], v[48:51]\n"
    "ds_read_b64_tr_b16 a[124:125], v113 offset:768\n"
    "v_cvt_pk_bf16_f32 v79, v38, v39\n"
    "v_mfma_f32_16x16x32_bf16 v[48:51], a[76:79], a[44:47], v[48:51]\n"
    "v_exp_f32_e32 v40, v40\n"
    "v_mfma_f32_16x16x32_bf16 v[52:55], a[80:83], a[0:3], v[92:95]\n"
    "buffer_load_dwordx4 v122, s[32:35], s42 offen lds\n"
    "v_mfma_f32_16x16x32_bf16 v[52:55], a[84:87], a[4:7], v[52:55]\n"
    "v_exp_f32_e32 v41, v41\n"
    "v_mfma_f32_16x16x32_bf16 v[52:55], a[88:91], a[8:11], v[52:55]\n"
    "ds_read_b64_tr_b16 a[126:127], v113 offset:4864\n"
    "v_cvt_pk_bf16_f32 v80, v40, v41\n"
    "v_mfma_f32_16x16x32_bf16 v[52:55], a[92:95], a[12:15], v[52:55]\n"
    "v_exp_f32_e32 v42, v42\n"
    "v_mfma_f32_16x16x32_bf16 v[56:59], a[92:95], a[28:31], v[96:99]\n"
    "s_barrier\n"
    "buffer_load_dwordx4 v123, s[32:35], s42 offen offset:1024 lds\n"
    "v_mfma_f32_16x16x32_bf16 v[56:59], a[88:91], a[24:27], v[56:59]\n"
    "v_exp_f32_e32 v43, v43\n"
    "v_mfma_f32_16x16x32_bf16 v[56:59], a[84:87], a[20:23], v[56:59]\n"
    "buffer_load_dwordx4 v124, s[32:35], s42 offen offset:2048 lds\n"
    "v_cvt_pk_bf16_f32 v81, v42, v43\n"
    "v_mfma_f32_16x16x32_bf16 v[56:59], a[80:83], a[16:19], v[56:59]\n"
    "v_exp_f32_e32 v44, v44\n"
    "v_mfma_f32_16x16x32_bf16 v[60:63], a[80:83], a[32:35], v[100:103]\n"
    "buffer_load_dwordx4 v125, s[32:35], s42 offen offset:3072 lds\n"
    "v_mfma_f32_16x16x32_bf16 v[60:63], a[84:87], a[36:39], v[60:63]\n"
    "s_add_u32 s42, s69, 0x60000\n"
    "v_exp_f32_e32 v45, v45\n"
    "v_mfma_f32_16x16x32_bf16 v[60:63], a[88:91], a[40:43], v[60:63]\n"
    "s_add_u32 m0, s55, 0x18000\n"
    "v_cvt_pk_bf16_f32 v84, v44, v45\n"
    "v_mfma_f32_16x16x32_bf16 v[60:63], a[92:95], a[44:47], v[60:63]\n"
    "v_exp_f32_e32 v46, v46\n"
    "v_mfma_f32_16x16x32_bf16 a[128:131], a[144:147], v[64:67], a[128:131]\n"
    "buffer_load_dwordx4 v122, s[32:35], s42 offen lds\n"
    "v_mfma_f32_16x16x32_bf16 a[132:135], a[144:147], v[68:71], a[132:135]\n"
    "v_exp_f32_e32 v47, v47\n"
    "v_mfma_f32_16x16x32_bf16 a[136:139], a[144:147], v[72:75], a[136:139]\n"
    "buffer_load_dwordx4 v123, s[32:35], s42 offen offset:1024 lds\n"
    "v_cvt_pk_bf16_f32 v85, v46, v47\n"
    "v_mfma_f32_16x16x32_bf16 a[140:143], a[144:147], v[76:79], a[140:143]\n"
    "s_waitcnt lgkmcnt(0)\n"
    "v_exp_f32_e32 v48, v48\n"
    "v_mfma_f32_16x16x32_bf16 v[128:131], a[96:99], v[64:67], v[128:131]\n"
    "v_mfma_f32_16x16x32_bf16 v[160:163], a[96:99], v[68:71], v[160:163]\n"
    "v_exp_f32_e32 v49, v49\n"
    "v_mfma_f32_16x16x32_bf16 v[192:195], a[96:99], v[72:75], v[192:195]\n"
    "buffer_load_dwordx4 v124, s[32:35], s42 offen offset:2048 lds\n"
    "v_cvt_pk_bf16_f32 v88, v48, v49\n"
    "v_mfma_f32_16x16x32_bf16 v[224:227], a[96:99], v[76:79], v[224:227]\n"
    "v_exp_f32_e32 v50, v50\n"
    "v_mfma_f32_16x16x32_bf16 v[132:135], a[100:103], v[64:67], v[132:135]\n"
    "ds_read_b64_tr_b16 a[96:97], v114\n"
    "ds_read_b64_tr_b16 a[98:99], v114 offset:4096\n"
    "v_mfma_f32_16x16x32_bf16 v[164:167], a[100:103], v[68:71], v[164:167]\n"
    "v_exp_f32_e32 v51, v51\n"
    "v_mfma_f32_16x16x32_bf16 v[196:199], a[100:103], v[72:75], v[196:199]\n"
    "buffer_load_dwordx4 v125, s[32:35], s42 offen offset:3072 lds\n"
    "v_cvt_pk_bf16_f32 v89, v50, v51\n"
    "v_mfma_f32_16x16x32_bf16 v[228:231], a[100:103], v[76:79], v[228:231]\n"
    "v_exp_f32_e32 v52, v52\n"
    "v_mfma_f32_16x16x32_bf16 v[136:139], a[104:107], v[64:67], v[136:139]\n"
    "ds_read_b64_tr_b16 a[100:101], v115\n"
    "ds_read_b64_tr_b16 a[102:103], v115 offset:4096\n"
    "v_mfma_f32_16x16x32_bf16 v[168:171], a[104:107], v[68:71], v[168:171]\n"
    "v_exp_f32_e32 v53, v53\n"
    "v_mfma_f32_16x16x32_bf16 v[200:203], a[104:107], v[72:75], v[200:203]\n"
    "v_cvt_pk_bf16_f32 v82, v52, v53\n"
    "v_mfma_f32_16x16x32_bf16 v[232:235], a[104:107], v[76:79], v[232:235]\n"
    "v_exp_f32_e32 v54, v54\n"
    "v_mfma_f32_16x16x32_bf16 v[140:143], a[108:111], v[64:67], v[140:143]\n"
    "ds_read_b64_tr_b16 a[104:105], v114 offset:256\n"
    "ds_read_b64_tr_b16 a[106:107], v114 offset:4352\n"
    "v_mfma_f32_16x16x32_bf16 v[172:175], a[108:111], v[68:71], v[172:175]\n"
    "v_exp_f32_e32 v55, v55\n"
    "v_mfma_f32_16x16x32_bf16 v[204:207], a[108:111], v[72:75], v[204:207]\n"
    "v_cvt_pk_bf16_f32 v83, v54, v55\n"
    "v_mfma_f32_16x16x32_bf16 v[236:239], a[108:111], v[76:79], v[236:239]\n"
    "v_exp_f32_e32 v56, v56\n"
    "v_mfma_f32_16x16x32_bf16 v[144:147], a[112:115], v[64:67], v[144:147]\n"
    "ds_read_b64_tr_b16 a[108:109], v115 offset:256\n"
    "ds_read_b64_tr_b16 a[110:111], v115 offset:4352\n"
    "v_mfma_f32_16x16x32_bf16 v[176:179], a[112:115], v[68:71], v[176:179]\n"
    "v_exp_f32_e32 v57, v57\n"
    "v_mfma_f32_16x16x32_bf16 v[208:211], a[112:115], v[72:75], v[208:211]\n"
    "v_cvt_pk_bf16_f32 v86, v56, v57\n"
    "v_mfma_f32_16x16x32_bf16 v[240:243], a[112:115], v[76:79], v[240:243]\n"
    "v_exp_f32_e32 v58, v58\n"
    "v_mfma_f32_16x16x32_bf16 v[148:151], a[116:119], v[64:67], v[148:151]\n"
    "ds_read_b64_tr_b16 a[112:113], v114 offset:512\n"
    "ds_read_b64_tr_b16 a[114:115], v114 offset:4608\n"
    "v_mfma_f32_16x16x32_bf16 v[180:183], a[116:119], v[68:71], v[180:183]\n"
    "v_exp_f32_e32 v59, v59\n"
    "v_mfma_f32_16x16x32_bf16 v[212:215], a[116:119], v[72:75], v[212:215]\n"
    "v_cvt_pk_bf16_f32 v87, v58, v59\n"
    "v_mfma_f32_16x16x32_bf16 v[244:247], a[116:119], v[76:79], v[244:247]\n"
    "v_exp_f32_e32 v60, v60\n"
    "v_mfma_f32_16x16x32_bf16 v[152:155], a[120:123], v[64:67], v[152:155]\n"
    "ds_read_b64_tr_b16 a[116:117], v115 offset:512\n"
    "ds_read_b64_tr_b16 a[118:119], v115 offset:4608\n"
    "v_mfma_f32_16x16x32_bf16 v[184:187], a[120:123], v[68:71], v[184:187]\n"
    "v_exp_f32_e32 v61, v61\n"
    "v_mfma_f32_16x16x32_bf16 v[216:219], a[120:123], v[72:75], v[216:219]\n"
    "v_cvt_pk_bf16_f32 v90, v60, v61\n"
    "v_mfma_f32_16x16x32_bf16 v[248:251], a[120:123], v[76:79], v[248:251]\n"
    "v_exp_f32_e32 v62, v62\n"
    "v_mfma_f32_16x16x32_bf16 v[156:159], a[124:127], v[64:67], v[156:159]\n"
    "ds_read_b64_tr_b16 a[120:121], v114 offset:768\n"
    "ds_read_b64_tr_b16 a[122:123], v114 offset:4864\n"
    "v_mfma_f32_16x16x32_bf16 v[188:191], a[124:127], v[68:71], v[188:191]\n"
    "v_exp_f32_e32 v63, v63\n"
    "v_mfma_f32_16x16x32_bf16 v[220:223], a[124:127], v[72:75], v[220:223]\n"
    "v_cvt_pk_bf16_f32 v91, v62, v63\n"
    "v_mfma_f32_16x16x32_bf16 v[252:255], a[124:127], v[76:79], v[252:255]\n"
    "L_drain%=:\n"
    "ds_read_b64_tr_b16 a[124:125], v115 offset:768\n"
    "ds_read_b64_tr_b16 a[126:127], v115 offset:4864\n"
    "s_add_u32 s45, s55, 0x10000\n"
    "v_lshl_add_u32 v4, v0, 4, s45\n"
    "s_add_u32 s45, s55, 0x1c000\n"
    "v_lshl_add_u32 v5, v0, 3, s45\n"
    "s_mul_i32 s51, s47, 0x1c0000\n"
    "s_add_u32 s51, s51, 0x10000\n"
    "s_mul_i32 s45, s50, 7\n"
    "s_lshl_b32 s52, s49, 2\n"
    "s_add_u32 s45, s45, s52\n"
    "s_add_u32 s45, s45, 0\n"
    "s_lshl_b32 s45, s45, 17\n"
    "s_add_u32 s45, s45, s51\n"
    "v_lshl_add_u32 v64, v0, 4, s45\n"
    "s_mul_i32 s45, s50, 7\n"
    "s_lshl_b32 s52, s49, 2\n"
    "s_add_u32 s45, s45, s52\n"
    "s_add_u32 s45, s45, 1\n"
    "s_lshl_b32 s45, s45, 17\n"
    "s_add_u32 s45, s45, s51\n"
    "v_lshl_add_u32 v65, v0, 4, s45\n"
    "s_mul_i32 s45, s50, 7\n"
    "s_lshl_b32 s52, s49, 2\n"
    "s_add_u32 s45, s45, s52\n"
    "s_add_u32 s45, s45, 2\n"
    "s_lshl_b32 s45, s45, 17\n"
    "s_add_u32 s45, s45, s51\n"
    "v_lshl_add_u32 v66, v0, 4, s45\n"
    "s_mul_i32 s45, s50, 7\n"
    "s_add_u32 s45, s45, 3\n"
    "s_lshl_b32 s45, s45, 17\n"
    "s_add_u32 s45, s45, s51\n"
    "s_lshl_b32 s52, s49, 11\n"
    "s_add_u32 s45, s45, s52\n"
    "v_lshl_add_u32 v67, v0, 4, s45\n"
    "v_mov_b32_e32 v6, v104\n"
    "v_accvgpr_read_b32 v7, a140\n"
    "v_mov_b32_e32 v71, v7\n"
    "ds_write_b64 v5, v[6:7]\n"
    "s_cmp_eq_u32 s49, 0\n"
    "s_cbranch_scc0 L_xw1%=\n"
    "ds_write_b128 v4, v[240:243]\n"
    "ds_write_b128 v4, v[244:247] offset:1024\n"
    "ds_write_b128 v4, v[248:251] offset:2048\n"
    "ds_write_b128 v4, v[252:255] offset:3072\n"
    "s_branch L_xw2%=\n"
    "L_xw1%=:\n"
    "ds_write_b128 v4, v[224:227]\n"
    "ds_write_b128 v4, v[228:231] offset:1024\n"
    "ds_write_b128 v4, v[232:235] offset:2048\n"
    "ds_write_b128 v4, v[236:239] offset:3072\n"
    "L_xw2%=:\n"
    "s_waitcnt lgkmcnt(5)\n"
    "v_mfma_f32_16x16x32_bf16 a[128:131], a[144:147], v[80:83], a[128:131]\n"
    "v_mfma_f32_16x16x32_bf16 a[132:135], a[144:147], v[84:87], a[132:135]\n"
    "v_mfma_f32_16x16x32_bf16 a[136:139], a[144:147], v[88:91], a[136:139]\n"
    "v_mfma_f32_16x16x32_bf16 v[128:131], a[96:99], v[80:83], v[128:131]\n"
    "v_mfma_f32_16x16x32_bf16 v[132:135], a[100:103], v[80:83], v[132:135]\n"
    "v_mfma_f32_16x16x32_bf16 v[136:139], a[104:107], v[80:83], v[136:139]\n"
    "v_mfma_f32_16x16x32_bf16 v[140:143], a[108:111], v[80:83], v[140:143]\n"
    "v_mfma_f32_16x16x32_bf16 v[144:147], a[112:115], v[80:83], v[144:147]\n"
    "v_mfma_f32_16x16x32_bf16 v[148:151], a[116:119], v[80:83], v[148:151]\n"
    "v_accvgpr_read_b32 v68, a128\n"
    "v_mfma_f32_16x16x32_bf16 v[152:155], a[120:123], v[80:83], v[152:155]\n"
    "v_accvgpr_read_b32 v69, a132\n"
    "v_mfma_f32_16x16x32_bf16 v[156:159], a[124:127], v[80:83], v[156:159]\n"
    "v_accvgpr_read_b32 v70, a136\n"
    "v_mfma_f32_16x16x32_bf16 v[160:163], a[96:99], v[84:87], v[160:163]\n"
    "v_rcp_f32_e32 v72, v68\n"
    "v_mfma_f32_16x16x32_bf16 v[164:167], a[100:103], v[84:87], v[164:167]\n"
    "v_rcp_f32_e32 v74, v69\n"
    "v_mfma_f32_16x16x32_bf16 v[168:171], a[104:107], v[84:87], v[168:171]\n"
    "v_rcp_f32_e32 v76, v70\n"
    "v_mfma_f32_16x16x32_bf16 v[172:175], a[108:111], v[84:87], v[172:175]\n"
    "v_mul_f32_e32 v128, v72, v128\n"
    "v_mul_f32_e32 v129, v72, v129\n"
    "v_mfma_f32_16x16x32_bf16 v[176:179], a[112:115], v[84:87], v[176:179]\n"
    "v_mul_f32_e32 v130, v72, v130\n"
    "v_mul_f32_e32 v131, v72, v131\n"
    "v_mfma_f32_16x16x32_bf16 v[180:183], a[116:119], v[84:87], v[180:183]\n"
    "v_mul_f32_e32 v132, v72, v132\n"
    "v_mul_f32_e32 v133, v72, v133\n"
    "v_mfma_f32_16x16x32_bf16 v[184:187], a[120:123], v[84:87], v[184:187]\n"
    "v_mul_f32_e32 v134, v72, v134\n"
    "v_mul_f32_e32 v135, v72, v135\n"
    "v_mfma_f32_16x16x32_bf16 v[188:191], a[124:127], v[84:87], v[188:191]\n"
    "v_cvt_pk_bf16_f32 v8, v128, v129\n"
    "v_cvt_pk_bf16_f32 v9, v130, v131\n"
    "v_mfma_f32_16x16x32_bf16 v[192:195], a[96:99], v[88:91], v[192:195]\n"
    "v_cvt_pk_bf16_f32 v10, v132, v133\n"
    "v_cvt_pk_bf16_f32 v11, v134, v135\n"
    "v_mfma_f32_16x16x32_bf16 v[196:199], a[100:103], v[88:91], v[196:199]\n"
    "buffer_store_dwordx4 v[8:11], v64, s[56:59], s79 offen sc1\n"
    "v_mul_f32_e32 v136, v72, v136\n"
    "v_mul_f32_e32 v137, v72, v137\n"
    "v_mfma_f32_16x16x32_bf16 v[200:203], a[104:107], v[88:91], v[200:203]\n"
    "v_mul_f32_e32 v138, v72, v138\n"
    "v_mul_f32_e32 v139, v72, v139\n"
    "v_mfma_f32_16x16x32_bf16 v[204:207], a[108:111], v[88:91], v[204:207]\n"
    "v_mul_f32_e32 v140, v72, v140\n"
    "v_mul_f32_e32 v141, v72, v141\n"
    "v_mfma_f32_16x16x32_bf16 v[208:211], a[112:115], v[88:91], v[208:211]\n"
    "v_mul_f32_e32 v142, v72, v142\n"
    "v_mul_f32_e32 v143, v72, v143\n"
    "v_mfma_f32_16x16x32_bf16 v[212:215], a[116:119], v[88:91], v[212:215]\n"
    "v_cvt_pk_bf16_f32 v12, v136, v137\n"
    "v_cvt_pk_bf16_f32 v13, v138, v139\n"
    "v_mfma_f32_16x16x32_bf16 v[216:219], a[120:123], v[88:91], v[216:219]\n"
    "v_cvt_pk_bf16_f32 v14, v140, v141\n"
    "v_cvt_pk_bf16_f32 v15, v142, v143\n"
    "v_mfma_f32_16x16x32_bf16 v[220:223], a[124:127], v[88:91], v[220:223]\n"
    "buffer_store_dwordx4 v[12:15], v64, s[56:59], s79 offen offset:1024 sc1\n"
    "s_mov_b64 s[22:23], 0\n"
    "v_cmp_nge_f32_e32 vcc, 0x53800000, v68\n"
    "s_nop 1\n"
    "s_or_b64 s[22:23], s[22:23], vcc\n"
    "v_cmp_nge_f32_e32 vcc, 0x53800000, v69\n"
    "s_nop 1\n"
    "s_or_b64 s[22:23], s[22:23], vcc\n"
    "v_cmp_nge_f32_e32 vcc, 0x53800000, v70\n"
    "s_nop 1\n"
    "s_or_b64 s[22:23], s[22:23], vcc\n"
    "v_cmp_nge_f32_e32 vcc, 0x53800000, v71\n"
    "s_nop 1\n"
    "s_or_b64 s[22:23], s[22:23], vcc\n"
    "s_cmp_lg_u64 s[22:23], 0\n"
    "s_cselect_b32 s45, 1, 0\n"
    "s_andn2_b32 s45, s45, s77\n"
    "v_mov_b32_e32 v1, s45\n"
    "v_and_b32_e32 v2, 3, v0\n"
    "v_lshlrev_b32_e32 v2, 12, v2\n"
    "s_lshl_b32 s45, s44, 2\n"
    "s_add_u32 s45, s45, 0x1c200\n"
    "v_add_u32_e32 v2, s45, v2\n"
    "ds_write_b32 v2, v1\n"
    "v_xor_b32_e32 v4, 0x1000, v4\n"
    "v_xor_b32_e32 v5, 0x1000, v5\n"
    "s_add_u32 s45, s97, 0x1c200\n"
    "v_mov_b32_e32 v2, s45\n"
    "s_waitcnt lgkmcnt(0)\n"
    "s_barrier\n"
    "ds_read_b128 v[96:99], v2\n"
    "ds_read_b64 v[2:3], v5\n"
    "ds_read_b128 v[80:83], v4\n"
    "ds_read_b128 v[84:87], v4 offset:1024\n"
    "ds_read_b128 v[88:91], v4 offset:2048\n"
    "ds_read_b128 v[92:95], v4 offset:3072\n"
    "v_pk_mul_f32 v[144:145], v[144:145], v[72:73] op_sel_hi:[1,0]\n"
    "v_pk_mul_f32 v[146:147], v[146:147], v[72:73] op_sel_hi:[1,0]\n"
    "v_pk_mul_f32 v[148:149], v[148:149], v[72:73] op_sel_hi:[1,0]\n"
    "v_pk_mul_f32 v[150:151], v[150:151], v[72:73] op_sel_hi:[1,0]\n"
    "v_cvt_pk_bf16_f32 v16, v144, v145\n"
    "v_cvt_pk_bf16_f32 v17, v146, v147\n"
    "v_cvt_pk_bf16_f32 v18, v148, v149\n"
    "v_cvt_pk_bf16_f32 v19, v150, v151\n"
    "buffer_store_dwordx4 v[16:19], v64, s[56:59], s79 offen offset:2048 sc1\n"
    "v_pk_mul_f32 v[152:153], v[152:153], v[72:73] op_sel_hi:[1,0]\n"
    "v_pk_mul_f32 v[154:155], v[154:155], v[72:73] op_sel_hi:[1,0]\n"
    "v_pk_mul_f32 v[156:157], v[156:157], v[72:73] op_sel_hi:[1,0]\n"
    "v_pk_mul_f32 v[158:159], v[158:159], v[72:73] op_sel_hi:[1,0]\n"
    "v_cvt_pk_bf16_f32 v20, v152, v153\n"
    "v_cvt_pk_bf16_f32 v21, v154, v155\n"
    "v_cvt_pk_bf16_f32 v22, v156, v157\n"
    "v_cvt_pk_bf16_f32 v23, v158, v159\n"
    "buffer_store_dwordx4 v[20:23], v64, s[56:59], s79 offen offset:3072 sc1\n"
    "v_pk_mul_f32 v[160:161], v[160:161], v[74:75] op_sel_hi:[1,0]\n"
    "v_pk_mul_f32 v[162:163], v[162:163], v[74:75] op_sel_hi:[1,0]\n"
    "v_pk_mul_f32 v[164:165], v[164:165], v[74:75] op_sel_hi:[1,0]\n"
    "v_pk_mul_f32 v[166:167], v[166:167], v[74:75] op_sel_hi:[1,0]\n"
    "v_cvt_pk_bf16_f32 v24, v160, v161\n"
    "v_cvt_pk_bf16_f32 v25, v162, v163\n"
    "v_cvt_pk_bf16_f32 v26, v164, v165\n"
    "v_cvt_pk_bf16_f32 v27, v166, v167\n"
    "buffer_store_dwordx4 v[24:27], v65, s[56:59], s79 offen sc1\n"
    "v_pk_mul_f32 v[168:169], v[168:169], v[74:75] op_sel_hi:[1,0]\n"
    "v_pk_mul_f32 v[170:171], v[170:171], v[74:75] op_sel_hi:[1,0]\n"
    "v_pk_mul_f32 v[172:173], v[172:173], v[74:75] op_sel_hi:[1,0]\n"
    "v_pk_mul_f32 v[174:175], v[174:175], v[74:75] op_sel_hi:[1,0]\n"
    "v_cvt_pk_bf16_f32 v28, v168, v169\n"
    "v_cvt_pk_bf16_f32 v29, v170, v171\n"
    "v_cvt_pk_bf16_f32 v30, v172, v173\n"
    "v_cvt_pk_bf16_f32 v31, v174, v175\n"
    "buffer_store_dwordx4 v[28:31], v65, s[56:59], s79 offen offset:1024 sc1\n"
    "v_pk_mul_f32 v[176:177], v[176:177], v[74:75] op_sel_hi:[1,0]\n"
    "v_pk_mul_f32 v[178:179], v[178:179], v[74:75] op_sel_hi:[1,0]\n"
    "v_pk_mul_f32 v[180:181], v[180:181], v[74:75] op_sel_hi:[1,0]\n"
    "v_pk_mul_f32 v[182:183], v[182:183], v[74:75] op_sel_hi:[1,0]\n"
    "v_cvt_pk_bf16_f32 v32, v176, v177\n"
    "v_cvt_pk_bf16_f32 v33, v178, v179\n"
    "v_cvt_pk_bf16_f32 v34, v180, v181\n"
    "v_cvt_pk_bf16_f32 v35, v182, v183\n"
    "buffer_store_dwordx4 v[32:35], v65, s[56:59], s79 offen offset:2048 sc1\n"
    "v_pk_mul_f32 v[184:185], v[184:185], v[74:75] op_sel_hi:[1,0]\n"
    "v_pk_mul_f32 v[186:187], v[186:187], v[74:75] op_sel_hi:[1,0]\n"
    "v_pk_mul_f32 v[188:189], v[188:189], v[74:75] op_sel_hi:[1,0]\n"
    "v_pk_mul_f32 v[190:191], v[190:191], v[74:75] op_sel_hi:[1,0]\n"
    "v_cvt_pk_bf16_f32 v36, v184, v185\n"
    "v_cvt_pk_bf16_f32 v37, v186, v187\n"
    "v_cvt_pk_bf16_f32 v38, v188, v189\n"
    "v_cvt_pk_bf16_f32 v39, v190, v191\n"
    "buffer_store_dwordx4 v[36:39], v65, s[56:59], s79 offen offset:3072 sc1\n"
    "v_pk_mul_f32 v[192:193], v[192:193], v[76:77] op_sel_hi:[1,0]\n"
    "v_pk_mul_f32 v[194:195], v[194:195], v[76:77] op_sel_hi:[1,0]\n"
    "v_pk_mul_f32 v[196:197], v[196:197], v[76:77] op_sel_hi:[1,0]\n"
    "v_pk_mul_f32 v[198:199], v[198:199], v[76:77] op_sel_hi:[1,0]\n"
    "v_cvt_pk_bf16_f32 v40, v192, v193\n"
    "v_cvt_pk_bf16_f32 v41, v194, v195\n"
    "v_cvt_pk_bf16_f32 v42, v196, v197\n"
    "v_cvt_pk_bf16_f32 v43, v198, v199\n"
    "buffer_store_dwordx4 v[40:43], v66, s[56:59], s79 offen sc1\n"
    "v_pk_mul_f32 v[200:201], v[200:201], v[76:77] op_sel_hi:[1,0]\n"
    "v_pk_mul_f32 v[202:203], v[202:203], v[76:77] op_sel_hi:[1,0]\n"
    "v_pk_mul_f32 v[204:205], v[204:205], v[76:77] op_sel_hi:[1,0]\n"
    "v_pk_mul_f32 v[206:207], v[206:207], v[76:77] op_sel_hi:[1,0]\n"
    "v_cvt_pk_bf16_f32 v44, v200, v201\n"
    "v_cvt_pk_bf16_f32 v45, v202, v203\n"
    "v_cvt_pk_bf16_f32 v46, v204, v205\n"
    "v_cvt_pk_bf16_f32 v47, v206, v207\n"
    "buffer_store_dwordx4 v[44:47], v66, s[56:59], s79 offen offset:1024 sc1\n"
    "v_pk_mul_f32 v[208:209], v[208:209], v[76:77] op_sel_hi:[1,0]\n"
    "v_pk_mul_f32 v[210:211], v[210:211], v[76:77] op_sel_hi:[1,0]\n"
    "v_pk_mul_f32 v[212:213], v[212:213], v[76:77] op_sel_hi:[1,0]\n"
    "v_pk_mul_f32 v[214:215], v[214:215], v[76:77] op_sel_hi:[1,0]\n"
    "v_cvt_pk_bf16_f32 v48, v208, v209\n"
    "v_cvt_pk_bf16_f32 v49, v210, v211\n"
    "v_cvt_pk_bf16_f32 v50, v212, v213\n"
    "v_cvt_pk_bf16_f32 v51, v214, v215\n"
    "buffer_store_dwordx4 v[48:51], v66, s[56:59], s79 offen offset:2048 sc1\n"
    "v_pk_mul_f32 v[216:217], v[216:217], v[76:77] op_sel_hi:[1,0]\n"
    "v_pk_mul_f32 v[218:219], v[218:219], v[76:77] op_sel_hi:[1,0]\n"
    "v_pk_mul_f32 v[220:221], v[220:221], v[76:77] op_sel_hi:[1,0]\n"
    "v_pk_mul_f32 v[222:223], v[222:223], v[76:77] op_sel_hi:[1,0]\n"
    "v_cvt_pk_bf16_f32 v52, v216, v217\n"
    "v_cvt_pk_bf16_f32 v53, v218, v219\n"
    "v_cvt_pk_bf16_f32 v54, v220, v221\n"
    "v_cvt_pk_bf16_f32 v55, v222, v223\n"
    "buffer_store_dwordx4 v[52:55], v66, s[56:59], s79 offen offset:3072 sc1\n"
    "s_waitcnt lgkmcnt(4)\n"
    "v_or3_b32 v96, v96, v97, v98\n"
    "v_or_b32_e32 v96, v96, v99\n"
    "s_nop 0\n"
    "v_readfirstlane_b32 s45, v96\n"
    "s_nop 1\n"
    "s_cmp_lg_u32 s45, 0\n"
    "s_cbranch_scc1 L_redo%=\n"
    "v_min_f32_e32 v1, v104, v2\n"
    "v_sub_f32_e32 v4, v1, v104\n"
    "v_sub_f32_e32 v5, v1, v2\n"
    "v_exp_f32_e32 v4, v4\n"
    "v_exp_f32_e32 v5, v5\n"
    "s_nop 0\n"
    "v_mul_f32_e32 v71, v71, v4\n"
    "v_fmac_f32_e32 v71, v3, v5\n"
    "v_rcp_f32_e32 v71, v71\n"
    "s_nop 0\n"
    "v_mul_f32_e32 v4, v4, v71\n"
    "v_mul_f32_e32 v5, v5, v71\n"
    "s_waitcnt lgkmcnt(0)\n"
    "s_cmp_eq_u32 s49, 0\n"
    "s_cbranch_scc0 L_mg1%=\n"
    "v_mul_f32_e32 v80, v5, v80\n"
    "v_fmac_f32_e32 v80, v4, v224\n"
    "v_mul_f32_e32 v81, v5, v81\n"
    "v_fmac_f32_e32 v81, v4, v225\n"
    "v_mul_f32_e32 v82, v5, v82\n"
    "v_fmac_f32_e32 v82, v4, v226\n"
    "v_mul_f32_e32 v83, v5, v83\n"
    "v_fmac_f32_e32 v83, v4, v227\n"
    "v_mul_f32_e32 v84, v5, v84\n"
    "v_fmac_f32_e32 v84, v4, v228\n"
    "v_mul_f32_e32 v85, v5, v85\n"
    "v_fmac_f32_e32 v85, v4, v229\n"
    "v_mul_f32_e32 v86, v5, v86\n"
    "v_fmac_f32_e32 v86, v4, v230\n"
    "v_mul_f32_e32 v87, v5, v87\n"
    "v_fmac_f32_e32 v87, v4, v231\n"
    "v_mul_f32_e32 v88, v5, v88\n"
    "v_fmac_f32_e32 v88, v4, v232\n"
    "v_mul_f32_e32 v89, v5, v89\n"
    "v_fmac_f32_e32 v89, v4, v233\n"
    "v_mul_f32_e32 v90, v5, v90\n"
    "v_fmac_f32_e32 v90, v4, v234\n"
    "v_mul_f32_e32 v91, v5, v91\n"
    "v_fmac_f32_e32 v91, v4, v235\n"
    "v_mul_f32_e32 v92, v5, v92\n"
    "v_fmac_f32_e32 v92, v4, v236\n"
    "v_mul_f32_e32 v93, v5, v93\n"
    "v_fmac_f32_e32 v93, v4, v237\n"
    "v_mul_f32_e32 v94, v5, v94\n"
    "v_fmac_f32_e32 v94, v4, v238\n"
    "v_mul_f32_e32 v95, v5, v95\n"
    "v_fmac_f32_e32 v95, v4, v239\n"
    "s_branch L_mg2%=\n"
    "L_mg1%=:\n"
    "v_mul_f32_e32 v80, v5, v80\n"
    "v_fmac_f32_e32 v80, v4, v240\n"
    "v_mul_f32_e32 v81, v5, v81\n"
    "v_fmac_f32_e32 v81, v4, v241\n"
    "v_mul_f32_e32 v82, v5, v82\n"
    "v_fmac_f32_e32 v82, v4, v242\n"
    "v_mul_f32_e32 v83, v5, v83\n"
    "v_fmac_f32_e32 v83, v4, v243\n"
    "v_mul_f32_e32 v84, v5, v84\n"
    "v_fmac_f32_e32 v84, v4, v244\n"
    "v_mul_f32_e32 v85, v5, v85\n"
    "v_fmac_f32_e32 v85, v4, v245\n"
    "v_mul_f32_e32 v86, v5, v86\n"
    "v_fmac_f32_e32 v86, v4, v246\n"
    "v_mul_f32_e32 v87, v5, v87\n"
    "v_fmac_f32_e32 v87, v4, v247\n"
    "v_mul_f32_e32 v88, v5, v88\n"
    "v_fmac_f32_e32 v88, v4, v248\n"
    "v_mul_f32_e32 v89, v5, v89\n"
    "v_fmac_f32_e32 v89, v4, v249\n"
    "v_mul_f32_e32 v90, v5, v90\n"
    "v_fmac_f32_e32 v90, v4, v250\n"
    "v_mul_f32_e32 v91, v5, v91\n"
    "v_fmac_f32_e32 v91, v4, v251\n"
    "v_mul_f32_e32 v92, v5, v92\n"
    "v_fmac_f32_e32 v92, v4, v252\n"
    "v_mul_f32_e32 v93, v5, v93\n"
    "v_fmac_f32_e32 v93, v4, v253\n"
    "v_mul_f32_e32 v94, v5, v94\n"
    "v_fmac_f32_e32 v94, v4, v254\n"
    "v_mul_f32_e32 v95, v5, v95\n"
    "v_fmac_f32_e32 v95, v4, v255\n"
    "L_mg2%=:\n"
    "v_cvt_pk_bf16_f32 v56, v80, v81\n"
    "v_cvt_pk_bf16_f32 v57, v82, v83\n"
    "v_cvt_pk_bf16_f32 v58, v84, v85\n"
    "v_cvt_pk_bf16_f32 v59, v86, v87\n"
    "buffer_store_dwordx4 v[56:59], v67, s[56:59], s79 offen sc1\n"
    "v_cvt_pk_bf16_f32 v60, v88, v89\n"
    "v_cvt_pk_bf16_f32 v61, v90, v91\n"
    "v_cvt_pk_bf16_f32 v62, v92, v93\n"
    "v_cvt_pk_bf16_f32 v63, v94, v95\n"
    "buffer_store_dwordx4 v[60:63], v67, s[56:59], s79 offen offset:1024 sc1\n"
    "L_tail%=:\n"
    "v_lshrrev_b32_e32 v1, 5, v0\n"
    "v_mul_u32_u24_e32 v1, 0x400, v1\n"
    "v_bfe_u32 v2, v0, 2, 3\n"
    "v_lshlrev_b32_e32 v2, 6, v2\n"
    "v_bfe_u32 v3, v0, 4, 1\n"
    "v_lshlrev_b32_e32 v3, 5, v3\n"
    "v_add3_u32 v1, v1, v2, v3\n"
    "v_and_b32_e32 v2, 3, v0\n"
    "v_lshl_add_u32 v5, v2, 3, v1\n"
    "v_add_u32_e32 v108, s55, v5\n"
    "v_xor_b32_e32 v109, 32, v108\n"
    "v_add_u32_e32 v110, 0x10000, v108\n"
    "v_add_u32_e32 v111, 0x10000, v109\n"
    "v_add_u32_e32 v112, 0x20000, v108\n"
    "v_add_u32_e32 v113, 0x20000, v109\n"
    "v_xor_b32_e32 v114, 0x1000, v110\n"
    "v_xor_b32_e32 v115, 0x1000, v111\n"
    "s_waitcnt vmcnt(14)\n"
    "ds_read_b64_tr_b16 a[0:1], v112\n"
    "ds_read_b64_tr_b16 a[2:3], v112 offset:2048\n"
    "ds_read_b64_tr_b16 a[4:5], v113\n"
    "ds_read_b64_tr_b16 a[6:7], v113 offset:2048\n"
    "ds_read_b64_tr_b16 a[8:9], v112 offset:512\n"
    "ds_read_b64_tr_b16 a[10:11], v112 offset:2560\n"
    "ds_read_b64_tr_b16 a[12:13], v113 offset:512\n"
    "ds_read_b64_tr_b16 a[14:15], v113 offset:2560\n"
    "ds_read_b64_tr_b16 a[16:17], v112 offset:16384\n"
    "ds_read_b64_tr_b16 a[18:19], v112 offset:18432\n"
    "ds_read_b64_tr_b16 a[20:21], v113 offset:16384\n"
    "ds_read_b64_tr_b16 a[22:23], v113 offset:18432\n"
    "ds_read_b64_tr_b16 a[24:25], v112 offset:16896\n"
    "ds_read_b64_tr_b16 a[26:27], v112 offset:18944\n"
    "ds_read_b64_tr_b16 a[28:29], v113 offset:16896\n"
    "ds_read_b64_tr_b16 a[30:31], v113 offset:18944\n"
    "ds_read_b64_tr_b16 a[32:33], v108 offset:32768\n"
    "ds_read_b64_tr_b16 a[34:35], v108 offset:34816\n"
    "ds_read_b64_tr_b16 a[36:37], v109 offset:32768\n"
    "ds_read_b64_tr_b16 a[38:39], v109 offset:34816\n"
    "ds_read_b64_tr_b16 a[40:41], v108 offset:33280\n"
    "ds_read_b64_tr_b16 a[42:43], v108 offset:35328\n"
    "ds_read_b64_tr_b16 a[44:45], v109 offset:33280\n"
    "ds_read_b64_tr_b16 a[46:47], v109 offset:35328\n"
    "ds_read_b64_tr_b16 a[48:49], v108 offset:49152\n"
    "ds_read_b64_tr_b16 a[50:51], v108 offset:51200\n"
    "ds_read_b64_tr_b16 a[52:53], v109 offset:49152\n"
    "ds_read_b64_tr_b16 a[54:55], v109 offset:51200\n"
    "ds_read_b64_tr_b16 a[56:57], v108 offset:49664\n"
    "ds_read_b64_tr_b16 a[58:59], v108 offset:51712\n"
    "ds_read_b64_tr_b16 a[60:61], v109 offset:49664\n"
    "ds_read_b64_tr_b16 a[62:63], v109 offset:51712\n"
    "ds_read_b64_tr_b16 a[64:65], v108\n"
    "ds_read_b64_tr_b16 a[66:67], v108 offset:2048\n"
    "ds_read_b64_tr_b16 a[68:69], v109\n"
    "ds_read_b64_tr_b16 a[70:71], v109 offset:2048\n"
    "ds_read_b64_tr_b16 a[72:73], v108 offset:512\n"
    "ds_read_b64_tr_b16 a[74:75], v108 offset:2560\n"
    "ds_read_b64_tr_b16 a[76:77], v109 offset:512\n"
    "ds_read_b64_tr_b16 a[78:79], v109 offset:2560\n"
    "ds_read_b64_tr_b16 a[80:81], v108 offset:16384\n"
    "ds_read_b64_tr_b16 a[82:83], v108 offset:18432\n"
    "ds_read_b64_tr_b16 a[84:85], v109 offset:16384\n"
    "ds_read_b64_tr_b16 a[86:87], v109 offset:18432\n"
    "ds_read_b64_tr_b16 a[88:89], v108 offset:16896\n"
    "ds_read_b64_tr_b16 a[90:91], v108 offset:18944\n"
    "ds_read_b64_tr_b16 a[92:93], v109 offset:16896\n"
    "ds_read_b64_tr_b16 a[94:95], v109 offset:18944\n"
    "ds_read_b64_tr_b16 a[96:97], v110 offset:32768\n"
    "ds_read_b64_tr_b16 a[98:99], v110 offset:34816\n"
    "ds_read_b64_tr_b16 a[100:101], v111 offset:32768\n"
    "ds_read_b64_tr_b16 a[102:103], v111 offset:34816\n"
    "ds_read_b64_tr_b16 a[104:105], v110 offset:33280\n"
    "ds_read_b64_tr_b16 a[106:107], v110 offset:35328\n"
    "ds_read_b64_tr_b16 a[108:109], v111 offset:33280\n"
    "ds_read_b64_tr_b16 a[110:111], v111 offset:35328\n"
    "s_lshl_b32 s45, s47, 12\n"
    "s_lshl_b32 s51, s48, 2\n"
    "s_add_u32 s51, s51, s45\n"
    "s_add_u32 s51, s51, 0x1000\n"
    "v_lshl_add_u32 v120, v0, 7, s51\n"
    "s_lshl_b32 s51, s48, 7\n"
    "s_add_u32 s51, s51, s45\n"
    "s_add_u32 s51, s51, 0x1000\n"
    "v_and_b32_e32 v1, 31, v0\n"
    "v_lshl_add_u32 v121, v1, 2, s51\n"
    "s_lshl_b32 s45, s44, 3\n"
    "s_lshl_b32 s22, 0xff, s45\n"
    "s_mov_b32 s23, 0\n"
    "s_lshr_b32 s73, s48, 4\n"
    "s_bfe_u32 s74, s48, 0x10003\n"
    "s_mul_i32 s51, s47, 0x1c0000\n"
    "s_lshl_b32 s45, s44, 15\n"
    "s_add_u32 s51, s51, s45\n"
    "s_add_u32 s51, s51, 0x10000\n"
    "s_and_b32 s45, s48, 7\n"
    "s_mul_i32 s52, s45, 43\n"
    "s_lshr_b32 s52, s52, 7\n"
    "s_mul_i32 s52, s52, 3\n"
    "s_sub_u32 s96, s45, s52\n"
    "s_mul_i32 s45, s73, 7\n"
    "s_lshl_b32 s52, s74, 2\n"
    "s_add_u32 s75, s45, s52\n"
    "s_add_u32 s45, s96, 0\n"
    "s_add_u32 s52, s45, -3\n"
    "s_min_u32 s45, s45, s52\n"
    "s_add_u32 s45, s45, s75\n"
    "s_lshl_b32 s45, s45, 17\n"
    "s_add_u32 s45, s45, s51\n"
    "v_lshl_add_u32 v116, v0, 4, s45\n"
    "s_add_u32 s45, s96, 1\n"
    "s_add_u32 s52, s45, -3\n"
    "s_min_u32 s45, s45, s52\n"
    "s_add_u32 s45, s45, s75\n"
    "s_lshl_b32 s45, s45, 17\n"
    "s_add_u32 s45, s45, s51\n"
    "v_lshl_add_u32 v117, v0, 4, s45\n"
    "s_add_u32 s45, s96, 2\n"
    "s_add_u32 s52, s45, -3\n"
    "s_min_u32 s45, s45, s52\n"
    "s_add_u32 s45, s45, s75\n"
    "s_lshl_b32 s45, s45, 17\n"
    "s_add_u32 s45, s45, s51\n"
    "v_lshl_add_u32 v118, v0, 4, s45\n"
    "s_lshr_b32 s64, s69, 5\n"
    "s_lshr_b32 s65, s70, 5\n"
    "s_lshr_b32 s66, s71, 5\n"
    "s_lshr_b32 s67, s72, 5\n"
    "s_mul_i32 s45, s73, 7\n"
    "s_add_u32 s45, s45, 3\n"
    "s_lshl_b32 s45, s45, 17\n"
    "s_add_u32 s45, s45, s51\n"
    "s_lshl_b32 s52, s74, 7\n"
    "s_add_u32 s45, s45, s52\n"
    "v_and_b32_e32 v1, 55, v0\n"
    "v_lshl_add_u32 v1, v1, 4, s45\n"
    "v_bfe_u32 v2, v0, 3, 1\n"
    "v_lshlrev_b32_e32 v2, 10, v2\n"
    "v_add_u32_e32 v3, s64, v2\n"
    "v_and_b32_e32 v3, 0xc00, v3\n"
    "v_add_u32_e32 v119, v1, v3\n"
    "v_add_u32_e32 v3, s66, v2\n"
    "v_and_b32_e32 v3, 0xc00, v3\n"
    "v_add_u32_e32 v107, v1, v3\n"
    "s_waitcnt lgkmcnt(0)\n"
    "s_waitcnt vmcnt(0)\n"
    "s_barrier\n"
    "s_mov_b64 exec, s[22:23]\n"
    "buffer_store_dword v126, v120, s[56:59], 0 offen sc1\n"
    "s_mov_b64 exec, -1\n"
    "s_add_u32 m0, s55, 0x20000\n"
    "s_add_u32 s42, s70, 0x60000\n"
    "buffer_load_dwordx4 v122, s[32:35], s42 offen lds\n"
    "buffer_load_dwordx4 v123, s[32:35], s42 offen offset:1024 lds\n"
    "buffer_load_dwordx4 v124, s[32:35], s42 offen offset:2048 lds\n"
    "buffer_load_dwordx4 v125, s[32:35], s42 offen offset:3072 lds\n"
    "s_add_u32 m0, s55, 0x24000\n"
    "s_add_u32 s42, s71, 0x60000\n"
    "buffer_load_dwordx4 v122, s[32:35], s42 offen lds\n"
    "buffer_load_dwordx4 v123, s[32:35], s42 offen offset:1024 lds\n"
    "buffer_load_dwordx4 v124, s[32:35], s42 offen offset:2048 lds\n"
    "buffer_load_dwordx4 v125, s[32:35], s42 offen offset:3072 lds\n"
    "s_add_u32 m0, s55, 0x8000\n"
    "s_add_u32 s42, s72, 0x60000\n"
    "buffer_load_dwordx4 v122, s[32:35], s42 offen lds\n"
    "buffer_load_dwordx4 v123, s[32:35], s42 offen offset:1024 lds\n"
    "buffer_load_dwordx4 v124, s[32:35], s42 offen offset:2048 lds\n"
    "buffer_load_dwordx4 v125, s[32:35], s42 offen offset:3072 lds\n"
    "s_add_u32 m0, s55, 0xc000\n"
    "s_add_u32 s42, s69, 0x80000\n"
    "buffer_load_dwordx4 v122, s[32:35], s42 offen lds\n"
    "buffer_load_dwordx4 v123, s[32:35], s42 offen offset:1024 lds\n"
    "buffer_load_dwordx4 v124, s[32:35], s42 offen offset:2048 lds\n"
    "buffer_load_dwordx4 v125, s[32:35], s42 offen offset:3072 lds\n"
    "s_add_u32 m0, s55, 0\n"
    "s_add_u32 s42, s70, 0x80000\n"
    "buffer_load_dwordx4 v122, s[32:35], s42 offen lds\n"
    "buffer_load_dwordx4 v123, s[32:35], s42 offen offset:1024 lds\n"
    "buffer_load_dwordx4 v124, s[32:35], s42 offen offset:2048 lds\n"
    "buffer_load_dwordx4 v125, s[32:35], s42 offen offset:3072 lds\n"
    "s_add_u32 m0, s55, 0x4000\n"
    "s_add_u32 s42, s71, 0x80000\n"
    "buffer_load_dwordx4 v122, s[32:35], s42 offen lds\n"
    "buffer_load_dwordx4 v123, s[32:35], s42 offen offset:1024 lds\n"
    "buffer_load_dwordx4 v124, s[32:35], s42 offen offset:2048 lds\n"
    "buffer_load_dwordx4 v125, s[32:35], s42 offen offset:3072 lds\n"
    "s_add_u32 m0, s55, 0x18000\n"
    "s_add_u32 s42, s72, 0x80000\n"
    "buffer_load_dwordx4 v122, s[32:35], s42 offen lds\n"
    "buffer_load_dwordx4 v123, s[32:35], s42 offen offset:1024 lds\n"
    "buffer_load_dwordx4 v124, s[32:35], s42 offen offset:2048 lds\n"
    "buffer_load_dwordx4 v125, s[32:35], s42 offen offset:3072 lds\n"
    "s_add_u32 m0, s55, 0x14000\n"
    "s_add_u32 s42, s69, 0xa0000\n"
    "buffer_load_dwordx4 v122, s[32:35], s42 offen lds\n"
    "buffer_load_dwordx4 v123, s[32:35], s42 offen offset:1024 lds\n"
    "buffer_load_dwordx4 v124, s[32:35], s42 offen offset:2048 lds\n"
    "buffer_load_dwordx4 v125, s[32:35], s42 offen offset:3072 lds\n"
    "s_add_u32 m0, s97, 0x10000\n"
    "s_add_u32 s42, s70, 0xa0000\n"
    "buffer_load_dwordx4 v122, s[32:35], s42 offen lds\n"
    "buffer_load_dwordx4 v123, s[32:35], s42 offen offset:1024 lds\n"
    "buffer_load_dwordx4 v124, s[32:35], s42 offen offset:2048 lds\n"
    "buffer_load_dwordx4 v125, s[32:35], s42 offen offset:3072 lds\n"
    "s_add_u32 m0, s97, 0x1c000\n"
    "s_add_u32 s42, s71, 0xa0000\n"
    "buffer_load_dwordx4 v122, s[32:35], s42 offen lds\n"
    "buffer_load_dwordx4 v123, s[32:35], s42 offen offset:1024 lds\n"
    "buffer_load_dwordx4 v124, s[32:35], s42 offen offset:2048 lds\n"
    "buffer_load_dwordx4 v125, s[32:35], s42 offen offset:3072 lds\n"
    "s_waitcnt vmcnt(36)\n"
    "ds_read_b64_tr_b16 a[112:113], v112\n"
    "ds_read_b64_tr_b16 a[114:115], v112 offset:2048\n"
    "ds_read_b64_tr_b16 a[116:117], v113\n"
    "ds_read_b64_tr_b16 a[118:119], v113 offset:2048\n"
    "ds_read_b64_tr_b16 a[120:121], v112 offset:512\n"
    "ds_read_b64_tr_b16 a[122:123], v112 offset:2560\n"
    "ds_read_b64_tr_b16 a[124:125], v113 offset:512\n"
    "ds_read_b64_tr_b16 a[126:127], v113 offset:2560\n"
    "s_waitcnt vmcnt(32)\n"
    "ds_read_b64_tr_b16 a[128:129], v112 offset:16384\n"
    "ds_read_b64_tr_b16 a[130:131], v112 offset:18432\n"
    "ds_read_b64_tr_b16 a[132:133], v113 offset:16384\n"
    "ds_read_b64_tr_b16 a[134:135], v113 offset:18432\n"
    "ds_read_b64_tr_b16 a[136:137], v112 offset:16896\n"
    "ds_read_b64_tr_b16 a[138:139], v112 offset:18944\n"
    "ds_read_b64_tr_b16 a[140:141], v113 offset:16896\n"
    "ds_read_b64_tr_b16 a[142:143], v113 offset:18944\n"
    "s_waitcnt vmcnt(28)\n"
    "ds_read_b64_tr_b16 a[144:145], v108 offset:32768\n"
    "ds_read_b64_tr_b16 a[146:147], v108 offset:34816\n"
    "ds_read_b64_tr_b16 a[148:149], v109 offset:32768\n"
    "ds_read_b64_tr_b16 a[150:151], v109 offset:34816\n"
    "ds_read_b64_tr_b16 a[152:153], v108 offset:33280\n"
    "ds_read_b64_tr_b16 a[154:155], v108 offset:35328\n"
    "ds_read_b64_tr_b16 a[156:157], v109 offset:33280\n"
    "ds_read_b64_tr_b16 a[158:159], v109 offset:35328\n"
    "s_waitcnt vmcnt(24)\n"
    "ds_read_b64_tr_b16 v[192:193], v108 offset:49152\n"
    "ds_read_b64_tr_b16 v[194:195], v108 offset:51200\n"
    "ds_read_b64_tr_b16 v[196:197], v109 offset:49152\n"
    "ds_read_b64_tr_b16 v[198:199], v109 offset:51200\n"
    "ds_read_b64_tr_b16 v[200:201], v108 offset:49664\n"
    "ds_read_b64_tr_b16 v[202:203], v108 offset:51712\n"
    "ds_read_b64_tr_b16 v[204:205], v109 offset:49664\n"
    "ds_read_b64_tr_b16 v[206:207], v109 offset:51712\n"
    "s_waitcnt vmcnt(20)\n"
    "ds_read_b64_tr_b16 v[208:209], v108\n"
    "ds_read_b64_tr_b16 v[210:211], v108 offset:2048\n"
    "ds_read_b64_tr_b16 v[212:213], v109\n"
    "ds_read_b64_tr_b16 v[214:215], v109 offset:2048\n"
    "ds_read_b64_tr_b16 v[216:217], v108 offset:512\n"
    "ds_read_b64_tr_b16 v[218:219], v108 offset:2560\n"
    "ds_read_b64_tr_b16 v[220:221], v109 offset:512\n"
    "ds_read_b64_tr_b16 v[222:223], v109 offset:2560\n"
    "s_mov_b32 s43, 0x400000\n"
    "L_poll%=:\n"
    "buffer_load_dword v1, v121, s[56:59], 0 offen sc1\n"
    "s_waitcnt vmcnt(0)\n"
    "v_cmp_eq_u32_e32 vcc, s76, v1\n"
    "s_nop 1\n"
    "s_and_b32 s45, vcc_lo, s22\n"
    "s_cmp_eq_u32 s45, s22\n"
    "s_cbranch_scc1 L_polled%=\n"
    "s_sleep 1\n"
    "s_sub_u32 s43, s43, 1\n"
    "s_cmp_lg_u32 s43, 0\n"
    "s_cbranch_scc1 L_poll%=\n"
    "L_polled%=:\n"
    "s_add_u32 s68, s64, 0\n"
    "buffer_load_dwordx4 v[224:227], v116, s[56:59], s68 offen sc1\n"
    "s_add_u32 s75, s65, 0\n"
    "buffer_load_dwordx4 v[228:231], v116, s[56:59], s75 offen sc1\n"
    "s_add_u32 s68, s66, 0\n"
    "buffer_load_dwordx4 v[232:235], v116, s[56:59], s68 offen sc1\n"
    "s_add_u32 s75, s67, 0\n"
    "buffer_load_dwordx4 v[236:239], v116, s[56:59], s75 offen sc1\n"
    "s_add_u32 s68, s64, 0\n"
    "buffer_load_dwordx4 v[240:243], v117, s[56:59], s68 offen sc1\n"
    "s_add_u32 s75, s65, 0\n"
    "buffer_load_dwordx4 v[244:247], v117, s[56:59], s75 offen sc1\n"
    "s_add_u32 s68, s66, 0\n"
    "buffer_load_dwordx4 v[248:251], v117, s[56:59], s68 offen sc1\n"
    "s_add_u32 s75, s67, 0\n"
    "buffer_load_dwordx4 v[252:255], v117, s[56:59], s75 offen sc1\n"
    "s_add_u32 s68, s64, 0\n"
    "buffer_load_dwordx4 v[24:27], v118, s[56:59], s68 offen sc1\n"
    "s_add_u32 s75, s65, 0\n"
    "buffer_load_dwordx4 v[28:31], v118, s[56:59], s75 offen sc1\n"
    "s_add_u32 s68, s66, 0\n"
    "buffer_load_dwordx4 v[32:35], v118, s[56:59], s68 offen sc1\n"
    "s_add_u32 s75, s67, 0\n"
    "buffer_load_dwordx4 v[36:39], v118, s[56:59], s75 offen sc1\n"
    "s_mov_b32 s75, 0\n"
    "buffer_load_dwordx4 v[40:43], v119, s[56:59], s75 offen sc1\n"
    "buffer_load_dwordx4 v[44:47], v107, s[56:59], s75 offen sc1\n"
    "v_and_b32_e32 v1, 31, v0\n"
    "v_lshlrev_b32_e32 v1, 2, v1\n"
    "s_lshl_b32 s45, s47, 7\n"
    "s_nop 0\n"
    "buffer_load_dword v127, v1, s[56:59], s45 offen sc1\n"
    "s_waitcnt lgkmcnt(0)\n"
    "s_waitcnt vmcnt(11)\n"
    "v_mfma_f32_16x16x32_bf16 v[128:131], a[160:163], v[224:227], 0\n"
    "s_add_u32 s68, s64, 0x1000\n"
    "v_mfma_f32_16x16x32_bf16 v[128:131], a[176:179], v[228:231], v[128:131]\n"
    "buffer_load_dwordx4 v[56:59], v116, s[56:59], s68 offen sc1\n"
    "v_mfma_f32_16x16x32_bf16 v[128:131], a[192:195], v[232:235], v[128:131]\n"
    "s_add_u32 s75, s65, 0x1000\n"
    "v_mfma_f32_16x16x32_bf16 v[128:131], a[208:211], v[236:239], v[128:131]\n"
    "buffer_load_dwordx4 v[60:63], v116, s[56:59], s75 offen sc1\n"
    "v_mfma_f32_16x16x32_bf16 v[132:135], a[212:215], v[236:239], 0\n"
    "s_add_u32 s68, s66, 0x1000\n"
    "v_mfma_f32_16x16x32_bf16 v[132:135], a[196:199], v[232:235], v[132:135]\n"
    "buffer_load_dwordx4 v[64:67], v116, s[56:59], s68 offen sc1\n"
    "v_mfma_f32_16x16x32_bf16 v[132:135], a[180:183], v[228:231], v[132:135]\n"
    "s_add_u32 s75, s67, 0x1000\n"
    "v_mfma_f32_16x16x32_bf16 v[132:135], a[164:167], v[224:227], v[132:135]\n"
    "v_mfma_f32_16x16x32_bf16 v[136:139], a[168:171], v[224:227], 0\n"
    "buffer_load_dwordx4 v[68:71], v116, s[56:59], s75 offen sc1\n"
    "v_mfma_f32_16x16x32_bf16 v[136:139], a[184:187], v[228:231], v[136:139]\n"
    "s_add_u32 s42, s72, 0xa0000\n"
    "v_mfma_f32_16x16x32_bf16 v[136:139], a[200:203], v[232:235], v[136:139]\n"
    "s_add_u32 m0, s55, 0x20000\n"
    "v_mfma_f32_16x16x32_bf16 v[136:139], a[216:219], v[236:239], v[136:139]\n"
    "buffer_load_dwordx4 v122, s[32:35], s42 offen lds\n"
    "v_mfma_f32_16x16x32_bf16 v[140:143], a[220:223], v[236:239], 0\n"
    "v_mfma_f32_16x16x32_bf16 v[140:143], a[204:207], v[232:235], v[140:143]\n"
    "v_mfma_f32_16x16x32_bf16 v[140:143], a[188:191], v[228:231], v[140:143]\n"
    "buffer_load_dwordx4 v123, s[32:35], s42 offen offset:1024 lds\n"
    "v_mfma_f32_16x16x32_bf16 v[140:143], a[172:175], v[224:227], v[140:143]\n"
    "s_waitcnt vmcnt(13)\n"
    "v_mfma_f32_16x16x32_bf16 v[144:147], a[160:163], v[240:243], 0\n"
    "buffer_load_dwordx4 v124, s[32:35], s42 offen offset:2048 lds\n"
    "v_mfma_f32_16x16x32_bf16 v[144:147], a[176:179], v[244:247], v[144:147]\n"
    "v_mfma_f32_16x16x32_bf16 v[144:147], a[192:195], v[248:251], v[144:147]\n"
    "v_mfma_f32_16x16x32_bf16 v[144:147], a[208:211], v[252:255], v[144:147]\n"
    "buffer_load_dwordx4 v125, s[32:35], s42 offen offset:3072 lds\n"
    "v_mfma_f32_16x16x32_bf16 v[148:151], a[212:215], v[252:255], 0\n"
    "s_add_u32 s68, s64, 0x1000\n"
    "v_mfma_f32_16x16x32_bf16 v[148:151], a[196:199], v[248:251], v[148:151]\n"
    "v_mfma_f32_16x16x32_bf16 v[148:151], a[180:183], v[244:247], v[148:151]\n"
    "buffer_load_dwordx4 v[72:75], v117, s[56:59], s68 offen sc1\n"
    "v_mfma_f32_16x16x32_bf16 v[148:151], a[164:167], v[240:243], v[148:151]\n"
    "s_add_u32 s75, s65, 0x1000\n"
    "v_mfma_f32_16x16x32_bf16 v[152:155], a[168:171], v[240:243], 0\n"
    "v_mfma_f32_16x16x32_bf16 v[152:155], a[184:187], v[244:247], v[152:155]\n"
    "buffer_load_dwordx4 v[76:79], v117, s[56:59], s75 offen sc1\n"
    "v_mfma_f32_16x16x32_bf16 v[152:155], a[200:203], v[248:251], v[152:155]\n"
    "s_add_u32 s68, s66, 0x1000\n"
    "v_mfma_f32_16x16x32_bf16 v[152:155], a[216:219], v[252:255], v[152:155]\n"
    "v_mfma_f32_16x16x32_bf16 v[156:159], a[220:223], v[252:255], 0\n"
    "buffer_load_dwordx4 v[80:83], v117, s[56:59], s68 offen sc1\n"
    "v_mfma_f32_16x16x32_bf16 v[156:159], a[204:207], v[248:251], v[156:159]\n"
    "s_add_u32 s75, s67, 0x1000\n"
    "v_mfma_f32_16x16x32_bf16 v[156:159], a[188:191], v[244:247], v[156:159]\n"
    "buffer_load_dwordx4 v[84:87], v117, s[56:59], s75 offen sc1\n"
    "v_mfma_f32_16x16x32_bf16 v[156:159], a[172:175], v[240:243], v[156:159]\n"
    "s_waitcnt vmcnt(15)\n"
    "s_add_u32 s68, s64, 0x1000\n"
    "v_mfma_f32_16x16x32_bf16 v[160:163], a[160:163], v[24:27], 0\n"
    "v_mfma_f32_16x16x32_bf16 v[160:163], a[176:179], v[28:31], v[160:163]\n"
    "buffer_load_dwordx4 v[88:91], v118, s[56:59], s68 offen sc1\n"
    "v_mfma_f32_16x16x32_bf16 v[160:163], a[192:195], v[32:35], v[160:163]\n"
    "s_add_u32 s75, s65, 0x1000\n"
    "v_mfma_f32_16x16x32_bf16 v[160:163], a[208:211], v[36:39], v[160:163]\n"
    "v_mfma_f32_16x16x32_bf16 v[164:167], a[212:215], v[36:39], 0\n"
    "buffer_load_dwordx4 v[92:95], v118, s[56:59], s75 offen sc1\n"
    "v_mfma_f32_16x16x32_bf16 v[164:167], a[196:199], v[32:35], v[164:167]\n"
    "s_add_u32 s68, s66, 0x1000\n"
    "v_mfma_f32_16x16x32_bf16 v[164:167], a[180:183], v[28:31], v[164:167]\n"
    "v_mfma_f32_16x16x32_bf16 v[164:167], a[164:167], v[24:27], v[164:167]\n"
    "buffer_load_dwordx4 v[96:99], v118, s[56:59], s68 offen sc1\n"
    "v_mfma_f32_16x16x32_bf16 v[168:171], a[168:171], v[24:27], 0\n"
    "s_add_u32 s75, s67, 0x1000\n"
    "v_mfma_f32_16x16x32_bf16 v[168:171], a[184:187], v[28:31], v[168:171]\n"
    "buffer_load_dwordx4 v[100:103], v118, s[56:59], s75 offen sc1\n"
    "v_mfma_f32_16x16x32_bf16 v[168:171], a[200:203], v[32:35], v[168:171]\n"
    "s_mov_b32 s75, 0x1000\n"
    "v_mfma_f32_16x16x32_bf16 v[168:171], a[216:219], v[36:39], v[168:171]\n"
    "v_mfma_f32_16x16x32_bf16 v[172:175], a[220:223], v[36:39], 0\n"
    "buffer_load_dwordx4 v[224:227], v119, s[56:59], s75 offen sc1\n"
    "v_mfma_f32_16x16x32_bf16 v[172:175], a[204:207], v[32:35], v[172:175]\n"
    "v_mfma_f32_16x16x32_bf16 v[172:175], a[188:191], v[28:31], v[172:175]\n"
    "v_mfma_f32_16x16x32_bf16 v[172:175], a[172:175], v[24:27], v[172:175]\n"
    "s_waitcnt vmcnt(18)\n"
    "buffer_load_dwordx4 v[228:231], v107, s[56:59], s75 offen sc1\n"
    "v_mfma_f32_16x16x32_bf16 v[176:179], a[160:163], v[40:43], 0\n"
    "s_add_u32 s42, s69, 0xc0000\n"
    "v_mfma_f32_16x16x32_bf16 v[176:179], a[192:195], v[44:47], v[176:179]\n"
    "s_add_u32 m0, s55, 0x24000\n"
    "v_mfma_f32_16x16x32_bf16 v[8:11], a[208:211], v[44:47], 0\n"
    "buffer_load_dwordx4 v122, s[32:35], s42 offen lds\n"
    "v_mfma_f32_16x16x32_bf16 v[8:11], a[176:179], v[40:43], v[8:11]\n"
    "v_mfma_f32_16x16x32_bf16 v[180:183], a[164:167], v[40:43], 0\n"
    "v_mfma_f32_16x16x32_bf16 v[180:183], a[196:199], v[44:47], v[180:183]\n"
    "buffer_load_dwordx4 v123, s[32:35], s42 offen offset:1024 lds\n"
    "v_mfma_f32_16x16x32_bf16 v[12:15], a[212:215], v[44:47], 0\n"
    "v_mfma_f32_16x16x32_bf16 v[12:15], a[180:183], v[40:43], v[12:15]\n"
    "buffer_load_dwordx4 v124, s[32:35], s42 offen offset:2048 lds\n"
    "v_mfma_f32_16x16x32_bf16 v[184:187], a[168:171], v[40:43], 0\n"
    "v_mfma_f32_16x16x32_bf16 v[184:187], a[200:203], v[44:47], v[184:187]\n"
    "v_mfma_f32_16x16x32_bf16 v[16:19], a[216:219], v[44:47], 0\n"
    "buffer_load_dwordx4 v125, s[32:35], s42 offen offset:3072 lds\n"
    "v_mfma_f32_16x16x32_bf16 v[16:19], a[184:187], v[40:43], v[16:19]\n"
    "s_add_u32 s68, s64, 0x2000\n"
    "v_mfma_f32_16x16x32_bf16 v[188:191], a[172:175], v[40:43], 0\n"
    "v_mfma_f32_16x16x32_bf16 v[188:191], a[204:207], v[44:47], v[188:191]\n"
    "buffer_load_dwordx4 v[240:243], v116, s[56:59], s68 offen sc1\n"
    "v_mfma_f32_16x16x32_bf16 v[20:23], a[220:223], v[44:47], 0\n"
    "s_add_u32 s75, s65, 0x2000\n"
    "v_mfma_f32_16x16x32_bf16 v[20:23], a[188:191], v[40:43], v[20:23]\n"
    "s_waitcnt vmcnt(19)\n"
    "v_mfma_f32_16x16x32_bf16 v[128:131], a[224:227], v[56:59], v[128:131]\n"
    "buffer_load_dwordx4 v[244:247], v116, s[56:59], s75 offen sc1\n"
    "v_mfma_f32_16x16x32_bf16 v[128:131], a[240:243], v[60:63], v[128:131]\n"
    "s_add_u32 s68, s66, 0x2000\n"
    "v_mfma_f32_16x16x32_bf16 v[128:131], a[0:3], v[64:67], v[128:131]\n"
    "ds_read_b64_tr_b16 v[24:25], v108 offset:16384\n"
    "v_mfma_f32_16x16x32_bf16 v[128:131], a[16:19], v[68:71], v[128:131]\n"
    "ds_read_b64_tr_b16 v[26:27], v108 offset:18432\n"
    "buffer_load_dwordx4 v[248:251], v116, s[56:59], s68 offen sc1\n"
    "v_mfma_f32_16x16x32_bf16 v[132:135], a[20:23], v[68:71], v[132:135]\n"
    "s_add_u32 s75, s67, 0x2000\n"
    "ds_read_b64_tr_b16 v[28:29], v109 offset:16384\n"
    "v_mfma_f32_16x16x32_bf16 v[132:135], a[4:7], v[64:67], v[132:135]\n"
    "ds_read_b64_tr_b16 v[30:31], v109 offset:18432\n"
    "buffer_load_dwordx4 v[252:255], v116, s[56:59], s75 offen sc1\n"
    "v_mfma_f32_16x16x32_bf16 v[132:135], a[244:247], v[60:63], v[132:135]\n"
    "s_add_u32 s68, s64, 0x2000\n"
    "ds_read_b64_tr_b16 v[32:33], v108 offset:16896\n"
    "v_mfma_f32_16x16x32_bf16 v[132:135], a[228:231], v[56:59], v[132:135]\n"
    "ds_read_b64_tr_b16 v[34:35], v108 offset:18944\n"
    "v_mfma_f32_16x16x32_bf16 v[136:139], a[232:235], v[56:59], v[136:139]\n"
    "ds_read_b64_tr_b16 v[36:37], v109 offset:16896\n"
    "buffer_load_dwordx4 v[40:43], v117, s[56:59], s68 offen sc1\n"
    "v_mfma_f32_16x16x32_bf16 v[136:139], a[248:251], v[60:63], v[136:139]\n"
    "s_add_u32 s75, s65, 0x2000\n"
    "ds_read_b64_tr_b16 v[38:39], v109 offset:18944\n"
    "v_mfma_f32_16x16x32_bf16 v[136:139], a[8:11], v[64:67], v[136:139]\n"
    "v_mfma_f32_16x16x32_bf16 v[136:139], a[24:27], v[68:71], v[136:139]\n"
    "buffer_load_dwordx4 v[44:47], v117, s[56:59], s75 offen sc1\n"
    "v_mfma_f32_16x16x32_bf16 v[140:143], a[28:31], v[68:71], v[140:143]\n"
    "s_add_u32 s68, s66, 0x2000\n"
    "ds_read_b64_tr_b16 a[160:161], v110 offset:32768\n"
    "v_mfma_f32_16x16x32_bf16 v[140:143], a[12:15], v[64:67], v[140:143]\n"
    "ds_read_b64_tr_b16 a[162:163], v110 offset:34816\n"
    "v_mfma_f32_16x16x32_bf16 v[140:143], a[252:255], v[60:63], v[140:143]\n"
    "ds_read_b64_tr_b16 a[164:165], v111 offset:32768\n"
    "buffer_load_dwordx4 v[48:51], v117, s[56:59], s68 offen sc1\n"
    "v_mfma_f32_16x16x32_bf16 v[140:143], a[236:239], v[56:59], v[140:143]\n"
    "s_waitcnt vmcnt(17)\n"
    "s_add_u32 s75, s67, 0x2000\n"
    "ds_read_b64_tr_b16 a[166:167], v111 offset:34816\n"
    "v_mfma_f32_16x16x32_bf16 v[144:147], a[224:227], v[72:75], v[144:147]\n"
    "ds_read_b64_tr_b16 a[168:169], v110 offset:33280\n"
    "v_mfma_f32_16x16x32_bf16 v[144:147], a[240:243], v[76:79], v[144:147]\n"
    "ds_read_b64_tr_b16 a[170:171], v110 offset:35328\n"
    "buffer_load_dwordx4 v[52:55], v117, s[56:59], s75 offen sc1\n"
    "v_mfma_f32_16x16x32_bf16 v[144:147], a[0:3], v[80:83], v[144:147]\n"
    "s_add_u32 s68, s64, 0x2000\n"
    "ds_read_b64_tr_b16 a[172:173], v111 offset:33280\n"
    "v_mfma_f32_16x16x32_bf16 v[144:147], a[16:19], v[84:87], v[144:147]\n"
    "ds_read_b64_tr_b16 a[174:175], v111 offset:35328\n"
    "buffer_load_dwordx4 a[176:179], v118, s[56:59], s68 offen sc1\n"
    "v_mfma_f32_16x16x32_bf16 v[148:151], a[20:23], v[84:87], v[148:151]\n"
    "s_add_u32 s75, s65, 0x2000\n"
    "v_mfma_f32_16x16x32_bf16 v[148:151], a[4:7], v[80:83], v[148:151]\n"
    "v_mfma_f32_16x16x32_bf16 v[148:151], a[244:247], v[76:79], v[148:151]\n"
    "s_waitcnt lgkmcnt(8)\n"
    "buffer_load_dwordx4 a[180:183], v118, s[56:59], s75 offen sc1\n"
    "v_mfma_f32_16x16x32_bf16 v[148:151], a[228:231], v[72:75], v[148:151]\n"
    "s_add_u32 s68, s66, 0x2000\n"
    "v_mfma_f32_16x16x32_bf16 v[152:155], a[232:235], v[72:75], v[152:155]\n"
    "v_mfma_f32_16x16x32_bf16 v[152:155], a[248:251], v[76:79], v[152:155]\n"
    "buffer_load_dwordx4 a[184:187], v118, s[56:59], s68 offen sc1\n"
    "v_mfma_f32_16x16x32_bf16 v[152:155], a[8:11], v[80:83], v[152:155]\n"
    "s_add_u32 s75, s67, 0x2000\n"
    "v_mfma_f32_16x16x32_bf16 v[152:155], a[24:27], v[84:87], v[152:155]\n"
    "v_mfma_f32_16x16x32_bf16 v[156:159], a[28:31], v[84:87], v[156:159]\n"
    "buffer_load_dwordx4 a[188:191], v118, s[56:59], s75 offen sc1\n"
    "v_mfma_f32_16x16x32_bf16 v[156:159], a[12:15], v[80:83], v[156:159]\n"
    "s_add_u32 s42, s70, 0xc0000\n"
    "v_mfma_f32_16x16x32_bf16 v[156:159], a[252:255], v[76:79], v[156:159]\n"
    "s_add_u32 m0, s55, 0\n"
    "v_mfma_f32_16x16x32_bf16 v[156:159], a[236:239], v[72:75], v[156:159]\n"
    "s_waitcnt vmcnt(18)\n"
    "buffer_load_dwordx4 v122, s[32:35], s42 offen lds\n"
    "v_mfma_f32_16x16x32_bf16 v[160:163], a[224:227], v[88:91], v[160:163]\n"
    "s_waitcnt lgkmcnt(0)\n"
    "v_mfma_f32_16x16x32_bf16 v[160:163], a[240:243], v[92:95], v[160:163]\n"
    "buffer_load_dwordx4 v123, s[32:35], s42 offen offset:1024 lds\n"
    "v_mfma_f32_16x16x32_bf16 v[160:163], a[0:3], v[96:99], v[160:163]\n"
    "v_mfma_f32_16x16x32_bf16 v[160:163], a[16:19], v[100:103], v[160:163]\n"
    "v_mfma_f32_16x16x32_bf16 v[164:167], a[20:23], v[100:103], v[164:167]\n"
    "buffer_load_dwordx4 v124, s[32:35], s42 offen offset:2048 lds\n"
    "v_mfma_f32_16x16x32_bf16 v[164:167], a[4:7], v[96:99], v[164:167]\n"
    "v_mfma_f32_16x16x32_bf16 v[164:167], a[244:247], v[92:95], v[164:167]\n"
    "v_mfma_f32_16x16x32_bf16 v[164:167], a[228:231], v[88:91], v[164:167]\n"
    "buffer_load_dwordx4 v125, s[32:35], s42 offen offset:3072 lds\n"
    "v_mfma_f32_16x16x32_bf16 v[168:171], a[232:235], v[88:91], v[168:171]\n"
    "s_mov_b32 s75, 0x2000\n"
    "v_mfma_f32_16x16x32_bf16 v[168:171], a[248:251], v[92:95], v[168:171]\n"
    "v_mfma_f32_16x16x32_bf16 v[168:171], a[8:11], v[96:99], v[168:171]\n"
    "buffer_load_dwordx4 a[192:195], v119, s[56:59], s75 offen sc1\n"
    "v_mfma_f32_16x16x32_bf16 v[168:171], a[24:27], v[100:103], v[168:171]\n"
    "v_mfma_f32_16x16x32_bf16 v[172:175], a[28:31], v[100:103], v[172:175]\n"
    "v_mfma_f32_16x16x32_bf16 v[172:175], a[12:15], v[96:99], v[172:175]\n"
    "buffer_load_dwordx4 a[196:199], v107, s[56:59], s75 offen sc1\n"
    "v_mfma_f32_16x16x32_bf16 v[172:175], a[252:255], v[92:95], v[172:175]\n"
    "s_add_u32 s68, s64, 0x3000\n"
    "v_mfma_f32_16x16x32_bf16 v[172:175], a[236:239], v[88:91], v[172:175]\n"
    "s_waitcnt vmcnt(22)\n"
    "v_mfma_f32_16x16x32_bf16 v[176:179], a[224:227], v[224:227], v[176:179]\n"
    "buffer_load_dwordx4 a[208:211], v116, s[56:59], s68 offen sc1\n"
    "v_mfma_f32_16x16x32_bf16 v[176:179], a[0:3], v[228:231], v[176:179]\n"
    "s_add_u32 s75, s65, 0x3000\n"
    "v_mfma_f32_16x16x32_bf16 v[8:11], a[16:19], v[228:231], v[8:11]\n"
    "buffer_load_dwordx4 a[212:215], v116, s[56:59], s75 offen sc1\n"
    "v_mfma_f32_16x16x32_bf16 v[8:11], a[240:243], v[224:227], v[8:11]\n"
    "s_add_u32 s68, s66, 0x3000\n"
    "v_mfma_f32_16x16x32_bf16 v[180:183], a[228:231], v[224:227], v[180:183]\n"
    "v_mfma_f32_16x16x32_bf16 v[180:183], a[4:7], v[228:231], v[180:183]\n"
    "buffer_load_dwordx4 a[216:219], v116, s[56:59], s68 offen sc1\n"
    "v_mfma_f32_16x16x32_bf16 v[12:15], a[20:23], v[228:231], v[12:15]\n"
    "s_add_u32 s75, s67, 0x3000\n"
    "v_mfma_f32_16x16x32_bf16 v[12:15], a[244:247], v[224:227], v[12:15]\n"
    "v_mfma_f32_16x16x32_bf16 v[184:187], a[232:235], v[224:227], v[184:187]\n"
    "buffer_load_dwordx4 a[220:223], v116, s[56:59], s75 offen sc1\n"
    "v_mfma_f32_16x16x32_bf16 v[184:187], a[8:11], v[228:231], v[184:187]\n"
    "s_add_u32 s42, s71, 0xc0000\n"
    "v_mfma_f32_16x16x32_bf16 v[16:19], a[24:27], v[228:231], v[16:19]\n"
    "s_add_u32 m0, s55, 0x8000\n"
    "v_mfma_f32_16x16x32_bf16 v[16:19], a[248:251], v[224:227], v[16:19]\n"
    "buffer_load_dwordx4 v122, s[32:35], s42 offen lds\n"
    "v_mfma_f32_16x16x32_bf16 v[188:191], a[236:239], v[224:227], v[188:191]\n"
    "v_mfma_f32_16x16x32_bf16 v[188:191], a[12:15], v[228:231], v[188:191]\n"
    "buffer_load_dwordx4 v123, s[32:35], s42 offen offset:1024 lds\n"
    "v_mfma_f32_16x16x32_bf16 v[20:23], a[28:31], v[228:231], v[20:23]\n"
    "v_mfma_f32_16x16x32_bf16 v[20:23], a[252:255], v[224:227], v[20:23]\n"
    "s_waitcnt vmcnt(20)\n"
    "v_mfma_f32_16x16x32_bf16 v[128:131], a[32:35], v[240:243], v[128:131]\n"
    "buffer_load_dwordx4 v124, s[32:35], s42 offen offset:2048 lds\n"
    "v_mfma_f32_16x16x32_bf16 v[128:131], a[48:51], v[244:247], v[128:131]\n"
    "v_mfma_f32_16x16x32_bf16 v[128:131], a[64:67], v[248:251], v[128:131]\n"
    "ds_read_b64_tr_b16 v[56:57], v110 offset:16384\n"
    "v_mfma_f32_16x16x32_bf16 v[128:131], a[80:83], v[252:255], v[128:131]\n"
    "ds_read_b64_tr_b16 v[58:59], v110 offset:18432\n"
    "buffer_load_dwordx4 v125, s[32:35], s42 offen offset:3072 lds\n"
    "v_mfma_f32_16x16x32_bf16 v[132:135], a[84:87], v[252:255], v[132:135]\n"
    "s_add_u32 s68, s64, 0x3000\n"
    "ds_read_b64_tr_b16 v[60:61], v111 offset:16384\n"
    "v_mfma_f32_16x16x32_bf16 v[132:135], a[68:71], v[248:251], v[132:135]\n"
    "ds_read_b64_tr_b16 v[62:63], v111 offset:18432\n"
    "v_mfma_f32_16x16x32_bf16 v[132:135], a[52:55], v[244:247], v[132:135]\n"
    "ds_read_b64_tr_b16 v[64:65], v110 offset:16896\n"
    "buffer_load_dwordx4 v[72:75], v117, s[56:59], s68 offen sc1\n"
    "v_mfma_f32_16x16x32_bf16 v[132:135], a[36:39], v[240:243], v[132:135]\n"
    "s_add_u32 s75, s65, 0x3000\n"
    "ds_read_b64_tr_b16 v[66:67], v110 offset:18944\n"
    "v_mfma_f32_16x16x32_bf16 v[136:139], a[40:43], v[240:243], v[136:139]\n"
    "ds_read_b64_tr_b16 v[68:69], v111 offset:16896\n"
    "v_mfma_f32_16x16x32_bf16 v[136:139], a[56:59], v[244:247], v[136:139]\n"
    "ds_read_b64_tr_b16 v[70:71], v111 offset:18944\n"
    "buffer_load_dwordx4 v[76:79], v117, s[56:59], s75 offen sc1\n"
    "v_mfma_f32_16x16x32_bf16 v[136:139], a[72:75], v[248:251], v[136:139]\n"
    "s_add_u32 s68, s66, 0x3000\n"
    "v_mfma_f32_16x16x32_bf16 v[136:139], a[88:91], v[252:255], v[136:139]\n"
    "v_mfma_f32_16x16x32_bf16 v[140:143], a[92:95], v[252:255], v[140:143]\n"
    "ds_read_b64_tr_b16 v[88:89], v114\n"
    "buffer_load_dwordx4 v[80:83], v117, s[56:59], s68 offen sc1\n"
    "v_mfma_f32_16x16x32_bf16 v[140:143], a[76:79], v[248:251], v[140:143]\n"
    "s_add_u32 s75, s67, 0x3000\n"
    "ds_read_b64_tr_b16 v[90:91], v114 offset:2048\n"
    "v_mfma_f32_16x16x32_bf16 v[140:143], a[60:63], v[244:247], v[140:143]\n"
    "ds_read_b64_tr_b16 v[92:93], v115\n"
    "buffer_load_dwordx4 v[84:87], v117, s[56:59], s75 offen sc1\n"
    "v_mfma_f32_16x16x32_bf16 v[140:143], a[44:47], v[240:243], v[140:143]\n"
    "s_waitcnt vmcnt(22)\n"
    "s_add_u32 s68, s64, 0x3000\n"
    "ds_read_b64_tr_b16 v[94:95], v115 offset:2048\n"
    "v_mfma_f32_16x16x32_bf16 v[144:147], a[32:35], v[40:43], v[144:147]\n"
    "ds_read_b64_tr_b16 v[96:97], v114 offset:512\n"
    "v_mfma_f32_16x16x32_bf16 v[144:147], a[48:51], v[44:47], v[144:147]\n"
    "ds_read_b64_tr_b16 v[98:99], v114 offset:2560\n"
    "buffer_load_dwordx4 v[224:227], v118, s[56:59], s68 offen sc1\n"
    "v_mfma_f32_16x16x32_bf16 v[144:147], a[64:67], v[48:51], v[144:147]\n"
    "s_add_u32 s75, s65, 0x3000\n"
    "ds_read_b64_tr_b16 v[100:101], v115 offset:512\n"
    "v_mfma_f32_16x16x32_bf16 v[144:147], a[80:83], v[52:55], v[144:147]\n"
    "ds_read_b64_tr_b16 v[102:103], v115 offset:2560\n"
    "v_mfma_f32_16x16x32_bf16 v[148:151], a[84:87], v[52:55], v[148:151]\n"
    "buffer_load_dwordx4 v[228:231], v118, s[56:59], s75 offen sc1\n"
    "v_mfma_f32_16x16x32_bf16 v[148:151], a[68:71], v[48:51], v[148:151]\n"
    "s_add_u32 s68, s66, 0x3000\n"
    "v_mfma_f32_16x16x32_bf16 v[148:151], a[52:55], v[44:47], v[148:151]\n"
    "s_waitcnt lgkmcnt(8)\n"
    "v_mfma_f32_16x16x32_bf16 v[148:151], a[36:39], v[40:43], v[148:151]\n"
    "buffer_load_dwordx4 v[232:235], v118, s[56:59], s68 offen sc1\n"
    "v_mfma_f32_16x16x32_bf16 v[152:155], a[40:43], v[40:43], v[152:155]\n"
    "s_add_u32 s75, s67, 0x3000\n"
    "v_mfma_f32_16x16x32_bf16 v[152:155], a[56:59], v[44:47], v[152:155]\n"
    "v_mfma_f32_16x16x32_bf16 v[152:155], a[72:75], v[48:51], v[152:155]\n"
    "buffer_load_dwordx4 v[236:239], v118, s[56:59], s75 offen sc1\n"
    "v_mfma_f32_16x16x32_bf16 v[152:155], a[88:91], v[52:55], v[152:155]\n"
    "v_mfma_f32_16x16x32_bf16 v[156:159], a[92:95], v[52:55], v[156:159]\n"
    "v_mfma_f32_16x16x32_bf16 v[156:159], a[76:79], v[48:51], v[156:159]\n"
    "s_mov_b32 s75, 0x3000\n"
    "v_mfma_f32_16x16x32_bf16 v[156:159], a[60:63], v[44:47], v[156:159]\n"
    "v_mfma_f32_16x16x32_bf16 v[156:159], a[44:47], v[40:43], v[156:159]\n"
    "s_waitcnt vmcnt(22)\n"
    "v_mfma_f32_16x16x32_bf16 v[160:163], a[32:35], a[176:179], v[160:163]\n"
    "s_waitcnt lgkmcnt(0)\n"
    "buffer_load_dwordx4 a[224:227], v119, s[56:59], s75 offen sc1\n"
    "v_mfma_f32_16x16x32_bf16 v[160:163], a[48:51], a[180:183], v[160:163]\n"
    "v_mfma_f32_16x16x32_bf16 v[160:163], a[64:67], a[184:187], v[160:163]\n"
    "buffer_load_dwordx4 a[228:231], v107, s[56:59], s75 offen sc1\n"
    "v_mfma_f32_16x16x32_bf16 v[160:163], a[80:83], a[188:191], v[160:163]\n"
    "s_add_u32 s42, s72, 0xc0000\n"
    "v_mfma_f32_16x16x32_bf16 v[164:167], a[84:87], a[188:191], v[164:167]\n"
    "s_add_u32 m0, s55, 0xc000\n"
    "v_mfma_f32_16x16x32_bf16 v[164:167], a[68:71], a[184:187], v[164:167]\n"
    "buffer_load_dwordx4 v122, s[32:35], s42 offen lds\n"
    "v_mfma_f32_16x16x32_bf16 v[164:167], a[52:55], a[180:183], v[164:167]\n"
    "v_mfma_f32_16x16x32_bf16 v[164:167], a[36:39], a[176:179], v[164:167]\n"
    "v_mfma_f32_16x16x32_bf16 v[168:171], a[40:43], a[176:179], v[168:171]\n"
    "buffer_load_dwordx4 v123, s[32:35], s42 offen offset:1024 lds\n"
    "v_mfma_f32_16x16x32_bf16 v[168:171], a[56:59], a[180:183], v[168:171]\n"
    "v_mfma_f32_16x16x32_bf16 v[168:171], a[72:75], a[184:187], v[168:171]\n"
    "v_mfma_f32_16x16x32_bf16 v[168:171], a[88:91], a[188:191], v[168:171]\n"
    "buffer_load_dwordx4 v124, s[32:35], s42 offen offset:2048 lds\n"
    "v_mfma_f32_16x16x32_bf16 v[172:175], a[92:95], a[188:191], v[172:175]\n"
    "v_mfma_f32_16x16x32_bf16 v[172:175], a[76:79], a[184:187], v[172:175]\n"
    "v_mfma_f32_16x16x32_bf16 v[172:175], a[60:63], a[180:183], v[172:175]\n"
    "buffer_load_dwordx4 v125, s[32:35], s42 offen offset:3072 lds\n"
    "v_mfma_f32_16x16x32_bf16 v[172:175], a[44:47], a[176:179], v[172:175]\n"
    "s_waitcnt vmcnt(22)\n"
    "s_add_u32 s68, s64, 0x4000\n"
    "v_mfma_f32_16x16x32_bf16 v[176:179], a[32:35], a[192:195], v[176:179]\n"
    "buffer_load_dwordx4 a[240:243], v116, s[56:59], s68 offen sc1\n"
    "v_mfma_f32_16x16x32_bf16 v[176:179], a[64:67], a[196:199], v[176:179]\n"
    "s_add_u32 s75, s65, 0x4000\n"
    "v_mfma_f32_16x16x32_bf16 v[8:11], a[80:83], a[196:199], v[8:11]\n"
    "v_mfma_f32_16x16x32_bf16 v[8:11], a[48:51], a[192:195], v[8:11]\n"
    "buffer_load_dwordx4 a[244:247], v116, s[56:59], s75 offen sc1\n"
    "v_mfma_f32_16x16x32_bf16 v[180:183], a[36:39], a[192:195], v[180:183]\n"
    "s_add_u32 s68, s66, 0x4000\n"
    "v_mfma_f32_16x16x32_bf16 v[180:183], a[68:71], a[196:199], v[180:183]\n"
    "v_mfma_f32_16x16x32_bf16 v[12:15], a[84:87], a[196:199], v[12:15]\n"
    "buffer_load_dwordx4 a[248:251], v116, s[56:59], s68 offen sc1\n"
    "v_mfma_f32_16x16x32_bf16 v[12:15], a[52:55], a[192:195], v[12:15]\n"
    "s_add_u32 s75, s67, 0x4000\n"
    "v_mfma_f32_16x16x32_bf16 v[184:187], a[40:43], a[192:195], v[184:187]\n"
    "v_mfma_f32_16x16x32_bf16 v[184:187], a[72:75], a[196:199], v[184:187]\n"
    "buffer_load_dwordx4 a[252:255], v116, s[56:59], s75 offen sc1\n"
    "v_mfma_f32_16x16x32_bf16 v[16:19], a[88:91], a[196:199], v[16:19]\n"
    "v_mfma_f32_16x16x32_bf16 v[16:19], a[56:59], a[192:195], v[16:19]\n"
    "v_mfma_f32_16x16x32_bf16 v[188:191], a[44:47], a[192:195], v[188:191]\n"
    "v_mfma_f32_16x16x32_bf16 v[188:191], a[76:79], a[196:199], v[188:191]\n"
    "s_add_u32 s68, s64, 0x4000\n"
    "v_mfma_f32_16x16x32_bf16 v[20:23], a[92:95], a[196:199], v[20:23]\n"
    "v_mfma_f32_16x16x32_bf16 v[20:23], a[60:63], a[192:195], v[20:23]\n"
    "s_waitcnt vmcnt(22)\n"
    "v_mfma_f32_16x16x32_bf16 v[128:131], a[96:99], a[208:211], v[128:131]\n"
    "buffer_load_dwordx4 a[0:3], v117, s[56:59], s68 offen sc1\n"
    "v_mfma_f32_16x16x32_bf16 v[128:131], a[112:115], a[212:215], v[128:131]\n"
    "s_add_u32 s75, s65, 0x4000\n"
    "v_mfma_f32_16x16x32_bf16 v[128:131], a[128:131], a[216:219], v[128:131]\n"
    "ds_read_b64_tr_b16 a[16:17], v114 offset:49152\n"
    "buffer_load_dwordx4 a[4:7], v117, s[56:59], s75 offen sc1\n"
    "v_mfma_f32_16x16x32_bf16 v[128:131], a[144:147], a[220:223], v[128:131]\n"
    "s_add_u32 s68, s66, 0x4000\n"
    "ds_read_b64_tr_b16 a[18:19], v114 offset:51200\n"
    "v_mfma_f32_16x16x32_bf16 v[132:135], a[148:151], a[220:223], v[132:135]\n"
    "ds_read_b64_tr_b16 a[20:21], v115 offset:49152\n"
    "v_mfma_f32_16x16x32_bf16 v[132:135], a[132:135], a[216:219], v[132:135]\n"
    "ds_read_b64_tr_b16 a[22:23], v115 offset:51200\n"
    "buffer_load_dwordx4 a[8:11], v117, s[56:59], s68 offen sc1\n"
    "v_mfma_f32_16x16x32_bf16 v[132:135], a[116:119], a[212:215], v[132:135]\n"
    "s_add_u32 s75, s67, 0x4000\n"
    "ds_read_b64_tr_b16 a[24:25], v114 offset:49664\n"
    "v_mfma_f32_16x16x32_bf16 v[132:135], a[100:103], a[208:211], v[132:135]\n"
    "ds_read_b64_tr_b16 a[26:27], v114 offset:51712\n"
    "v_mfma_f32_16x16x32_bf16 v[136:139], a[104:107], a[208:211], v[136:139]\n"
    "ds_read_b64_tr_b16 a[28:29], v115 offset:49664\n"
    "buffer_load_dwordx4 a[12:15], v117, s[56:59], s75 offen sc1\n"
    "v_mfma_f32_16x16x32_bf16 v[136:139], a[120:123], a[212:215], v[136:139]\n"
    "s_add_u32 s42, s69, 0xe0000\n"
    "ds_read_b64_tr_b16 a[30:31], v115 offset:51712\n"
    "v_mfma_f32_16x16x32_bf16 v[136:139], a[136:139], a[216:219], v[136:139]\n"
    "s_add_u32 m0, s55, 0x4000\n"
    "v_mfma_f32_16x16x32_bf16 v[136:139], a[152:155], a[220:223], v[136:139]\n"
    "s_waitcnt vmcnt(62)\n"
    "buffer_load_dwordx4 v122, s[32:35], s42 offen lds\n"
    "v_mfma_f32_16x16x32_bf16 v[140:143], a[156:159], a[220:223], v[140:143]\n"
    "ds_read_b64_tr_b16 v[240:241], v112\n"
    "v_mfma_f32_16x16x32_bf16 v[140:143], a[140:143], a[216:219], v[140:143]\n"
    "ds_read_b64_tr_b16 v[242:243], v112 offset:2048\n"
    "v_mfma_f32_16x16x32_bf16 v[140:143], a[124:127], a[212:215], v[140:143]\n"
    "ds_read_b64_tr_b16 v[244:245], v113\n"
    "buffer_load_dwordx4 v123, s[32:35], s42 offen offset:1024 lds\n"
    "v_mfma_f32_16x16x32_bf16 v[140:143], a[108:111], a[208:211], v[140:143]\n"
    "s_waitcnt vmcnt(20)\n"
    "ds_read_b64_tr_b16 v[246:247], v113 offset:2048\n"
    "v_mfma_f32_16x16x32_bf16 v[144:147], a[96:99], v[72:75], v[144:147]\n"
    "ds_read_b64_tr_b16 v[248:249], v112 offset:512\n"
    "buffer_load_dwordx4 v124, s[32:35], s42 offen offset:2048 lds\n"
    "v_mfma_f32_16x16x32_bf16 v[144:147], a[112:115], v[76:79], v[144:147]\n"
    "ds_read_b64_tr_b16 v[250:251], v112 offset:2560\n"
    "v_mfma_f32_16x16x32_bf16 v[144:147], a[128:131], v[80:83], v[144:147]\n"
    "ds_read_b64_tr_b16 v[252:253], v113 offset:512\n"
    "v_mfma_f32_16x16x32_bf16 v[144:147], a[144:147], v[84:87], v[144:147]\n"
    "ds_read_b64_tr_b16 v[254:255], v113 offset:2560\n"
    "buffer_load_dwordx4 v125, s[32:35], s42 offen offset:3072 lds\n"
    "v_mfma_f32_16x16x32_bf16 v[148:151], a[148:151], v[84:87], v[148:151]\n"
    "s_add_u32 s68, s64, 0x4000\n"
    "v_mfma_f32_16x16x32_bf16 v[148:151], a[132:135], v[80:83], v[148:151]\n"
    "v_mfma_f32_16x16x32_bf16 v[148:151], a[116:119], v[76:79], v[148:151]\n"
    "s_waitcnt lgkmcnt(8)\n"
    "buffer_load_dwordx4 v[40:43], v118, s[56:59], s68 offen sc1\n"
    "v_mfma_f32_16x16x32_bf16 v[148:151], a[100:103], v[72:75], v[148:151]\n"
    "s_add_u32 s75, s65, 0x4000\n"
    "v_mfma_f32_16x16x32_bf16 v[152:155], a[104:107], v[72:75], v[152:155]\n"
    "v_mfma_f32_16x16x32_bf16 v[152:155], a[120:123], v[76:79], v[152:155]\n"
    "buffer_load_dwordx4 v[44:47], v118, s[56:59], s75 offen sc1\n"
    "v_mfma_f32_16x16x32_bf16 v[152:155], a[136:139], v[80:83], v[152:155]\n"
    "s_add_u32 s68, s66, 0x4000\n"
    "v_mfma_f32_16x16x32_bf16 v[152:155], a[152:155], v[84:87], v[152:155]\n"
    "v_mfma_f32_16x16x32_bf16 v[156:159], a[156:159], v[84:87], v[156:159]\n"
    "buffer_load_dwordx4 v[48:51], v118, s[56:59], s68 offen sc1\n"
    "v_mfma_f32_16x16x32_bf16 v[156:159], a[140:143], v[80:83], v[156:159]\n"
    "s_add_u32 s75, s67, 0x4000\n"
    "v_mfma_f32_16x16x32_bf16 v[156:159], a[124:127], v[76:79], v[156:159]\n"
    "buffer_load_dwordx4 v[52:55], v118, s[56:59], s75 offen sc1\n"
    "v_mfma_f32_16x16x32_bf16 v[156:159], a[108:111], v[72:75], v[156:159]\n"
    "s_waitcnt vmcnt(22)\n"
    "s_mov_b32 s75, 0x4000\n"
    "v_mfma_f32_16x16x32_bf16 v[160:163], a[96:99], v[224:227], v[160:163]\n"
    "s_waitcnt lgkmcnt(0)\n"
    "v_mfma_f32_16x16x32_bf16 v[160:163], a[112:115], v[228:231], v[160:163]\n"
    "buffer_load_dwordx4 a[176:179], v119, s[56:59], s75 offen sc1\n"
    "v_mfma_f32_16x16x32_bf16 v[160:163], a[128:131], v[232:235], v[160:163]\n"
    "v_mfma_f32_16x16x32_bf16 v[160:163], a[144:147], v[236:239], v[160:163]\n"
    "v_mfma_f32_16x16x32_bf16 v[164:167], a[148:151], v[236:239], v[164:167]\n"
    "buffer_load_dwordx4 a[180:183], v107, s[56:59], s75 offen sc1\n"
    "v_mfma_f32_16x16x32_bf16 v[164:167], a[132:135], v[232:235], v[164:167]\n"
    "v_mfma_f32_16x16x32_bf16 v[164:167], a[116:119], v[228:231], v[164:167]\n"
    "v_mfma_f32_16x16x32_bf16 v[164:167], a[100:103], v[224:227], v[164:167]\n"
    "v_mfma_f32_16x16x32_bf16 v[168:171], a[104:107], v[224:227], v[168:171]\n"
    "v_mfma_f32_16x16x32_bf16 v[168:171], a[120:123], v[228:231], v[168:171]\n"
    "v_mfma_f32_16x16x32_bf16 v[168:171], a[136:139], v[232:235], v[168:171]\n"
    "v_mfma_f32_16x16x32_bf16 v[168:171], a[152:155], v[236:239], v[168:171]\n"
    "v_mfma_f32_16x16x32_bf16 v[172:175], a[156:159], v[236:239], v[172:175]\n"
    "v_mfma_f32_16x16x32_bf16 v[172:175], a[140:143], v[232:235], v[172:175]\n"
    "s_add_u32 s68, s64, 0x5000\n"
    "v_mfma_f32_16x16x32_bf16 v[172:175], a[124:127], v[228:231], v[172:175]\n"
    "v_mfma_f32_16x16x32_bf16 v[172:175], a[108:111], v[224:227], v[172:175]\n"
    "s_waitcnt vmcnt(22)\n"
    "v_mfma_f32_16x16x32_bf16 v[176:179], a[96:99], a[224:227], v[176:179]\n"
    "buffer_load_dwordx4 a[192:195], v116, s[56:59], s68 offen sc1\n"
    "v_mfma_f32_16x16x32_bf16 v[176:179], a[128:131], a[228:231], v[176:179]\n"
    "s_add_u32 s75, s65, 0x5000\n"
    "v_mfma_f32_16x16x32_bf16 v[8:11], a[144:147], a[228:231], v[8:11]\n"
    "buffer_load_dwordx4 a[196:199], v116, s[56:59], s75 offen sc1\n"
    "v_mfma_f32_16x16x32_bf16 v[8:11], a[112:115], a[224:227], v[8:11]\n"
    "s_add_u32 s68, s66, 0x5000\n"
    "v_mfma_f32_16x16x32_bf16 v[180:183], a[100:103], a[224:227], v[180:183]\n"
    "v_mfma_f32_16x16x32_bf16 v[180:183], a[132:135], a[228:231], v[180:183]\n"
    "buffer_load_dwordx4 a[200:203], v116, s[56:59], s68 offen sc1\n"
    "v_mfma_f32_16x16x32_bf16 v[12:15], a[148:151], a[228:231], v[12:15]\n"
    "s_add_u32 s75, s67, 0x5000\n"
    "v_mfma_f32_16x16x32_bf16 v[12:15], a[116:119], a[224:227], v[12:15]\n"
    "v_mfma_f32_16x16x32_bf16 v[184:187], a[104:107], a[224:227], v[184:187]\n"
    "buffer_load_dwordx4 a[204:207], v116, s[56:59], s75 offen sc1\n"
    "v_mfma_f32_16x16x32_bf16 v[184:187], a[136:139], a[228:231], v[184:187]\n"
    "s_add_u32 s42, s70, 0xe0000\n"
    "v_mfma_f32_16x16x32_bf16 v[16:19], a[152:155], a[228:231], v[16:19]\n"
    "s_add_u32 m0, s55, 0x18000\n"
    "v_mfma_f32_16x16x32_bf16 v[16:19], a[120:123], a[224:227], v[16:19]\n"
    "buffer_load_dwordx4 v122, s[32:35], s42 offen lds\n"
    "v_mfma_f32_16x16x32_bf16 v[188:191], a[108:111], a[224:227], v[188:191]\n"
    "v_mfma_f32_16x16x32_bf16 v[188:191], a[140:143], a[228:231], v[188:191]\n"
    "v_mfma_f32_16x16x32_bf16 v[20:23], a[156:159], a[228:231], v[20:23]\n"
    "buffer_load_dwordx4 v123, s[32:35], s42 offen offset:1024 lds\n"
    "v_mfma_f32_16x16x32_bf16 v[20:23], a[124:127], a[224:227], v[20:23]\n"
    "s_waitcnt vmcnt(20)\n"
    "v_mfma_f32_16x16x32_bf16 v[128:131], v[192:195], a[240:243], v[128:131]\n"
    "buffer_load_dwordx4 v124, s[32:35], s42 offen offset:2048 lds\n"
    "v_mfma_f32_16x16x32_bf16 v[128:131], v[208:211], a[244:247], v[128:131]\n"
    "s_waitcnt vmcnt(63)\n"
    "v_mfma_f32_16x16x32_bf16 v[128:131], v[24:27], a[248:251], v[128:131]\n"
    "ds_read_b64_tr_b16 a[32:33], v112 offset:16384\n"
    "v_mfma_f32_16x16x32_bf16 v[128:131], a[160:163], a[252:255], v[128:131]\n"
    "ds_read_b64_tr_b16 a[34:35], v112 offset:18432\n"
    "buffer_load_dwordx4 v125, s[32:35], s42 offen offset:3072 lds\n"
    "v_mfma_f32_16x16x32_bf16 v[132:135], a[164:167], a[252:255], v[132:135]\n"
    "s_add_u32 s68, s64, 0x5000\n"
    "ds_read_b64_tr_b16 a[36:37], v113 offset:16384\n"
    "v_mfma_f32_16x16x32_bf16 v[132:135], v[28:31], a[248:251], v[132:135]\n"
    "ds_read_b64_tr_b16 a[38:39], v113 offset:18432\n"
    "v_mfma_f32_16x16x32_bf16 v[132:135], v[212:215], a[244:247], v[132:135]\n"
    "ds_read_b64_tr_b16 a[40:41], v112 offset:16896\n"
    "buffer_load_dwordx4 a[48:51], v117, s[56:59], s68 offen sc1\n"
    "v_mfma_f32_16x16x32_bf16 v[132:135], v[196:199], a[240:243], v[132:135]\n"
    "s_add_u32 s75, s65, 0x5000\n"
    "ds_read_b64_tr_b16 a[42:43], v112 offset:18944\n"
    "v_mfma_f32_16x16x32_bf16 v[136:139], v[200:203], a[240:243], v[136:139]\n"
    "ds_read_b64_tr_b16 a[44:45], v113 offset:16896\n"
    "v_mfma_f32_16x16x32_bf16 v[136:139], v[216:219], a[244:247], v[136:139]\n"
    "ds_read_b64_tr_b16 a[46:47], v113 offset:18944\n"
    "buffer_load_dwordx4 a[52:55], v117, s[56:59], s75 offen sc1\n"
    "v_mfma_f32_16x16x32_bf16 v[136:139], v[32:35], a[248:251], v[136:139]\n"
    "s_add_u32 s68, s66, 0x5000\n"
    "v_mfma_f32_16x16x32_bf16 v[136:139], a[168:171], a[252:255], v[136:139]\n"
    "s_waitcnt vmcnt(52)\n"
    "v_mfma_f32_16x16x32_bf16 v[140:143], a[172:175], a[252:255], v[140:143]\n"
    "ds_read_b64_tr_b16 a[64:65], v108\n"
    "buffer_load_dwordx4 a[56:59], v117, s[56:59], s68 offen sc1\n"
    "v_mfma_f32_16x16x32_bf16 v[140:143], v[36:39], a[248:251], v[140:143]\n"
    "s_add_u32 s75, s67, 0x5000\n"
    "ds_read_b64_tr_b16 a[66:67], v108 offset:2048\n"
    "v_mfma_f32_16x16x32_bf16 v[140:143], v[220:223], a[244:247], v[140:143]\n"
    "ds_read_b64_tr_b16 a[68:69], v109\n"
    "buffer_load_dwordx4 a[60:63], v117, s[56:59], s75 offen sc1\n"
    "v_mfma_f32_16x16x32_bf16 v[140:143], v[204:207], a[240:243], v[140:143]\n"
    "s_waitcnt vmcnt(22)\n"
    "s_add_u32 s68, s64, 0x5000\n"
    "ds_read_b64_tr_b16 a[70:71], v109 offset:2048\n"
    "v_mfma_f32_16x16x32_bf16 v[144:147], v[192:195], a[0:3], v[144:147]\n"
    "ds_read_b64_tr_b16 a[72:73], v108 offset:512\n"
    "v_mfma_f32_16x16x32_bf16 v[144:147], v[208:211], a[4:7], v[144:147]\n"
    "ds_read_b64_tr_b16 a[74:75], v108 offset:2560\n"
    "buffer_load_dwordx4 a[80:83], v118, s[56:59], s68 offen sc1\n"
    "v_mfma_f32_16x16x32_bf16 v[144:147], v[24:27], a[8:11], v[144:147]\n"
    "s_add_u32 s75, s65, 0x5000\n"
    "ds_read_b64_tr_b16 a[76:77], v109 offset:512\n"
    "v_mfma_f32_16x16x32_bf16 v[144:147], a[160:163], a[12:15], v[144:147]\n"
    "ds_read_b64_tr_b16 a[78:79], v109 offset:2560\n"
    "v_mfma_f32_16x16x32_bf16 v[148:151], a[164:167], a[12:15], v[148:151]\n"
    "buffer_load_dwordx4 a[84:87], v118, s[56:59], s75 offen sc1\n"
    "v_mfma_f32_16x16x32_bf16 v[148:151], v[28:31], a[8:11], v[148:151]\n"
    "s_add_u32 s68, s66, 0x5000\n"
    "v_mfma_f32_16x16x32_bf16 v[148:151], v[212:215], a[4:7], v[148:151]\n"
    "s_waitcnt lgkmcnt(8)\n"
    "v_mfma_f32_16x16x32_bf16 v[148:151], v[196:199], a[0:3], v[148:151]\n"
    "buffer_load_dwordx4 a[88:91], v118, s[56:59], s68 offen sc1\n"
    "v_mfma_f32_16x16x32_bf16 v[152:155], v[200:203], a[0:3], v[152:155]\n"
    "s_add_u32 s75, s67, 0x5000\n"
    "v_mfma_f32_16x16x32_bf16 v[152:155], v[216:219], a[4:7], v[152:155]\n"
    "v_mfma_f32_16x16x32_bf16 v[152:155], v[32:35], a[8:11], v[152:155]\n"
    "buffer_load_dwordx4 a[92:95], v118, s[56:59], s75 offen sc1\n"
    "v_mfma_f32_16x16x32_bf16 v[152:155], a[168:171], a[12:15], v[152:155]\n"
    "s_add_u32 s42, s71, 0xe0000\n"
    "v_mfma_f32_16x16x32_bf16 v[156:159], a[172:175], a[12:15], v[156:159]\n"
    "s_add_u32 m0, s55, 0x14000\n"
    "v_mfma_f32_16x16x32_bf16 v[156:159], v[36:39], a[8:11], v[156:159]\n"
    "buffer_load_dwordx4 v122, s[32:35], s42 offen lds\n"
    "v_mfma_f32_16x16x32_bf16 v[156:159], v[220:223], a[4:7], v[156:159]\n"
    "v_mfma_f32_16x16x32_bf16 v[156:159], v[204:207], a[0:3], v[156:159]\n"
    "s_waitcnt vmcnt(19)\n"
    "buffer_load_dwordx4 v123, s[32:35], s42 offen offset:1024 lds\n"
    "v_mfma_f32_16x16x32_bf16 v[160:163], v[192:195], v[40:43], v[160:163]\n"
    "s_waitcnt lgkmcnt(0)\n"
    "v_mfma_f32_16x16x32_bf16 v[160:163], v[208:211], v[44:47], v[160:163]\n"
    "v_mfma_f32_16x16x32_bf16 v[160:163], v[24:27], v[48:51], v[160:163]\n"
    "buffer_load_dwordx4 v124, s[32:35], s42 offen offset:2048 lds\n"
    "v_mfma_f32_16x16x32_bf16 v[160:163], a[160:163], v[52:55], v[160:163]\n"
    "v_mfma_f32_16x16x32_bf16 v[164:167], a[164:167], v[52:55], v[164:167]\n"
    "v_mfma_f32_16x16x32_bf16 v[164:167], v[28:31], v[48:51], v[164:167]\n"
    "buffer_load_dwordx4 v125, s[32:35], s42 offen offset:3072 lds\n"
    "v_mfma_f32_16x16x32_bf16 v[164:167], v[212:215], v[44:47], v[164:167]\n"
    "s_mov_b32 s75, 0x5000\n"
    "v_mfma_f32_16x16x32_bf16 v[164:167], v[196:199], v[40:43], v[164:167]\n"
    "v_mfma_f32_16x16x32_bf16 v[168:171], v[200:203], v[40:43], v[168:171]\n"
    "buffer_load_dwordx4 a[208:211], v119, s[56:59], s75 offen sc1\n"
    "v_mfma_f32_16x16x32_bf16 v[168:171], v[216:219], v[44:47], v[168:171]\n"
    "v_mfma_f32_16x16x32_bf16 v[168:171], v[32:35], v[48:51], v[168:171]\n"
    "buffer_load_dwordx4 a[212:215], v107, s[56:59], s75 offen sc1\n"
    "v_mfma_f32_16x16x32_bf16 v[168:171], a[168:171], v[52:55], v[168:171]\n"
    "v_mfma_f32_16x16x32_bf16 v[172:175], a[172:175], v[52:55], v[172:175]\n"
    "v_lshl_add_u32 v1, v0, 4, s55\n"
    "v_mfma_f32_16x16x32_bf16 v[172:175], v[36:39], v[48:51], v[172:175]\n"
    "s_add_u32 s68, s64, 0x6000\n"
    "v_mfma_f32_16x16x32_bf16 v[172:175], v[220:223], v[44:47], v[172:175]\n"
    "s_lshl_b32 s45, s44, 14\n"
    "v_mfma_f32_16x16x32_bf16 v[172:175], v[204:207], v[40:43], v[172:175]\n"
    "s_waitcnt vmcnt(22)\n"
    "v_mfma_f32_16x16x32_bf16 v[176:179], v[192:195], a[176:179], v[176:179]\n"
    "buffer_load_dwordx4 v[72:75], v116, s[56:59], s68 offen sc1\n"
    "v_lshl_add_u32 v2, v0, 4, s45\n"
    "v_mfma_f32_16x16x32_bf16 v[176:179], v[24:27], a[180:183], v[176:179]\n"
    "s_add_u32 s75, s65, 0x6000\n"
    "v_mfma_f32_16x16x32_bf16 v[8:11], a[160:163], a[180:183], v[8:11]\n"
    "s_mov_b32 s36, s18\n"
    "buffer_load_dwordx4 v[76:79], v116, s[56:59], s75 offen sc1\n"
    "v_mfma_f32_16x16x32_bf16 v[8:11], v[208:211], a[176:179], v[8:11]\n"
    "s_mov_b32 s37, s19\n"
    "s_add_u32 s68, s66, 0x6000\n"
    "v_mfma_f32_16x16x32_bf16 v[180:183], v[196:199], a[176:179], v[180:183]\n"
    "s_and_b32 s37, s37, 0xffff\n"
    "v_mfma_f32_16x16x32_bf16 v[180:183], v[28:31], a[180:183], v[180:183]\n"
    "s_mov_b32 s38, 0x1c0000\n"
    "buffer_load_dwordx4 v[80:83], v116, s[56:59], s68 offen sc1\n"
    "v_mfma_f32_16x16x32_bf16 v[12:15], a[164:167], a[180:183], v[12:15]\n"
    "s_mov_b32 s39, 0x20000\n"
    "s_add_u32 s75, s67, 0x6000\n"
    "v_mfma_f32_16x16x32_bf16 v[12:15], v[212:215], a[176:179], v[12:15]\n"
    "s_add_u32 s45, s96, s44\n"
    "v_mfma_f32_16x16x32_bf16 v[184:187], v[200:203], a[176:179], v[184:187]\n"
    "s_add_u32 s52, s45, -3\n"
    "buffer_load_dwordx4 v[84:87], v116, s[56:59], s75 offen sc1\n"
    "v_mfma_f32_16x16x32_bf16 v[184:187], v[32:35], a[180:183], v[184:187]\n"
    "s_min_u32 s51, s45, s52\n"
    "s_add_u32 s42, s72, 0xe0000\n"
    "v_mfma_f32_16x16x32_bf16 v[16:19], a[168:171], a[180:183], v[16:19]\n"
    "s_mul_i32 s45, s73, 7\n"
    "s_add_u32 m0, s97, 0x10000\n"
    "v_mfma_f32_16x16x32_bf16 v[16:19], v[216:219], a[176:179], v[16:19]\n"
    "s_lshl_b32 s52, s74, 2\n"
    "buffer_load_dwordx4 v122, s[32:35], s42 offen lds\n"
    "v_mfma_f32_16x16x32_bf16 v[188:191], v[204:207], a[176:179], v[188:191]\n"
    "s_add_u32 s52, s52, s51\n"
    "v_mfma_f32_16x16x32_bf16 v[188:191], v[36:39], a[180:183], v[188:191]\n"
    "s_cmp_eq_u32 s44, 3\n"
    "s_cselect_b32 s52, 3, s52\n"
    "v_mfma_f32_16x16x32_bf16 v[20:23], a[172:175], a[180:183], v[20:23]\n"
    "s_add_u32 s45, s45, s52\n"
    "buffer_load_dwordx4 v123, s[32:35], s42 offen offset:1024 lds\n"
    "v_mfma_f32_16x16x32_bf16 v[20:23], v[220:223], a[176:179], v[20:23]\n"
    "s_waitcnt vmcnt(24)\n"
    "s_lshl_b32 s45, s45, 4\n"
    "v_mfma_f32_16x16x32_bf16 v[128:131], v[56:59], a[192:195], v[128:131]\n"
    "s_lshl_b32 s52, s74, 3\n"
    "buffer_load_dwordx4 v124, s[32:35], s42 offen offset:2048 lds\n"
    "v_mfma_f32_16x16x32_bf16 v[128:131], v[88:91], a[196:199], v[128:131]\n"
    "s_waitcnt vmcnt(61)\n"
    "s_cmp_eq_u32 s44, 3\n"
    "s_cselect_b32 s52, s52, 0\n"
    "v_mfma_f32_16x16x32_bf16 v[128:131], a[16:19], a[200:203], v[128:131]\n"
    "s_add_u32 s45, s45, s52\n"
    "ds_read_b64_tr_b16 v[224:225], v108 offset:32768\n"
    "v_mfma_f32_16x16x32_bf16 v[128:131], v[240:243], a[204:207], v[128:131]\n"
    "s_mul_i32 s51, s47, 0xe0\n"
    "ds_read_b64_tr_b16 v[226:227], v108 offset:34816\n"
    "buffer_load_dwordx4 v125, s[32:35], s42 offen offset:3072 lds\n"
    "v_mfma_f32_16x16x32_bf16 v[132:135], v[244:247], a[204:207], v[132:135]\n"
    "s_add_u32 s45, s45, s51\n"
    "s_add_u32 s68, s64, 0x6000\n"
    "ds_read_b64_tr_b16 v[228:229], v109 offset:32768\n"
    "v_mfma_f32_16x16x32_bf16 v[132:135], a[20:23], a[200:203], v[132:135]\n"
    "s_lshl_b32 s45, s45, 10\n"
    "ds_read_b64_tr_b16 v[230:231], v109 offset:34816\n"
    "v_mfma_f32_16x16x32_bf16 v[132:135], v[92:95], a[196:199], v[132:135]\n"
    "s_and_b32 s51, s48, 7\n"
    "ds_read_b64_tr_b16 v[232:233], v108 offset:33280\n"
    "buffer_load_dwordx4 a[224:227], v117, s[56:59], s68 offen sc1\n"
    "v_mfma_f32_16x16x32_bf16 v[132:135], v[60:63], a[192:195], v[132:135]\n"
    "s_lshl_b32 s51, s51, 7\n"
    "s_add_u32 s75, s65, 0x6000\n"
    "ds_read_b64_tr_b16 v[234:235], v108 offset:35328\n"
    "v_mfma_f32_16x16x32_bf16 v[136:139], v[64:67], a[192:195], v[136:139]\n"
    "s_add_u32 s45, s45, s51\n"
    "ds_read_b64_tr_b16 v[236:237], v109 offset:33280\n"
    "v_mfma_f32_16x16x32_bf16 v[136:139], v[96:99], a[196:199], v[136:139]\n"
    "ds_read_b64_tr_b16 v[238:239], v109 offset:35328\n"
    "v_and_b32_e32 v4, 15, v0\n"
    "v_mfma_f32_16x16x32_bf16 v[136:139], a[24:27], a[200:203], v[136:139]\n"
    "buffer_load_dwordx4 a[228:231], v117, s[56:59], s75 offen sc1\n"
    "v_lshlrev_b32_e32 v4, 10, v4\n"
    "v_mfma_f32_16x16x32_bf16 v[136:139], v[248:251], a[204:207], v[136:139]\n"
    "s_waitcnt vmcnt(50)\n"
    "s_add_u32 s68, s66, 0x6000\n"
    "v_lshrrev_b32_e32 v5, 4, v0\n"
    "v_mfma_f32_16x16x32_bf16 v[140:143], v[252:255], a[204:207], v[140:143]\n"
    "ds_read_b64_tr_b16 a[96:97], v108 offset:49152\n"
    "v_lshl_add_u32 v4, v5, 3, v4\n"
    "v_mfma_f32_16x16x32_bf16 v[140:143], a[28:31], a[200:203], v[140:143]\n"
    "ds_read_b64_tr_b16 a[98:99], v108 offset:51200\n"
    "v_add_u32_e32 v3, s45, v4\n"
    "v_mfma_f32_16x16x32_bf16 v[140:143], v[100:103], a[196:199], v[140:143]\n"
    "ds_read_b64_tr_b16 a[100:101], v109 offset:49152\n"
    "v_bfe_u32 v4, v0, 3, 1\n"
    "v_mfma_f32_16x16x32_bf16 v[140:143], v[68:71], a[192:195], v[140:143]\n"
    "s_waitcnt vmcnt(20)\n"
    "ds_read_b64_tr_b16 a[102:103], v109 offset:51200\n"
    "buffer_load_dwordx4 a[232:235], v117, s[56:59], s68 offen sc1\n"
    "v_mfma_f32_16x16x32_bf16 v[144:147], v[56:59], a[48:51], v[144:147]\n"
    "s_add_u32 s75, s67, 0x6000\n"
    "ds_read_b64_tr_b16 a[104:105], v108 offset:49664\n"
    "v_mfma_f32_16x16x32_bf16 v[144:147], v[88:91], a[52:55], v[144:147]\n"
    "ds_read_b64_tr_b16 a[106:107], v108 offset:51712\n"
    "buffer_load_dwordx4 a[236:239], v117, s[56:59], s75 offen sc1\n"
    "v_mfma_f32_16x16x32_bf16 v[144:147], a[16:19], a[56:59], v[144:147]\n"
    "s_add_u32 s68, s64, 0x6000\n"
    "ds_read_b64_tr_b16 a[108:109], v109 offset:49664\n"
    "v_mfma_f32_16x16x32_bf16 v[144:147], v[240:243], a[60:63], v[144:147]\n"
    "ds_read_b64_tr_b16 a[110:111], v109 offset:51712\n"
    "buffer_load_dwordx4 a[112:115], v118, s[56:59], s68 offen sc1\n"
    "v_mfma_f32_16x16x32_bf16 v[148:151], v[244:247], a[60:63], v[148:151]\n"
    "s_add_u32 s75, s65, 0x6000\n"
    "v_mfma_f32_16x16x32_bf16 v[148:151], a[20:23], a[56:59], v[148:151]\n"
    "buffer_load_dwordx4 a[116:119], v118, s[56:59], s75 offen sc1\n"
    "v_mfma_f32_16x16x32_bf16 v[148:151], v[92:95], a[52:55], v[148:151]\n"
    "s_waitcnt lgkmcnt(8)\n"
    "s_add_u32 s68, s66, 0x6000\n"
    "v_mfma_f32_16x16x32_bf16 v[148:151], v[60:63], a[48:51], v[148:151]\n"
    "buffer_load_dwordx4 a[120:123], v118, s[56:59], s68 offen sc1\n"
    "v_mfma_f32_16x16x32_bf16 v[152:155], v[64:67], a[48:51], v[152:155]\n"
    "s_add_u32 s75, s67, 0x6000\n"
    "v_mfma_f32_16x16x32_bf16 v[152:155], v[96:99], a[52:55], v[152:155]\n"
    "v_mfma_f32_16x16x32_bf16 v[152:155], a[24:27], a[56:59], v[152:155]\n"
    "buffer_load_dwordx4 a[124:127], v118, s[56:59], s75 offen sc1\n"
    "v_mfma_f32_16x16x32_bf16 v[152:155], v[248:251], a[60:63], v[152:155]\n"
    "v_mfma_f32_16x16x32_bf16 v[156:159], v[252:255], a[60:63], v[156:159]\n"
    "v_mfma_f32_16x16x32_bf16 v[156:159], a[28:31], a[56:59], v[156:159]\n"
    "s_mov_b32 s75, 0x6000\n"
    "v_mfma_f32_16x16x32_bf16 v[156:159], v[100:103], a[52:55], v[156:159]\n"
    "v_mfma_f32_16x16x32_bf16 v[156:159], v[68:71], a[48:51], v[156:159]\n"
    "s_waitcnt vmcnt(22)\n"
    "v_mfma_f32_16x16x32_bf16 v[160:163], v[56:59], a[80:83], v[160:163]\n"
    "s_waitcnt lgkmcnt(0)\n"
    "buffer_load_dwordx4 a[128:131], v119, s[56:59], s75 offen sc1\n"
    "v_mfma_f32_16x16x32_bf16 v[160:163], v[88:91], a[84:87], v[160:163]\n"
    "v_mfma_f32_16x16x32_bf16 v[160:163], a[16:19], a[88:91], v[160:163]\n"
    "buffer_load_dwordx4 a[132:135], v107, s[56:59], s75 offen sc1\n"
    "v_mfma_f32_16x16x32_bf16 v[160:163], v[240:243], a[92:95], v[160:163]\n"
    "v_mfma_f32_16x16x32_bf16 v[164:167], v[244:247], a[92:95], v[164:167]\n"
    "v_mfma_f32_16x16x32_bf16 v[164:167], a[20:23], a[88:91], v[164:167]\n"
    "v_mfma_f32_16x16x32_bf16 v[164:167], v[92:95], a[84:87], v[164:167]\n"
    "v_mfma_f32_16x16x32_bf16 v[164:167], v[60:63], a[80:83], v[164:167]\n"
    "v_mfma_f32_16x16x32_bf16 v[168:171], v[64:67], a[80:83], v[168:171]\n"
    "v_mfma_f32_16x16x32_bf16 v[168:171], v[96:99], a[84:87], v[168:171]\n"
    "v_mfma_f32_16x16x32_bf16 v[168:171], a[24:27], a[88:91], v[168:171]\n"
    "v_mfma_f32_16x16x32_bf16 v[168:171], v[248:251], a[92:95], v[168:171]\n"
    "v_mfma_f32_16x16x32_bf16 v[172:175], v[252:255], a[92:95], v[172:175]\n"
    "v_mfma_f32_16x16x32_bf16 v[172:175], a[28:31], a[88:91], v[172:175]\n"
    "s_add_u32 s68, s64, 0x7000\n"
    "v_mfma_f32_16x16x32_bf16 v[172:175], v[100:103], a[84:87], v[172:175]\n"
    "v_mfma_f32_16x16x32_bf16 v[172:175], v[68:71], a[80:83], v[172:175]\n"
    "s_waitcnt vmcnt(18)\n"
    "v_mfma_f32_16x16x32_bf16 v[176:179], v[56:59], a[208:211], v[176:179]\n"
    "buffer_load_dwordx4 a[144:147], v116, s[56:59], s68 offen sc1\n"
    "v_mfma_f32_16x16x32_bf16 v[176:179], a[16:19], a[212:215], v[176:179]\n"
    "s_add_u32 s75, s65, 0x7000\n"
    "v_mfma_f32_16x16x32_bf16 v[8:11], v[240:243], a[212:215], v[8:11]\n"
    "buffer_load_dwordx4 a[148:151], v116, s[56:59], s75 offen sc1\n"
    "v_mfma_f32_16x16x32_bf16 v[8:11], v[88:91], a[208:211], v[8:11]\n"
    "s_add_u32 s68, s66, 0x7000\n"
    "v_mfma_f32_16x16x32_bf16 v[180:183], v[60:63], a[208:211], v[180:183]\n"
    "v_mfma_f32_16x16x32_bf16 v[180:183], a[20:23], a[212:215], v[180:183]\n"
    "buffer_load_dwordx4 a[152:155], v116, s[56:59], s68 offen sc1\n"
    "v_mfma_f32_16x16x32_bf16 v[12:15], v[244:247], a[212:215], v[12:15]\n"
    "s_add_u32 s75, s67, 0x7000\n"
    "v_mfma_f32_16x16x32_bf16 v[12:15], v[92:95], a[208:211], v[12:15]\n"
    "v_mfma_f32_16x16x32_bf16 v[184:187], v[64:67], a[208:211], v[184:187]\n"
    "buffer_load_dwordx4 a[156:159], v116, s[56:59], s75 offen sc1\n"
    "v_mfma_f32_16x16x32_bf16 v[184:187], a[24:27], a[212:215], v[184:187]\n"
    "v_mfma_f32_16x16x32_bf16 v[16:19], v[248:251], a[212:215], v[16:19]\n"
    "v_mfma_f32_16x16x32_bf16 v[16:19], v[96:99], a[208:211], v[16:19]\n"
    "v_mfma_f32_16x16x32_bf16 v[188:191], v[68:71], a[208:211], v[188:191]\n"
    "v_mfma_f32_16x16x32_bf16 v[188:191], a[28:31], a[212:215], v[188:191]\n"
    "s_add_u32 s68, s64, 0x7000\n"
    "v_mfma_f32_16x16x32_bf16 v[20:23], v[252:255], a[212:215], v[20:23]\n"
    "v_mfma_f32_16x16x32_bf16 v[20:23], v[100:103], a[208:211], v[20:23]\n"
    "s_waitcnt vmcnt(18)\n"
    "v_mfma_f32_16x16x32_bf16 v[128:131], a[32:35], v[72:75], v[128:131]\n"
    "buffer_load_dwordx4 a[240:243], v117, s[56:59], s68 offen sc1\n"
    "v_mfma_f32_16x16x32_bf16 v[128:131], a[64:67], v[76:79], v[128:131]\n"
    "s_waitcnt vmcnt(51)\n"
    "s_add_u32 s75, s65, 0x7000\n"
    "v_mfma_f32_16x16x32_bf16 v[128:131], v[224:227], v[80:83], v[128:131]\n"
    "ds_read_b64_tr_b16 a[0:1], v108 offset:16384\n"
    "buffer_load_dwordx4 a[244:247], v117, s[56:59], s75 offen sc1\n"
    "v_mfma_f32_16x16x32_bf16 v[128:131], a[96:99], v[84:87], v[128:131]\n"
    "s_add_u32 s68, s66, 0x7000\n"
    "ds_read_b64_tr_b16 a[2:3], v108 offset:18432\n"
    "v_mfma_f32_16x16x32_bf16 v[132:135], a[100:103], v[84:87], v[132:135]\n"
    "ds_read_b64_tr_b16 a[4:5], v109 offset:16384\n"
    "v_mfma_f32_16x16x32_bf16 v[132:135], v[228:231], v[80:83], v[132:135]\n"
    "ds_read_b64_tr_b16 a[6:7], v109 offset:18432\n"
    "buffer_load_dwordx4 a[248:251], v117, s[56:59], s68 offen sc1\n"
    "v_mfma_f32_16x16x32_bf16 v[132:135], a[68:71], v[76:79], v[132:135]\n"
    "s_add_u32 s75, s67, 0x7000\n"
    "ds_read_b64_tr_b16 a[8:9], v108 offset:16896\n"
    "v_mfma_f32_16x16x32_bf16 v[132:135], a[36:39], v[72:75], v[132:135]\n"
    "ds_read_b64_tr_b16 a[10:11], v108 offset:18944\n"
    "v_mfma_f32_16x16x32_bf16 v[136:139], a[40:43], v[72:75], v[136:139]\n"
    "ds_read_b64_tr_b16 a[12:13], v109 offset:16896\n"
    "buffer_load_dwordx4 a[252:255], v117, s[56:59], s75 offen sc1\n"
    "v_mfma_f32_16x16x32_bf16 v[136:139], a[72:75], v[76:79], v[136:139]\n"
    "ds_read_b64_tr_b16 a[14:15], v109 offset:18944\n"
    "v_mfma_f32_16x16x32_bf16 v[136:139], v[232:235], v[80:83], v[136:139]\n"
    "v_mfma_f32_16x16x32_bf16 v[136:139], a[104:107], v[84:87], v[136:139]\n"
    "s_waitcnt vmcnt(40)\n"
    "v_mfma_f32_16x16x32_bf16 v[140:143], a[108:111], v[84:87], v[140:143]\n"
    "ds_read_b64_tr_b16 a[176:177], v110 offset:32768\n"
    "v_mfma_f32_16x16x32_bf16 v[140:143], v[236:239], v[80:83], v[140:143]\n"
    "s_add_u32 s68, s64, 0x7000\n"
    "ds_read_b64_tr_b16 a[178:179], v110 offset:34816\n"
    "v_mfma_f32_16x16x32_bf16 v[140:143], a[76:79], v[76:79], v[140:143]\n"
    "ds_read_b64_tr_b16 a[180:181], v111 offset:32768\n"
    "v_mfma_f32_16x16x32_bf16 v[140:143], a[44:47], v[72:75], v[140:143]\n"
    "s_waitcnt vmcnt(14)\n"
    "ds_read_b64_tr_b16 a[182:183], v111 offset:34816\n"
    "v_mfma_f32_16x16x32_bf16 v[144:147], a[32:35], a[224:227], v[144:147]\n"
    "ds_read_b64_tr_b16 a[184:185], v110 offset:33280\n"
    "buffer_load_dwordx4 v[40:43], v118, s[56:59], s68 offen sc1\n"
    "v_mfma_f32_16x16x32_bf16 v[144:147], a[64:67], a[228:231], v[144:147]\n"
    "s_add_u32 s75, s65, 0x7000\n"
    "ds_read_b64_tr_b16 a[186:187], v110 offset:35328\n"
    "v_mfma_f32_16x16x32_bf16 v[144:147], v[224:227], a[232:235], v[144:147]\n"
    "ds_read_b64_tr_b16 a[188:189], v111 offset:33280\n"
    "buffer_load_dwordx4 v[44:47], v118, s[56:59], s75 offen sc1\n"
    "v_mfma_f32_16x16x32_bf16 v[144:147], a[96:99], a[236:239], v[144:147]\n"
    "s_add_u32 s68, s66, 0x7000\n"
    "ds_read_b64_tr_b16 a[190:191], v111 offset:35328\n"
    "v_mfma_f32_16x16x32_bf16 v[148:151], a[100:103], a[236:239], v[148:151]\n"
    "v_mfma_f32_16x16x32_bf16 v[148:151], v[228:231], a[232:235], v[148:151]\n"
    "s_waitcnt vmcnt(30)\n"
    "buffer_load_dwordx4 v[48:51], v118, s[56:59], s68 offen sc1\n"
    "v_mfma_f32_16x16x32_bf16 v[148:151], a[68:71], a[228:231], v[148:151]\n"
    "s_waitcnt lgkmcnt(8)\n"
    "s_add_u32 s75, s67, 0x7000\n"
    "ds_read_b64_tr_b16 v[192:193], v110 offset:16384\n"
    "v_mfma_f32_16x16x32_bf16 v[148:151], a[36:39], a[224:227], v[148:151]\n"
    "ds_read_b64_tr_b16 v[194:195], v110 offset:18432\n"
    "v_mfma_f32_16x16x32_bf16 v[152:155], a[40:43], a[224:227], v[152:155]\n"
    "ds_read_b64_tr_b16 v[196:197], v111 offset:16384\n"
    "buffer_load_dwordx4 v[52:55], v118, s[56:59], s75 offen sc1\n"
    "v_mfma_f32_16x16x32_bf16 v[152:155], a[72:75], a[228:231], v[152:155]\n"
    "ds_read_b64_tr_b16 v[198:199], v111 offset:18432\n"
    "v_mfma_f32_16x16x32_bf16 v[152:155], v[232:235], a[232:235], v[152:155]\n"
    "ds_read_b64_tr_b16 v[200:201], v110 offset:16896\n"
    "v_mfma_f32_16x16x32_bf16 v[152:155], a[104:107], a[236:239], v[152:155]\n"
    "ds_read_b64_tr_b16 v[202:203], v110 offset:18944\n"
    "v_mfma_f32_16x16x32_bf16 v[156:159], a[108:111], a[236:239], v[156:159]\n"
    "ds_read_b64_tr_b16 v[204:205], v111 offset:16896\n"
    "v_mfma_f32_16x16x32_bf16 v[156:159], v[236:239], a[232:235], v[156:159]\n"
    "s_mov_b32 s75, 0x7000\n"
    "ds_read_b64_tr_b16 v[206:207], v111 offset:18944\n"
    "v_mfma_f32_16x16x32_bf16 v[156:159], a[76:79], a[228:231], v[156:159]\n"
    "v_mfma_f32_16x16x32_bf16 v[156:159], a[44:47], a[224:227], v[156:159]\n"
    "s_waitcnt vmcnt(22)\n"
    "s_waitcnt vmcnt(14)\n"
    "v_mfma_f32_16x16x32_bf16 v[160:163], a[32:35], a[112:115], v[160:163]\n"
    "s_waitcnt lgkmcnt(8)\n"
    "ds_read_b64_tr_b16 v[24:25], v114\n"
    "buffer_load_dwordx4 v[208:211], v119, s[56:59], s75 offen sc1\n"
    "v_mfma_f32_16x16x32_bf16 v[160:163], a[64:67], a[116:119], v[160:163]\n"
    "ds_read_b64_tr_b16 v[26:27], v114 offset:2048\n"
    "v_mfma_f32_16x16x32_bf16 v[160:163], v[224:227], a[120:123], v[160:163]\n"
    "ds_read_b64_tr_b16 v[28:29], v115\n"
    "buffer_load_dwordx4 v[212:215], v107, s[56:59], s75 offen sc1\n"
    "v_mfma_f32_16x16x32_bf16 v[160:163], a[96:99], a[124:127], v[160:163]\n"
    "ds_read_b64_tr_b16 v[30:31], v115 offset:2048\n"
    "v_mfma_f32_16x16x32_bf16 v[164:167], a[100:103], a[124:127], v[164:167]\n"
    "ds_read_b64_tr_b16 v[32:33], v114 offset:512\n"
    "v_mfma_f32_16x16x32_bf16 v[164:167], v[228:231], a[120:123], v[164:167]\n"
    "ds_read_b64_tr_b16 v[34:35], v114 offset:2560\n"
    "v_mfma_f32_16x16x32_bf16 v[164:167], a[68:71], a[116:119], v[164:167]\n"
    "ds_read_b64_tr_b16 v[36:37], v115 offset:512\n"
    "v_mfma_f32_16x16x32_bf16 v[164:167], a[36:39], a[112:115], v[164:167]\n"
    "ds_read_b64_tr_b16 v[38:39], v115 offset:2560\n"
    "v_mfma_f32_16x16x32_bf16 v[168:171], a[40:43], a[112:115], v[168:171]\n"
    "v_mfma_f32_16x16x32_bf16 v[168:171], a[72:75], a[116:119], v[168:171]\n"
    "v_mfma_f32_16x16x32_bf16 v[168:171], v[232:235], a[120:123], v[168:171]\n"
    "s_waitcnt lgkmcnt(8)\n"
    "v_mfma_f32_16x16x32_bf16 v[168:171], a[104:107], a[124:127], v[168:171]\n"
    "v_mfma_f32_16x16x32_bf16 v[172:175], a[108:111], a[124:127], v[172:175]\n"
    "v_mfma_f32_16x16x32_bf16 v[172:175], v[236:239], a[120:123], v[172:175]\n"
    "v_mfma_f32_16x16x32_bf16 v[172:175], a[76:79], a[116:119], v[172:175]\n"
    "v_mfma_f32_16x16x32_bf16 v[172:175], a[44:47], a[112:115], v[172:175]\n"
    "s_waitcnt vmcnt(14)\n"
    "v_mfma_f32_16x16x32_bf16 v[176:179], a[32:35], a[128:131], v[176:179]\n"
    "v_mfma_f32_16x16x32_bf16 v[176:179], v[224:227], a[132:135], v[176:179]\n"
    "v_mfma_f32_16x16x32_bf16 v[8:11], a[96:99], a[132:135], v[8:11]\n"
    "v_mfma_f32_16x16x32_bf16 v[8:11], a[64:67], a[128:131], v[8:11]\n"
    "v_mfma_f32_16x16x32_bf16 v[180:183], a[36:39], a[128:131], v[180:183]\n"
    "s_waitcnt lgkmcnt(0)\n"
    "v_mfma_f32_16x16x32_bf16 v[180:183], v[228:231], a[132:135], v[180:183]\n"
    "v_mfma_f32_16x16x32_bf16 v[12:15], a[100:103], a[132:135], v[12:15]\n"
    "v_mfma_f32_16x16x32_bf16 v[12:15], a[68:71], a[128:131], v[12:15]\n"
    "v_mfma_f32_16x16x32_bf16 v[184:187], a[40:43], a[128:131], v[184:187]\n"
    "v_mfma_f32_16x16x32_bf16 v[184:187], v[232:235], a[132:135], v[184:187]\n"
    "v_mfma_f32_16x16x32_bf16 v[16:19], a[104:107], a[132:135], v[16:19]\n"
    "v_mfma_f32_16x16x32_bf16 v[16:19], a[72:75], a[128:131], v[16:19]\n"
    "v_mfma_f32_16x16x32_bf16 v[188:191], a[44:47], a[128:131], v[188:191]\n"
    "v_mfma_f32_16x16x32_bf16 v[188:191], v[236:239], a[132:135], v[188:191]\n"
    "v_mfma_f32_16x16x32_bf16 v[20:23], a[108:111], a[132:135], v[20:23]\n"
    "v_mfma_f32_16x16x32_bf16 v[20:23], a[76:79], a[128:131], v[20:23]\n"
    "s_waitcnt vmcnt(10)\n"
    "v_mfma_f32_16x16x32_bf16 v[128:131], a[0:3], a[144:147], v[128:131]\n"
    "v_mfma_f32_16x16x32_bf16 v[128:131], a[176:179], a[148:151], v[128:131]\n"
    "v_mfma_f32_16x16x32_bf16 v[128:131], v[192:195], a[152:155], v[128:131]\n"
    "v_mfma_f32_16x16x32_bf16 v[128:131], v[24:27], a[156:159], v[128:131]\n"
    "v_mfma_f32_16x16x32_bf16 v[132:135], v[28:31], a[156:159], v[132:135]\n"
    "v_mfma_f32_16x16x32_bf16 v[132:135], v[196:199], a[152:155], v[132:135]\n"
    "v_mfma_f32_16x16x32_bf16 v[132:135], a[180:183], a[148:151], v[132:135]\n"
    "v_mfma_f32_16x16x32_bf16 v[132:135], a[4:7], a[144:147], v[132:135]\n"
    "v_mfma_f32_16x16x32_bf16 v[136:139], a[8:11], a[144:147], v[136:139]\n"
    "v_mfma_f32_16x16x32_bf16 v[136:139], a[184:187], a[148:151], v[136:139]\n"
    "v_mfma_f32_16x16x32_bf16 v[136:139], v[200:203], a[152:155], v[136:139]\n"
    "v_mfma_f32_16x16x32_bf16 v[136:139], v[32:35], a[156:159], v[136:139]\n"
    "v_mfma_f32_16x16x32_bf16 v[140:143], v[36:39], a[156:159], v[140:143]\n"
    "v_mfma_f32_16x16x32_bf16 v[140:143], v[204:207], a[152:155], v[140:143]\n"
    "v_mfma_f32_16x16x32_bf16 v[140:143], a[188:191], a[148:151], v[140:143]\n"
    "v_mfma_f32_16x16x32_bf16 v[140:143], a[12:15], a[144:147], v[140:143]\n"
    "s_waitcnt vmcnt(6)\n"
    "v_mfma_f32_16x16x32_bf16 v[144:147], a[0:3], a[240:243], v[144:147]\n"
    "v_mfma_f32_16x16x32_bf16 v[144:147], a[176:179], a[244:247], v[144:147]\n"
    "v_mfma_f32_16x16x32_bf16 v[144:147], v[192:195], a[248:251], v[144:147]\n"
    "v_mfma_f32_16x16x32_bf16 v[144:147], v[24:27], a[252:255], v[144:147]\n"
    "v_mfma_f32_16x16x32_bf16 v[148:151], v[28:31], a[252:255], v[148:151]\n"
    "v_mfma_f32_16x16x32_bf16 v[148:151], v[196:199], a[248:251], v[148:151]\n"
    "v_mfma_f32_16x16x32_bf16 v[148:151], a[180:183], a[244:247], v[148:151]\n"
    "v_mfma_f32_16x16x32_bf16 v[148:151], a[4:7], a[240:243], v[148:151]\n"
    "v_mfma_f32_16x16x32_bf16 v[152:155], a[8:11], a[240:243], v[152:155]\n"
    "v_mfma_f32_16x16x32_bf16 v[152:155], a[184:187], a[244:247], v[152:155]\n"
    "v_mfma_f32_16x16x32_bf16 v[152:155], v[200:203], a[248:251], v[152:155]\n"
    "v_mfma_f32_16x16x32_bf16 v[152:155], v[32:35], a[252:255], v[152:155]\n"
    "v_mfma_f32_16x16x32_bf16 v[156:159], v[36:39], a[252:255], v[156:159]\n"
    "v_mfma_f32_16x16x32_bf16 v[156:159], v[204:207], a[248:251], v[156:159]\n"
    "v_mfma_f32_16x16x32_bf16 v[156:159], a[188:191], a[244:247], v[156:159]\n"
    "v_mfma_f32_16x16x32_bf16 v[156:159], a[12:15], a[240:243], v[156:159]\n"
    "s_waitcnt vmcnt(2)\n"
    "v_mfma_f32_16x16x32_bf16 v[160:163], a[0:3], v[40:43], v[160:163]\n"
    "v_mfma_f32_16x16x32_bf16 v[160:163], a[176:179], v[44:47], v[160:163]\n"
    "v_mfma_f32_16x16x32_bf16 v[160:163], v[192:195], v[48:51], v[160:163]\n"
    "v_mfma_f32_16x16x32_bf16 v[160:163], v[24:27], v[52:55], v[160:163]\n"
    "v_mfma_f32_16x16x32_bf16 v[164:167], v[28:31], v[52:55], v[164:167]\n"
    "v_mfma_f32_16x16x32_bf16 v[164:167], v[196:199], v[48:51], v[164:167]\n"
    "v_mfma_f32_16x16x32_bf16 v[164:167], a[180:183], v[44:47], v[164:167]\n"
    "v_mfma_f32_16x16x32_bf16 v[164:167], a[4:7], v[40:43], v[164:167]\n"
    "v_mfma_f32_16x16x32_bf16 v[168:171], a[8:11], v[40:43], v[168:171]\n"
    "v_mfma_f32_16x16x32_bf16 v[168:171], a[184:187], v[44:47], v[168:171]\n"
    "v_mfma_f32_16x16x32_bf16 v[168:171], v[200:203], v[48:51], v[168:171]\n"
    "v_mfma_f32_16x16x32_bf16 v[168:171], v[32:35], v[52:55], v[168:171]\n"
    "v_mfma_f32_16x16x32_bf16 v[172:175], v[36:39], v[52:55], v[172:175]\n"
    "v_mfma_f32_16x16x32_bf16 v[172:175], v[204:207], v[48:51], v[172:175]\n"
    "v_mfma_f32_16x16x32_bf16 v[172:175], a[188:191], v[44:47], v[172:175]\n"
    "v_mfma_f32_16x16x32_bf16 v[172:175], a[12:15], v[40:43], v[172:175]\n"
    "s_waitcnt vmcnt(0)\n"
    "v_mfma_f32_16x16x32_bf16 v[176:179], a[0:3], v[208:211], v[176:179]\n"
    "v_mfma_f32_16x16x32_bf16 v[176:179], v[192:195], v[212:215], v[176:179]\n"
    "v_mfma_f32_16x16x32_bf16 v[8:11], v[24:27], v[212:215], v[8:11]\n"
    "v_mfma_f32_16x16x32_bf16 v[8:11], a[176:179], v[208:211], v[8:11]\n"
    "v_mfma_f32_16x16x32_bf16 v[180:183], a[4:7], v[208:211], v[180:183]\n"
    "v_mfma_f32_16x16x32_bf16 v[180:183], v[196:199], v[212:215], v[180:183]\n"
    "v_mfma_f32_16x16x32_bf16 v[12:15], v[28:31], v[212:215], v[12:15]\n"
    "v_mfma_f32_16x16x32_bf16 v[12:15], a[180:183], v[208:211], v[12:15]\n"
    "v_mfma_f32_16x16x32_bf16 v[184:187], a[8:11], v[208:211], v[184:187]\n"
    "v_mfma_f32_16x16x32_bf16 v[184:187], v[200:203], v[212:215], v[184:187]\n"
    "v_mfma_f32_16x16x32_bf16 v[16:19], v[32:35], v[212:215], v[16:19]\n"
    "v_mfma_f32_16x16x32_bf16 v[16:19], a[184:187], v[208:211], v[16:19]\n"
    "v_mfma_f32_16x16x32_bf16 v[188:191], a[12:15], v[208:211], v[188:191]\n"
    "v_mfma_f32_16x16x32_bf16 v[188:191], v[204:207], v[212:215], v[188:191]\n"
    "v_mfma_f32_16x16x32_bf16 v[20:23], v[36:39], v[212:215], v[20:23]\n"
    "v_mfma_f32_16x16x32_bf16 v[20:23], a[188:191], v[208:211], v[20:23]\n"
    "s_cmp_eq_u32 s44, 3\n"
    "s_cbranch_scc0 L_nomask%=\n"
    "v_lshl_add_u32 v3, v4, 31, v3\n"
    "L_nomask%=:\n"
    "ds_write_b128 v1, v[128:131]\n"
    "ds_write_b128 v1, v[132:135] offset:1024\n"
    "ds_write_b128 v1, v[136:139] offset:2048\n"
    "ds_write_b128 v1, v[140:143] offset:3072\n"
    "ds_write_b128 v1, v[144:147] offset:16384\n"
    "ds_write_b128 v1, v[148:151] offset:17408\n"
    "ds_write_b128 v1, v[152:155] offset:18432\n"
    "ds_write_b128 v1, v[156:159] offset:19456\n"
    "ds_write_b128 v1, v[160:163] offset:32768\n"
    "ds_write_b128 v1, v[164:167] offset:33792\n"
    "ds_write_b128 v1, v[168:171] offset:34816\n"
    "ds_write_b128 v1, v[172:175] offset:35840\n"
    "v_add_f32_dpp v176, v8, v176 row_ror:8 row_mask:0xf bank_mask:0xf\n"
    "v_add_f32_dpp v177, v9, v177 row_ror:8 row_mask:0xf bank_mask:0xf\n"
    "v_add_f32_dpp v178, v10, v178 row_ror:8 row_mask:0xf bank_mask:0xf\n"
    "v_add_f32_dpp v179, v11, v179 row_ror:8 row_mask:0xf bank_mask:0xf\n"
    "v_add_f32_dpp v180, v12, v180 row_ror:8 row_mask:0xf bank_mask:0xf\n"
    "v_add_f32_dpp v181, v13, v181 row_ror:8 row_mask:0xf bank_mask:0xf\n"
    "v_add_f32_dpp v182, v14, v182 row_ror:8 row_mask:0xf bank_mask:0xf\n"
    "v_add_f32_dpp v183, v15, v183 row_ror:8 row_mask:0xf bank_mask:0xf\n"
    "v_add_f32_dpp v184, v16, v184 row_ror:8 row_mask:0xf bank_mask:0xf\n"
    "v_add_f32_dpp v185, v17, v185 row_ror:8 row_mask:0xf bank_mask:0xf\n"
    "v_add_f32_dpp v186, v18, v186 row_ror:8 row_mask:0xf bank_mask:0xf\n"
    "v_add_f32_dpp v187, v19, v187 row_ror:8 row_mask:0xf bank_mask:0xf\n"
    "v_add_f32_dpp v188, v20, v188 row_ror:8 row_mask:0xf bank_mask:0xf\n"
    "v_add_f32_dpp v189, v21, v189 row_ror:8 row_mask:0xf bank_mask:0xf\n"
    "v_add_f32_dpp v190, v22, v190 row_ror:8 row_mask:0xf bank_mask:0xf\n"
    "v_add_f32_dpp v191, v23, v191 row_ror:8 row_mask:0xf bank_mask:0xf\n"
    "ds_write_b128 v1, v[176:179] offset:49152\n"
    "ds_write_b128 v1, v[180:183] offset:50176\n"
    "ds_write_b128 v1, v[184:187] offset:51200\n"
    "ds_write_b128 v1, v[188:191] offset:52224\n"
    "s_waitcnt lgkmcnt(0)\n"
    "s_barrier\n"
    "ds_read_b128 v[192:195], v2\n"
    "ds_read_b128 v[208:211], v2 offset:4096\n"
    "ds_read_b128 v[224:227], v2 offset:8192\n"
    "ds_read_b128 v[240:243], v2 offset:12288\n"
    "ds_read_b128 v[196:199], v2 offset:1024\n"
    "ds_read_b128 v[212:215], v2 offset:5120\n"
    "ds_read_b128 v[228:231], v2 offset:9216\n"
    "ds_read_b128 v[244:247], v2 offset:13312\n"
    "ds_read_b128 v[200:203], v2 offset:2048\n"
    "ds_read_b128 v[216:219], v2 offset:6144\n"
    "ds_read_b128 v[232:235], v2 offset:10240\n"
    "ds_read_b128 v[248:251], v2 offset:14336\n"
    "ds_read_b128 v[204:207], v2 offset:3072\n"
    "ds_read_b128 v[220:223], v2 offset:7168\n"
    "ds_read_b128 v[236:239], v2 offset:11264\n"
    "ds_read_b128 v[252:255], v2 offset:15360\n"
    "v_cmp_ne_u32_e32 vcc, v126, v127\n"
    "s_nop 1\n"
    "s_and_b32 s22, vcc_lo, s22\n"
    "s_cbranch_scc0 L_noecho%=\n"
    "s_mov_b64 exec, s[22:23]\n"
    "buffer_store_dword v127, v120, s[56:59], 0 offen sc1\n"
    "s_mov_b64 exec, -1\n"
    "L_noecho%=:\n"
    "s_waitcnt lgkmcnt(12)\n"
    "v_pk_add_f32 v[192:193], v[192:193], v[208:209]\n"
    "v_pk_add_f32 v[194:195], v[194:195], v[210:211]\n"
    "v_pk_add_f32 v[224:225], v[224:225], v[240:241]\n"
    "v_pk_add_f32 v[226:227], v[226:227], v[242:243]\n"
    "v_pk_add_f32 v[192:193], v[192:193], v[224:225]\n"
    "v_pk_add_f32 v[194:195], v[194:195], v[226:227]\n"
    "v_cvt_pk_bf16_f32 v208, v192, v193\n"
    "v_cvt_pk_bf16_f32 v209, v194, v195\n"
    "buffer_store_dwordx2 v[208:209], v3, s[36:39], 0 offen\n"
    "s_waitcnt lgkmcnt(8)\n"
    "v_pk_add_f32 v[196:197], v[196:197], v[212:213]\n"
    "v_pk_add_f32 v[198:199], v[198:199], v[214:215]\n"
    "v_pk_add_f32 v[228:229], v[228:229], v[244:245]\n"
    "v_pk_add_f32 v[230:231], v[230:231], v[246:247]\n"
    "v_pk_add_f32 v[196:197], v[196:197], v[228:229]\n"
    "v_pk_add_f32 v[198:199], v[198:199], v[230:231]\n"
    "v_cvt_pk_bf16_f32 v212, v196, v197\n"
    "v_cvt_pk_bf16_f32 v213, v198, v199\n"
    "buffer_store_dwordx2 v[212:213], v3, s[36:39], 0 offen offset:32\n"
    "s_waitcnt lgkmcnt(4)\n"
    "v_pk_add_f32 v[200:201], v[200:201], v[216:217]\n"
    "v_pk_add_f32 v[202:203], v[202:203], v[218:219]\n"
    "v_pk_add_f32 v[232:233], v[232:233], v[248:249]\n"
    "v_pk_add_f32 v[234:235], v[234:235], v[250:251]\n"
    "v_pk_add_f32 v[200:201], v[200:201], v[232:233]\n"
    "v_pk_add_f32 v[202:203], v[202:203], v[234:235]\n"
    "v_cvt_pk_bf16_f32 v216, v200, v201\n"
    "v_cvt_pk_bf16_f32 v217, v202, v203\n"
    "buffer_store_dwordx2 v[216:217], v3, s[36:39], 0 offen offset:64\n"
    "s_waitcnt lgkmcnt(0)\n"
    "v_pk_add_f32 v[204:205], v[204:205], v[220:221]\n"
    "v_pk_add_f32 v[206:207], v[206:207], v[222:223]\n"
    "v_pk_add_f32 v[236:237], v[236:237], v[252:253]\n"
    "v_pk_add_f32 v[238:239], v[238:239], v[254:255]\n"
    "v_pk_add_f32 v[204:205], v[204:205], v[236:237]\n"
    "v_pk_add_f32 v[206:207], v[206:207], v[238:239]\n"
    "v_cvt_pk_bf16_f32 v220, v204, v205\n"
    "v_cvt_pk_bf16_f32 v221, v206, v207\n"
    "buffer_store_dwordx2 v[220:221], v3, s[36:39], 0 offen offset:96\n"
    "s_endpgm\n"
    "L_redo%=:\n"
    "s_waitcnt vmcnt(0) lgkmcnt(0)\n"
    "s_barrier\n"
    "v_xor_b32_e32 v108, 0x10000, v108\n"
    "v_xor_b32_e32 v109, 0x10000, v109\n"
    "v_xor_b32_e32 v112, 0x10000, v112\n"
    "v_xor_b32_e32 v113, 0x10000, v113\n"
    "v_xor_b32_e32 v114, 0x10000, v114\n"
    "v_xor_b32_e32 v115, 0x10000, v115\n"
    "v_mov_b32_e32 v64, 0xff800000\n"
    "v_mov_b32_e32 v65, 0xff800000\n"
    "v_mov_b32_e32 v66, 0xff800000\n"
    "v_mov_b32_e32 v67, 0xff800000\n"
    "s_mov_b32 s42, 0\n"
    "s_mov_b32 m0, s55\n"
    "s_nop 0\n"
    "L_mx%=:\n"
    "buffer_load_dwordx4 v116, s[24:27], s42 offen nt lds\n"
    "buffer_load_dwordx4 v117, s[24:27], s42 offen offset:1024 nt lds\n"
    "buffer_load_dwordx4 v118, s[24:27], s42 offen offset:2048 nt lds\n"
    "buffer_load_dwordx4 v119, s[24:27], s42 offen offset:3072 nt lds\n"
    "s_waitcnt vmcnt(0)\n"
    "s_barrier\n"
    "ds_read_b128 a[64:67], v108\n"
    "ds_read_b128 a[68:71], v108 offset:256\n"
    "ds_read_b128 a[72:75], v108 offset:512\n"
    "ds_read_b128 a[76:79], v108 offset:768\n"
    "ds_read_b128 a[80:83], v108 offset:4096\n"
    "ds_read_b128 a[84:87], v108 offset:4352\n"
    "ds_read_b128 a[88:91], v108 offset:4608\n"
    "ds_read_b128 a[92:95], v108 offset:4864\n"
    "s_waitcnt lgkmcnt(0)\n"
    "v_mfma_f32_16x16x32_bf16 v[8:11], a[64:67], a[0:3], 0\n"
    "v_mfma_f32_16x16x32_bf16 v[8:11], a[68:71], a[4:7], v[8:11]\n"
    "v_mfma_f32_16x16x32_bf16 v[8:11], a[72:75], a[8:11], v[8:11]\n"
    "v_mfma_f32_16x16x32_bf16 v[8:11], a[76:79], a[12:15], v[8:11]\n"
    "v_mfma_f32_16x16x32_bf16 v[12:15], a[76:79], a[28:31], 0\n"
    "v_mfma_f32_16x16x32_bf16 v[12:15], a[72:75], a[24:27], v[12:15]\n"
    "v_mfma_f32_16x16x32_bf16 v[12:15], a[68:71], a[20:23], v[12:15]\n"
    "v_mfma_f32_16x16x32_bf16 v[12:15], a[64:67], a[16:19], v[12:15]\n"
    "v_mfma_f32_16x16x32_bf16 v[16:19], a[64:67], a[32:35], 0\n"
    "v_mfma_f32_16x16x32_bf16 v[16:19], a[68:71], a[36:39], v[16:19]\n"
    "v_mfma_f32_16x16x32_bf16 v[16:19], a[72:75], a[40:43], v[16:19]\n"
    "v_mfma_f32_16x16x32_bf16 v[16:19], a[76:79], a[44:47], v[16:19]\n"
    "v_mfma_f32_16x16x32_bf16 v[20:23], a[76:79], a[60:63], 0\n"
    "v_mfma_f32_16x16x32_bf16 v[20:23], a[72:75], a[56:59], v[20:23]\n"
    "v_mfma_f32_16x16x32_bf16 v[20:23], a[68:71], a[52:55], v[20:23]\n"
    "v_mfma_f32_16x16x32_bf16 v[20:23], a[64:67], a[48:51], v[20:23]\n"
    "v_mfma_f32_16x16x32_bf16 v[24:27], a[80:83], a[0:3], 0\n"
    "v_mfma_f32_16x16x32_bf16 v[24:27], a[84:87], a[4:7], v[24:27]\n"
    "v_mfma_f32_16x16x32_bf16 v[24:27], a[88:91], a[8:11], v[24:27]\n"
    "v_mfma_f32_16x16x32_bf16 v[24:27], a[92:95], a[12:15], v[24:27]\n"
    "v_mfma_f32_16x16x32_bf16 v[28:31], a[92:95], a[28:31], 0\n"
    "v_mfma_f32_16x16x32_bf16 v[28:31], a[88:91], a[24:27], v[28:31]\n"
    "v_mfma_f32_16x16x32_bf16 v[28:31], a[84:87], a[20:23], v[28:31]\n"
    "v_mfma_f32_16x16x32_bf16 v[28:31], a[80:83], a[16:19], v[28:31]\n"
    "v_mfma_f32_16x16x32_bf16 v[32:35], a[80:83], a[32:35], 0\n"
    "v_mfma_f32_16x16x32_bf16 v[32:35], a[84:87], a[36:39], v[32:35]\n"
    "v_mfma_f32_16x16x32_bf16 v[32:35], a[88:91], a[40:43], v[32:35]\n"
    "v_mfma_f32_16x16x32_bf16 v[32:35], a[92:95], a[44:47], v[32:35]\n"
    "v_mfma_f32_16x16x32_bf16 v[36:39], a[92:95], a[60:63], 0\n"
    "v_mfma_f32_16x16x32_bf16 v[36:39], a[88:91], a[56:59], v[36:39]\n"
    "v_mfma_f32_16x16x32_bf16 v[36:39], a[84:87], a[52:55], v[36:39]\n"
    "v_mfma_f32_16x16x32_bf16 v[36:39], a[80:83], a[48:51], v[36:39]\n"
    "ds_read_b128 a[64:67], v109\n"
    "ds_read_b128 a[68:71], v109 offset:256\n"
    "ds_read_b128 a[72:75], v109 offset:512\n"
    "ds_read_b128 a[76:79], v109 offset:768\n"
    "ds_read_b128 a[80:83], v109 offset:4096\n"
    "ds_read_b128 a[84:87], v109 offset:4352\n"
    "ds_read_b128 a[88:91], v109 offset:4608\n"
    "ds_read_b128 a[92:95], v109 offset:4864\n"
    "s_waitcnt lgkmcnt(0)\n"
    "s_barrier\n"
    "v_mfma_f32_16x16x32_bf16 v[40:43], a[64:67], a[0:3], 0\n"
    "v_mfma_f32_16x16x32_bf16 v[40:43], a[68:71], a[4:7], v[40:43]\n"
    "v_mfma_f32_16x16x32_bf16 v[40:43], a[72:75], a[8:11], v[40:43]\n"
    "v_mfma_f32_16x16x32_bf16 v[40:43], a[76:79], a[12:15], v[40:43]\n"
    "v_mfma_f32_16x16x32_bf16 v[44:47], a[76:79], a[28:31], 0\n"
    "v_mfma_f32_16x16x32_bf16 v[44:47], a[72:75], a[24:27], v[44:47]\n"
    "v_mfma_f32_16x16x32_bf16 v[44:47], a[68:71], a[20:23], v[44:47]\n"
    "v_mfma_f32_16x16x32_bf16 v[44:47], a[64:67], a[16:19], v[44:47]\n"
    "v_mfma_f32_16x16x32_bf16 v[48:51], a[64:67], a[32:35], 0\n"
    "v_mfma_f32_16x16x32_bf16 v[48:51], a[68:71], a[36:39], v[48:51]\n"
    "v_mfma_f32_16x16x32_bf16 v[48:51], a[72:75], a[40:43], v[48:51]\n"
    "v_mfma_f32_16x16x32_bf16 v[48:51], a[76:79], a[44:47], v[48:51]\n"
    "v_mfma_f32_16x16x32_bf16 v[52:55], a[80:83], a[0:3], 0\n"
    "v_mfma_f32_16x16x32_bf16 v[52:55], a[84:87], a[4:7], v[52:55]\n"
    "v_mfma_f32_16x16x32_bf16 v[52:55], a[88:91], a[8:11], v[52:55]\n"
    "v_mfma_f32_16x16x32_bf16 v[52:55], a[92:95], a[12:15], v[52:55]\n"
    "v_mfma_f32_16x16x32_bf16 v[56:59], a[92:95], a[28:31], 0\n"
    "v_mfma_f32_16x16x32_bf16 v[56:59], a[88:91], a[24:27], v[56:59]\n"
    "v_mfma_f32_16x16x32_bf16 v[56:59], a[84:87], a[20:23], v[56:59]\n"
    "v_mfma_f32_16x16x32_bf16 v[56:59], a[80:83], a[16:19], v[56:59]\n"
    "v_mfma_f32_16x16x32_bf16 v[60:63], a[80:83], a[32:35], 0\n"
    "v_mfma_f32_16x16x32_bf16 v[60:63], a[84:87], a[36:39], v[60:63]\n"
    "v_mfma_f32_16x16x32_bf16 v[60:63], a[88:91], a[40:43], v[60:63]\n"
    "v_mfma_f32_16x16x32_bf16 v[60:63], a[92:95], a[44:47], v[60:63]\n"
    "s_nop 7\n"
    "v_max3_f32 v64, v64, v8, v9\n"
    "v_max3_f32 v64, v64, v10, v11\n"
    "v_max3_f32 v65, v65, v12, v13\n"
    "v_max3_f32 v65, v65, v14, v15\n"
    "v_max3_f32 v66, v66, v16, v17\n"
    "v_max3_f32 v66, v66, v18, v19\n"
    "v_max3_f32 v67, v67, v20, v21\n"
    "v_max3_f32 v67, v67, v22, v23\n"
    "v_max3_f32 v64, v64, v24, v25\n"
    "v_max3_f32 v64, v64, v26, v27\n"
    "v_max3_f32 v65, v65, v28, v29\n"
    "v_max3_f32 v65, v65, v30, v31\n"
    "v_max3_f32 v66, v66, v32, v33\n"
    "v_max3_f32 v66, v66, v34, v35\n"
    "v_max3_f32 v67, v67, v36, v37\n"
    "v_max3_f32 v67, v67, v38, v39\n"
    "v_max3_f32 v64, v64, v40, v41\n"
    "v_max3_f32 v64, v64, v42, v43\n"
    "v_max3_f32 v65, v65, v44, v45\n"
    "v_max3_f32 v65, v65, v46, v47\n"
    "v_max3_f32 v66, v66, v48, v49\n"
    "v_max3_f32 v66, v66, v50, v51\n"
    "v_max3_f32 v64, v64, v52, v53\n"
    "v_max3_f32 v64, v64, v54, v55\n"
    "v_max3_f32 v65, v65, v56, v57\n"
    "v_max3_f32 v65, v65, v58, v59\n"
    "v_max3_f32 v66, v66, v60, v61\n"
    "v_max3_f32 v66, v66, v62, v63\n"
    "s_add_u32 s42, s42, 0x4000\n"
    "s_cmp_lg_u32 s42, 0x40000\n"
    "s_cbranch_scc1 L_mx%=\n"
    "v_mov_b32_e32 v68, v64\n"
    "v_mov_b32_e32 v69, v65\n"
    "v_mov_b32_e32 v70, v66\n"
    "v_mov_b32_e32 v71, v67\n"
    "v_permlane32_swap_b32_e32 v64, v68\n"
    "v_permlane32_swap_b32_e32 v65, v69\n"
    "v_permlane32_swap_b32_e32 v66, v70\n"
    "v_permlane32_swap_b32_e32 v67, v71\n"
    "v_max_f32_e32 v64, v64, v68\n"
    "v_max_f32_e32 v65, v65, v69\n"
    "v_max_f32_e32 v66, v66, v70\n"
    "v_max_f32_e32 v67, v67, v71\n"
    "v_mov_b32_e32 v68, v64\n"
    "v_mov_b32_e32 v69, v65\n"
    "v_mov_b32_e32 v70, v66\n"
    "v_mov_b32_e32 v71, v67\n"
    "v_permlane16_swap_b32_e32 v64, v68\n"
    "v_permlane16_swap_b32_e32 v65, v69\n"
    "v_permlane16_swap_b32_e32 v66, v70\n"
    "v_permlane16_swap_b32_e32 v67, v71\n"
    "v_max_f32_e32 v64, v64, v68\n"
    "v_max_f32_e32 v65, v65, v69\n"
    "v_max_f32_e32 v66, v66, v70\n"
    "v_max_f32_e32 v67, v67, v71\n"
    "v_xor_b32_e32 v92, 0x80000000, v64\n"
    "v_xor_b32_e32 v96, 0x80000000, v65\n"
    "v_xor_b32_e32 v100, 0x80000000, v66\n"
    "v_xor_b32_e32 v104, 0x80000000, v67\n"
    "s_mov_b32 s77, 1\n"
    "v_mov_b32_e32 v7, s76\n"
    "s_add_u32 s42, s78, 0\n"
    "s_and_b32 s42, s42, 0x3ffff\n"
    "s_add_u32 m0, s55, 0\n"
    "s_nop 0\n"
    "buffer_load_dwordx4 v116, s[24:27], s42 offen nt lds\n"
    "buffer_load_dwordx4 v117, s[24:27], s42 offen offset:1024 nt lds\n"
    "buffer_load_dwordx4 v118, s[24:27], s42 offen offset:2048 nt lds\n"
    "buffer_load_dwordx4 v119, s[24:27], s42 offen offset:3072 nt lds\n"
    "s_add_u32 s42, s78, 0\n"
    "s_and_b32 s42, s42, 0x3ffff\n"
    "s_add_u32 m0, s55, 0x4000\n"
    "s_nop 0\n"
    "buffer_load_dwordx4 v116, s[28:31], s42 offen nt lds\n"
    "buffer_load_dwordx4 v118, s[28:31], s42 offen offset:1024 nt lds\n"
    "buffer_load_dwordx4 v116, s[28:31], s42 offen offset:2048 nt lds\n"
    "buffer_load_dwordx4 v118, s[28:31], s42 offen offset:3072 nt lds\n"
    "s_add_u32 s42, s78, 0x4000\n"
    "s_and_b32 s42, s42, 0x3ffff\n"
    "s_add_u32 m0, s55, 0x8000\n"
    "s_nop 0\n"
    "buffer_load_dwordx4 v116, s[24:27], s42 offen nt lds\n"
    "buffer_load_dwordx4 v117, s[24:27], s42 offen offset:1024 nt lds\n"
    "buffer_load_dwordx4 v118, s[24:27], s42 offen offset:2048 nt lds\n"
    "buffer_load_dwordx4 v119, s[24:27], s42 offen offset:3072 nt lds\n"
    "s_add_u32 s42, s78, 0x4000\n"
    "s_and_b32 s42, s42, 0x3ffff\n"
    "s_add_u32 m0, s55, 0xc000\n"
    "s_nop 0\n"
    "buffer_load_dwordx4 v116, s[28:31], s42 offen nt lds\n"
    "buffer_load_dwordx4 v118, s[28:31], s42 offen offset:1024 nt lds\n"
    "buffer_load_dwordx4 v116, s[28:31], s42 offen offset:2048 nt lds\n"
    "buffer_load_dwordx4 v118, s[28:31], s42 offen offset:3072 nt lds\n"
    "s_add_u32 s42, s78, 0x8000\n"
    "s_and_b32 s42, s42, 0x3ffff\n"
    "s_add_u32 m0, s55, 0x18000\n"
    "s_nop 0\n"
    "buffer_load_dwordx4 v116, s[24:27], s42 offen nt lds\n"
    "buffer_load_dwordx4 v117, s[24:27], s42 offen offset:1024 nt lds\n"
    "buffer_load_dwordx4 v118, s[24:27], s42 offen offset:2048 nt lds\n"
    "buffer_load_dwordx4 v119, s[24:27], s42 offen offset:3072 nt lds\n"
    "s_add_u32 s42, s78, 0x8000\n"
    "s_and_b32 s42, s42, 0x3ffff\n"
    "s_add_u32 m0, s55, 0x1c000\n"
    "s_nop 0\n"
    "buffer_load_dwordx4 v116, s[28:31], s42 offen nt lds\n"
    "buffer_load_dwordx4 v118, s[28:31], s42 offen offset:1024 nt lds\n"
    "buffer_load_dwordx4 v116, s[28:31], s42 offen offset:2048 nt lds\n"
    "buffer_load_dwordx4 v118, s[28:31], s42 offen offset:3072 nt lds\n"
    "s_waitcnt vmcnt(0)\n"
    "s_barrier\n"
    "ds_read_b128 a[64:67], v108\n"
    "ds_read_b128 a[68:71], v108 offset:256\n"
    "ds_read_b128 a[72:75], v108 offset:512\n"
    "ds_read_b128 a[76:79], v108 offset:768\n"
    "ds_read_b128 a[80:83], v108 offset:4096\n"
    "ds_read_b128 a[84:87], v108 offset:4352\n"
    "ds_read_b128 a[88:91], v108 offset:4608\n"
    "ds_read_b128 a[92:95], v108 offset:4864\n"
    "s_waitcnt lgkmcnt(0)\n"
    "s_branch L_tile0%=\n"
    "L_end%=:\n"
    :: "s"(__builtin_amdgcn_kernarg_segment_ptr()), "s"(blockIdx.x), "v"(threadIdx.x)
    : "v1", "v2", "v3", "v4", "v5", "v6", "v7", "v8", "v9", "v10", "v11", "v12", "v13", "v14", "v15", "v16", "v17", "v18", "v19", "v20", "v21", "v22", "v23", "v24", "v25", "v26", "v27", "v28", "v29", "v30", "v31", "v32", "v33", "v34", "v35", "v36", "v37", "v38", "v39", "v40", "v41", "v42", "v43", "v44", "v45", "v46", "v47", "v48", "v49", "v50", "v51", "v52", "v53", "v54", "v55", "v56", "v57", "v58", "v59", "v60", "v61", "v62", "v63", "v64", "v65", "v66", "v67", "v68", "v69", "v70", "v71", "v72", "v73", "v74", "v75", "v76", "v77", "v78", "v79", "v80", "v81", "v82", "v83", "v84", "v85", "v86", "v87", "v88", "v89", "v90", "v91", "v92", "v93", "v94", "v95", "v96", "v97", "v98", "v99", "v100", "v101", "v102", "v103", "v104", "v105", "v106", "v107", "v108", "v109", "v110", "v111", "v112", "v113", "v114", "v115", "v116", "v117", "v118", "v119", "v120", "v121", "v122", "v123", "v124", "v125", "v126", "v127", "v128", "v129", "v130", "v131", "v132", "v133", "v134", "v135", "v136", "v137", "v138", "v139", "v140", "v141", "v142", "v143", "v144", "v145", "v146", "v147", "v148", "v149", "v150", "v151", "v152", "v153", "v154", "v155", "v156", "v157", "v158", "v159", "v160", "v161", "v162", "v163", "v164", "v165", "v166", "v167", "v168", "v169", "v170", "v171", "v172", "v173", "v174", "v175", "v176", "v177", "v178", "v179", "v180", "v181", "v182", "v183", "v184", "v185", "v186", "v187", "v188", "v189", "v190", "v191", "v192", "v193", "v194", "v195", "v196", "v197", "v198", "v199", "v200", "v201", "v202", "v203", "v204", "v205", "v206", "v207", "v208", "v209", "v210", "v211", "v212", "v213", "v214", "v215", "v216", "v217", "v218", "v219", "v220", "v221", "v222", "v223", "v224", "v225", "v226", "v227", "v228", "v229", "v230", "v231", "v232", "v233", "v234", "v235", "v236", "v237", "v238", "v239", "v240", "v241", "v242", "v243", "v244", "v245", "v246", "v247", "v248", "v249", "v250", "v251", "v252", "v253", "v254", "v255", "a0", "a1", "a2", "a3", "a4", "a5", "a6", "a7", "a8", "a9", "a10", "a11", "a12", "a13", "a14", "a15", "a16", "a17", "a18", "a19", "a20", "a21", "a22", "a23", "a24", "a25", "a26", "a27", "a28", "a29", "a30", "a31", "a32", "a33", "a34", "a35", "a36", "a37", "a38", "a39", "a40", "a41", "a42", "a43", "a44", "a45", "a46", "a47", "a48", "a49", "a50", "a51", "a52", "a53", "a54", "a55", "a56", "a57", "a58", "a59", "a60", "a61", "a62", "a63", "a64", "a65", "a66", "a67", "a68", "a69", "a70", "a71", "a72", "a73", "a74", "a75", "a76", "a77", "a78", "a79", "a80", "a81", "a82", "a83", "a84", "a85", "a86", "a87", "a88", "a89", "a90", "a91", "a92", "a93", "a94", "a95", "a96", "a97", "a98", "a99", "a100", "a101", "a102", "a103", "a104", "a105", "a106", "a107", "a108", "a109", "a110", "a111", "a112", "a113", "a114", "a115", "a116", "a117", "a118", "a119", "a120", "a121", "a122", "a123", "a124", "a125", "a126", "a127", "a128", "a129", "a130", "a131", "a132", "a133", "a134", "a135", "a136", "a137", "a138", "a139", "a140", "a141", "a142", "a143", "a144", "a145", "a146", "a147", "a148", "a149", "a150", "a151", "a152", "a153", "a154", "a155", "a156", "a157", "a158", "a159", "a160", "a161", "a162", "a163", "a164", "a165", "a166", "a167", "a168", "a169", "a170", "a171", "a172", "a173", "a174", "a175", "a176", "a177", "a178", "a179", "a180", "a181", "a182", "a183", "a184", "a185", "a186", "a187", "a188", "a189", "a190", "a191", "a192", "a193", "a194", "a195", "a196", "a197", "a198", "a199", "a200", "a201", "a202", "a203", "a204", "a205", "a206", "a207", "a208", "a209", "a210", "a211", "a212", "a213", "a214", "a215", "a216", "a217", "a218", "a219", "a220", "a221", "a222", "a223", "a224", "a225", "a226", "a227", "a228", "a229", "a230", "a231", "a232", "a233", "a234", "a235", "a236", "a237", "a238", "a239", "a240", "a241", "a242", "a243", "a244", "a245", "a246", "a247", "a248", "a249", "a250", "a251", "a252", "a253", "a254", "a255", "s8", "s9", "s10", "s11", "s12", "s13", "s14", "s15", "s16", "s17", "s18", "s19", "s20", "s21", "s22", "s23", "s24", "s25", "s26", "s27", "s28", "s29", "s30", "s31", "s32", "s33", "s34", "s35", "s36", "s37", "s38", "s39", "s40", "s41", "s42", "s43", "s44", "s45", "s46", "s47", "s48", "s49", "s50", "s51", "s52", "s53", "s54", "s55", "s56", "s57", "s58", "s59", "s60", "s61", "s62", "s63", "s64", "s65", "s66", "s67", "s68", "s69", "s70", "s71", "s72", "s73", "s74", "s75", "s76", "s77", "s78", "s79", "s80", "s81", "s82", "s83", "s84", "s85", "s86", "s87", "s88", "s89", "s90", "s91", "s92", "s93", "s94", "s95", "s96", "s97", "s98", "s99", "vcc", "scc", "memory");
}

extern "C" void kernel_launch(void* const* d_in, const int* in_sizes, int n_in, void* d_out, int out_size, void* d_ws, size_t ws_size, hipStream_t stream) {
  (void)hipFuncSetAttribute((const void*)gqa_block_fused, hipFuncAttributeMaxDynamicSharedMemorySize, 163840);
  gqa_block_fused<<<256, 256, 163840, stream>>>(d_in[0], d_in[1], d_in[2], d_in[3], d_in[4], d_out, d_ws);
}
```
